```python
import torch

def _softmax(x, axis=-1):
    return torch.softmax(x, dim=axis)
def _gelu(x):
    return torch.nn.functional.gelu(x, approximate="tanh")
def _silu(x):
    return torch.nn.functional.silu(x)
def _sigmoid(x):
    return torch.sigmoid(x)
def _layernorm(x, axis=-1, eps=1e-5):
    m = x.mean(dim=axis, keepdim=True)
    v = x.var(dim=axis, keepdim=True, unbiased=False)
    return (x - m) / torch.sqrt(v + eps)
def _scatter_add(tgt, idx, src, axis=0):
    return tgt.clone().index_add_(axis, idx, src)
def _segment_sum(src, seg, n):
    out = torch.zeros((n,) + src.shape[1:], dtype=src.dtype)
    return out.index_add_(0, seg, src)
def _pad(x, pw):
    flat = tuple(v for pair in reversed(pw) for v in pair)
    return torch.nn.functional.pad(x, flat)

kernel_name = 'concat_heads__mha_block__compressed_kv_cla__prompt'

def setup_inputs():

    _w = (lambda a: (lambda t: t.to(torch.bfloat16) if t.is_floating_point() else t)(torch.as_tensor(a)))
    def _A_setup():
        B, S, D, H = 1, 2048, 640, 32
        return {
            "x": _w((lambda *s: torch.randn(*s, dtype=torch.bfloat16) * 0.2)(B, S, D)),
            "wqkv": _w((lambda *s: torch.randn(*s, dtype=torch.bfloat16) * 0.2)(D, 3 * D)),
            "wo": _w((lambda *s: torch.randn(*s, dtype=torch.bfloat16) * 0.05)(D, D)),
        }
    _A_in = _A_setup()

    def _B_setup():
        B, H, Sq, Sk, D, M = 1, 32, 2048, 2048, 128, 640
        Dc = 4 * D
        return {
            "x": _w((lambda *s: torch.randn(*s, dtype=torch.bfloat16) * 0.4)(B, Sq, M)),
            "c_kv": _w((lambda *s: torch.randn(*s, dtype=torch.bfloat16) * 0.4)(B, Sk, Dc)),
            "w_q": _w((lambda *s: torch.randn(*s, dtype=torch.bfloat16) * 3.2)(M, H, D) * (M ** -0.5)),
            "w_uk": _w((lambda *s: torch.randn(*s, dtype=torch.bfloat16) * 3.2)(Dc, H, D) * (Dc ** -0.5)),
            "w_uv": _w((lambda *s: torch.randn(*s, dtype=torch.bfloat16) * 0.4)(Dc, H, D) * (Dc ** -0.5)),
            "w_o": _w((lambda *s: torch.randn(*s, dtype=torch.bfloat16) * 0.4)(H, D, M) * ((H * D) ** -0.5)),
        }
    _B_in = _B_setup()

    return {
        "A_x": _A_in["x"],
        "A_wqkv": _A_in["wqkv"],
        "A_wo": _A_in["wo"],
        "B_x": _B_in["x"],
        "B_c_kv": _B_in["c_kv"],
        "B_w_q": _B_in["w_q"],
        "B_w_uk": _B_in["w_uk"],
        "B_w_uv": _B_in["w_uv"],
        "B_w_o": _B_in["w_o"],
    }

def reference(A_x, A_wqkv, A_wo, B_x, B_c_kv, B_w_q, B_w_uk, B_w_uv, B_w_o):
    def _A_ref(x, wqkv, wo):
        B, S, D = x.shape
        H = 32
        Dh = D // H
        qkv = (lambda x, *d: x.permute(*d))((x @ wqkv).reshape(B, S, 3, H, Dh), 2, 0, 3, 1, 4)
        q, k, v = qkv[0], qkv[1], qkv[2]
        scores = torch.einsum("bhqd,bhkd->bhqk", q, k) * (Dh ** -0.5)
        p = _softmax(scores, axis=-1)
        o = torch.einsum("bhqk,bhkd->bhqd", p, v)
        return (lambda x, *d: x.permute(*d))(o, 0, 2, 1, 3).reshape(B, S, D) @ wo

    def _B_ref(x, c_kv, w_q, w_uk, w_uv, w_o):
        B, Sq, M = x.shape[0], x.shape[1], x.shape[2]
        Sk = c_kv.shape[1]
        H, D = w_q.shape[1], w_q.shape[2]
        q = torch.einsum("bqm,mhd->bhqd", x, w_q)
        k = torch.einsum("bkc,chd->bhkd", c_kv, w_uk)
        v = torch.einsum("bkc,chd->bhkd", c_kv, w_uv)
        scores = torch.einsum("bhqd,bhkd->bhqk", q, k) * (D ** -0.5)
        off = Sk - Sq
        qi = torch.arange(Sq)[:, None]
        kj = torch.arange(Sk)[None, :]
        cmask = kj <= (qi + off)
        scores = torch.where(cmask[None, None], scores, -1e9)
        p = _softmax(scores, axis=-1)
        ctx = torch.einsum("bhqk,bhkd->bhqd", p, v)
        return torch.einsum("bhqd,hdm->bqm", ctx, w_o)

    a = _A_ref(A_x, A_wqkv, A_wo)
    b = _B_ref(B_x, B_c_kv, B_w_q, B_w_uk, B_w_uv, B_w_o)
    return torch.concatenate([a, b], axis=-1)
```

```cpp
#include <hip/hip_runtime.h>
#include <hip/hip_bf16.h>
#include <stdint.h>

constexpr int S_ = 2048;
constexpr int DM = 640;
constexpr int HA = 32, DHA = 20;
constexpr int HB = 32, DHB = 128, DC = 512;
constexpr int NB = HB * DHB;

typedef unsigned short u16;
typedef short bf16x8 __attribute__((ext_vector_type(8)));
typedef short s16x4 __attribute__((ext_vector_type(4)));
typedef float f32x16 __attribute__((ext_vector_type(16)));
typedef float f32x4 __attribute__((ext_vector_type(4)));
typedef float f32x2 __attribute__((ext_vector_type(2)));
typedef __bf16 bf16x2 __attribute__((ext_vector_type(2)));
typedef unsigned u32x2 __attribute__((ext_vector_type(2)));
typedef unsigned u32x4 __attribute__((ext_vector_type(4)));
typedef __attribute__((address_space(3))) s16x4 lds_s16x4;
typedef __attribute__((address_space(3))) bf16x8 lds_bf16x8;

#define DI __device__ __forceinline__
#define MFMA32(a, b, c) __builtin_amdgcn_mfma_f32_32x32x16_bf16((a), (b), (c), 0, 0, 0)

DI unsigned cvtpk(float lo, float hi) {
  f32x2 v = {lo, hi};
  bf16x2 b = __builtin_convertvector(v, bf16x2);
  return __builtin_bit_cast(unsigned, b);
}
DI void dma16(const void* sbase, unsigned voff, unsigned lds_addr) {
  unsigned keep;
  asm volatile("s_mov_b32 %0, m0\n\ts_mov_b32 m0, %3\n\ts_nop 0\n\tglobal_load_lds_dwordx4 %1, %2\n\ts_mov_b32 m0, %0"
               : "=&s"(keep) : "v"(voff), "s"(sbase), "s"(lds_addr) : "memory");
}
#define WAIT_VM0() asm volatile("s_waitcnt vmcnt(0)" ::: "memory")
#define BARRIER() do { asm volatile("s_waitcnt lgkmcnt(0)" ::: "memory"); __builtin_amdgcn_s_barrier(); asm volatile("" ::: "memory"); } while (0)

DI bf16x8 lds_read128(unsigned off) { return *(const lds_bf16x8*)(uintptr_t)off; }
DI s16x4 lds_read_tr(unsigned off) { return __builtin_amdgcn_ds_read_tr16_b64_v4i16((lds_s16x4*)(uintptr_t)off); }
DI bf16x8 cat8(s16x4 a, s16x4 b) { return __builtin_shufflevector(a, b, 0, 1, 2, 3, 4, 5, 6, 7); }
DI float xhalf_max(float x) {
  auto r = __builtin_amdgcn_permlane32_swap(__float_as_uint(x), __float_as_uint(x), false, false);
  return fmaxf(__uint_as_float(r[0]), __uint_as_float(r[1]));
}
DI float xhalf_sum(float x) {
  auto r = __builtin_amdgcn_permlane32_swap(__float_as_uint(x), __float_as_uint(x), false, false);
  return __uint_as_float(r[0]) + __uint_as_float(r[1]);
}

template <int BM, int BN, int WM, int WN>
struct GCfg {
  static constexpr int NW = WM * WN, TM = BM / WM, TN = BN / WN, MT = TM / 32, NT = TN / 32;
  static constexpr int XB = BM * 128, WROW = BN * 2, WB = 64 * WROW, STAGE = XB + WB;
  static constexpr int XI = BM / 8 / NW;
  static constexpr int WI = WB / 1024 / NW;
  static constexpr int WCH = BN / 8;
  static constexpr int WRPI = 64 / WCH;
  static constexpr int LDS = 2 * STAGE;
};

template <class C, class Epi>
DI void gemm_tile(const u16* __restrict__ X, int ldx, const u16* __restrict__ W, int ldw, int m0, int n0, int ncols,
                  int kbeg, int nk, const Epi& epi) {
  const int tid = threadIdx.x, lane = tid & 63, wid = __builtin_amdgcn_readfirstlane(tid >> 6);
  const int wm = wid / C::WN_, wn = wid % C::WN_;
  const int r = lane & 31, h = lane >> 5;
  unsigned xoff[C::XI], woff[C::WI];
#pragma unroll
  for (int i = 0; i < C::XI; ++i) {
    const int row = 8 * (wid * C::XI + i) + (lane >> 3);
    xoff[i] = (unsigned)(row * ldx + (((lane & 7) ^ ((row >> 1) & 7)) * 8)) * 2u;
  }
#pragma unroll
  for (int i = 0; i < C::WI; ++i) {
    const int k = C::WRPI * (wid * C::WI + i) + lane / C::WCH;
    int col = (((lane % C::WCH) ^ ((k & 3) << 2)) * 8);
    if (n0 + col > ncols - 8) col = ncols - 8 - n0;
    woff[i] = (unsigned)(k * ldw + col) * 2u;
  }
  const u16* xs = X + (size_t)m0 * ldx + kbeg;
  const u16* ws = W + (size_t)kbeg * ldw + n0;
  auto stage = [&](int buf, int kt) {
    const u16* xb = xs + kt * 64;
    const u16* wb = ws + (size_t)kt * 64 * ldw;
    const unsigned lb = buf * C::STAGE;
#pragma unroll
    for (int i = 0; i < C::XI; ++i) dma16(xb, xoff[i], lb + (wid * C::XI + i) * 1024);
#pragma unroll
    for (int i = 0; i < C::WI; ++i) dma16(wb, woff[i], lb + C::XB + (wid * C::WI + i) * 1024);
  };
  unsigned xa[4];
#pragma unroll
  for (int ks = 0; ks < 4; ++ks) xa[ks] = (wm * C::TM + r) * 128 + (((2 * ks + h) ^ ((r >> 1) & 7)) * 16);
  unsigned wa[C::NT];
  {
    const int q = (lane & 15) >> 2, p = lane & 3, cb = (lane >> 4) & 1;
#pragma unroll
    for (int nt = 0; nt < C::NT; ++nt) {
      const int nb = wn * C::NT + nt;
      wa[nt] = C::XB + (8 * h + q) * C::WROW + ((nb ^ q) * 64) + cb * 32 + p * 8;
    }
  }
  f32x16 acc[C::NT][C::MT];
#pragma unroll
  for (int nt = 0; nt < C::NT; ++nt)
#pragma unroll
    for (int mt = 0; mt < C::MT; ++mt) acc[nt][mt] = f32x16{};

  stage(0, 0);
  WAIT_VM0();
  BARRIER();
  for (int kt = 0; kt < nk; ++kt) {
    const unsigned lb = (kt & 1) * C::STAGE;
    if (kt + 1 < nk) stage((kt + 1) & 1, kt + 1);
#pragma unroll
    for (int ks = 0; ks < 4; ++ks) {
      bf16x8 xf[C::MT], wf[C::NT];
#pragma unroll
      for (int mt = 0; mt < C::MT; ++mt) xf[mt] = lds_read128(lb + xa[ks] + mt * 4096);
#pragma unroll
      for (int nt = 0; nt < C::NT; ++nt)
        wf[nt] = cat8(lds_read_tr(lb + wa[nt] + (16 * ks) * C::WROW), lds_read_tr(lb + wa[nt] + (16 * ks + 4) * C::WROW));
#pragma unroll
      for (int nt = 0; nt < C::NT; ++nt)
#pragma unroll
        for (int mt = 0; mt < C::MT; ++mt) acc[nt][mt] = MFMA32(wf[nt], xf[mt], acc[nt][mt]);
    }
    WAIT_VM0();
    BARRIER();
  }
#pragma unroll
  for (int nt = 0; nt < C::NT; ++nt)
#pragma unroll
    for (int mt = 0; mt < C::MT; ++mt)
#pragma unroll
      for (int g = 0; g < 4; ++g) {
        const int n = n0 + wn * C::TN + nt * 32 + 8 * g + 4 * h;
        const int m = m0 + wm * C::TM + mt * 32 + r;
        epi(m, n, acc[nt][mt][4 * g], acc[nt][mt][4 * g + 1], acc[nt][mt][4 * g + 2], acc[nt][mt][4 * g + 3]);
      }
}

template <int BM, int BN, int WM, int WN>
struct GC : GCfg<BM, BN, WM, WN> { static constexpr int WN_ = WN; };

struct EpiPlain {
  u16* out; int ld; float scale;
  DI void operator()(int m, int n, float a, float b, float c, float d) const {
    u32x2 v = {cvtpk(a * scale, b * scale), cvtpk(c * scale, d * scale)};
    *(u32x2*)(out + (size_t)m * ld + n) = v;
  }
};
struct EpiQKVA {
  u16* out; float qscale;
  DI void operator()(int m, int n, float a, float b, float c, float d) const {
    if (n >= 3 * DM) return;
    const int which = n / DM, rem = n - which * DM, head = rem / DHA, dd = rem - head * DHA;
    const float sc = which == 0 ? qscale : 1.f;
    u16* dst = out + ((size_t)((which * HA + head) * S_ + m)) * 32;
    u32x2 v = {cvtpk(a * sc, b * sc), cvtpk(c * sc, d * sc)};
    *(u32x2*)(dst + dd) = v;
    if (dd == 16) {
      u32x2 z0 = {which == 2 ? 0x3F80u : 0u, 0u};
      u32x4 z1 = {0u, 0u, 0u, 0u};
      *(u32x2*)(dst + 20) = z0;
      *(u32x4*)(dst + 24) = z1;
    }
  }
};
struct EpiF32 {
  float* out; int ld;
  DI void operator()(int m, int n, float a, float b, float c, float d) const {
    f32x4 v = {a, b, c, d};
    *(f32x4*)(out + (size_t)m * ld + n) = v;
  }
};

typedef GC<256, 256, 2, 4> G1;
typedef GC<128, 128, 2, 2> G2;

constexpr float LOG2E = 1.4426950408889634f;

__global__ __launch_bounds__(512, 2) void k_proj(const u16* __restrict__ Ax, const u16* __restrict__ Awqkv,
                                                 const u16* __restrict__ Bx, const u16* __restrict__ Bckv,
                                                 const u16* __restrict__ Bwq, const u16* __restrict__ Bwuk,
                                                 const u16* __restrict__ Bwuv, u16* __restrict__ qkvA,
                                                 u16* __restrict__ qB, u16* __restrict__ kB, u16* __restrict__ vB) {
  const int b = blockIdx.x, xcd = b & 7, idx = b >> 3;
  const int tn = xcd + 8 * (idx >> 3), tm = idx & 7;
  const int m0 = tm * 256;
  if (tn < 8) {
    EpiQKVA e{qkvA, 0.22360679774997896f * LOG2E};
    gemm_tile<G1>(Ax, DM, Awqkv, 3 * DM, m0, tn * 256, 3 * DM, 0, DM / 64, e);
  } else if (tn < 24) {
    EpiPlain e{qB, NB, 0.08838834764831845f * LOG2E};
    gemm_tile<G1>(Bx, DM, Bwq, NB, m0, (tn - 8) * 256, NB, 0, DM / 64, e);
  } else {
    const bool isv = tn >= 40;
    EpiPlain e{isv ? vB : kB, NB, 1.f};
    gemm_tile<G1>(Bckv, DC, isv ? Bwuv : Bwuk, NB, m0, (tn - (isv ? 40 : 24)) * 256, NB, 0, DC / 64, e);
  }
}

constexpr int SPLITK = 4;
__global__ __launch_bounds__(256, 2) void k_out(const u16* __restrict__ oA, const u16* __restrict__ Awo,
                                                const u16* __restrict__ ctx, const u16* __restrict__ Bwo,
                                                u16* __restrict__ out, float* __restrict__ part) {
  const int b = blockIdx.x;
  if (b < 80 * SPLITK) {
    const int sp = b & 3, t = b >> 2, tn = t % 5, tm = t / 5;
    EpiF32 e{part + (size_t)sp * S_ * DM, DM};
    gemm_tile<G2>(ctx, NB, Bwo, DM, tm * 128, tn * 128, DM, sp * (NB / SPLITK), NB / SPLITK / 64, e);
  } else {
    const int t = b - 80 * SPLITK, tn = t % 5, tm = t / 5;
    EpiPlain e{out, 2 * DM, 1.f};
    gemm_tile<G2>(oA, DM, Awo, DM, tm * 128, tn * 128, DM, 0, DM / 64, e);
  }
}

__global__ __launch_bounds__(256) void k_reduce(const float* __restrict__ part, u16* __restrict__ out) {
  const int i = blockIdx.x * 256 + threadIdx.x;
  const int m = i / (DM / 4), n = (i % (DM / 4)) * 4;
  f32x4 s = *(const f32x4*)(part + (size_t)m * DM + n);
#pragma unroll
  for (int sp = 1; sp < SPLITK; ++sp) s += *(const f32x4*)(part + (size_t)sp * S_ * DM + (size_t)m * DM + n);
  u32x2 v = {cvtpk(s[0], s[1]), cvtpk(s[2], s[3])};
  *(u32x2*)(out + (size_t)m * 2 * DM + DM + n) = v;
}

constexpr float THR = 8.f;
DI bf16x8 pack8(const f32x16& x, int s) {
  u32x4 w = {cvtpk(x[8 * s], x[8 * s + 1]), cvtpk(x[8 * s + 2], x[8 * s + 3]), cvtpk(x[8 * s + 4], x[8 * s + 5]),
             cvtpk(x[8 * s + 6], x[8 * s + 7])};
  return __builtin_bit_cast(bf16x8, w);
}
DI f32x16 splat16(float v) {
  f32x16 x;
#pragma unroll
  for (int i = 0; i < 16; ++i) x[i] = v;
  return x;
}
DI float max16(const f32x16& x) {
  float a = fmaxf(fmaxf(x[0], x[1]), x[2]);
#pragma unroll
  for (int i = 3; i < 15; i += 2) a = fmaxf(fmaxf(a, x[i]), x[i + 1]);
  return fmaxf(a, x[15]);
}

DI void attnB_block(const u16* __restrict__ Q, const u16* __restrict__ K, const u16* __restrict__ V,
                    u16* __restrict__ ctx, int head, int qb) {
  const int tid = threadIdx.x, lane = tid & 63, wid = __builtin_amdgcn_readfirstlane(tid >> 6);
  const int r = lane & 31, h = lane >> 5;
  const int q0 = qb * 128 + wid * 32;
  const int nkt = 2 * (qb + 1);
  constexpr unsigned KBUF = 0, VBUF = 32768, TB = 16384;
  unsigned koff[4], voff[4];
#pragma unroll
  for (int i = 0; i < 4; ++i) {
    const int row = 4 * (wid * 4 + i) + (lane >> 4), c = lane & 15;
    koff[i] = (unsigned)(row * NB + ((c ^ (row & 15)) * 8)) * 2u;
    voff[i] = (unsigned)(row * NB + ((c ^ ((row & 3) << 2)) * 8)) * 2u;
  }
  const u16* Kh = K + head * DHB;
  const u16* Vh = V + head * DHB;
  auto stage = [&](int buf, int t) {
    const u16* kb = Kh + (size_t)t * 64 * NB;
    const u16* vb = Vh + (size_t)t * 64 * NB;
#pragma unroll
    for (int i = 0; i < 4; ++i) dma16(kb, koff[i], KBUF + buf * TB + (wid * 4 + i) * 1024);
#pragma unroll
    for (int i = 0; i < 4; ++i) dma16(vb, voff[i], VBUF + buf * TB + (wid * 4 + i) * 1024);
  };
  stage(0, 0);
  bf16x8 qf[8];
  {
    const u16* qp = Q + (size_t)(q0 + r) * NB + head * DHB + h * 8;
#pragma unroll
    for (int ks = 0; ks < 8; ++ks) qf[ks] = *(const bf16x8*)(qp + ks * 16);
  }
  unsigned ka[8], va[4];
#pragma unroll
  for (int ks = 0; ks < 8; ++ks) ka[ks] = KBUF + r * 256 + (((2 * ks + h) ^ (r & 15)) * 16);
  {
    const int q = (lane & 15) >> 2, p = lane & 3, cb = (lane >> 4) & 1;
#pragma unroll
    for (int db = 0; db < 4; ++db) va[db] = VBUF + (4 * h + q) * 256 + ((db ^ q) * 64) + cb * 32 + p * 8;
  }
  f32x16 o[4];
#pragma unroll
  for (int db = 0; db < 4; ++db) o[db] = f32x16{};
  float mcur = 0.f, lsum = 0.f;
  f32x16 negm = f32x16{};
  const int qb32 = q0 >> 5;
  WAIT_VM0();
  BARRIER();

  for (int t = 0; t < nkt; ++t) {
    const unsigned bo = (t & 1) * TB;
    if (t + 1 < nkt) stage((t + 1) & 1, t + 1);
    const bool act0 = 2 * t <= qb32, act1 = 2 * t + 1 <= qb32;
    if (act0) {
      f32x16 s0 = negm, s1 = negm;
#pragma unroll
      for (int ks = 0; ks < 8; ++ks) s0 = MFMA32(lds_read128(bo + ka[ks]), qf[ks], s0);
      if (act1) {
#pragma unroll
        for (int ks = 0; ks < 8; ++ks) s1 = MFMA32(lds_read128(bo + ka[ks] + 8192), qf[ks], s1);
      }
      const float NEG = -__builtin_inff();
      if (2 * t == qb32) {
#pragma unroll
        for (int i = 0; i < 16; ++i) {
          const int kr = (i & 3) + 8 * (i >> 2) + 4 * h;
          if (kr > r) s0[i] = NEG;
        }
      }
      if (2 * t + 1 == qb32) {
#pragma unroll
        for (int i = 0; i < 16; ++i) {
          const int kr = (i & 3) + 8 * (i >> 2) + 4 * h;
          if (kr > r) s1[i] = NEG;
        }
      }
      if (!act1) s1 = splat16(NEG);
      float tm = xhalf_max(fmaxf(max16(s0), max16(s1)));
      if (t == 0 || __any(tm > THR)) {
        const float dpos = fmaxf(tm, 0.f);
        const float delta = t == 0 ? tm : dpos;
        const float f = __builtin_amdgcn_exp2f(-dpos);
        mcur += delta;
        negm = splat16(-mcur);
#pragma unroll
        for (int i = 0; i < 16; ++i) { s0[i] -= delta; s1[i] -= delta; }
#pragma unroll
        for (int db = 0; db < 4; ++db)
#pragma unroll
          for (int i = 0; i < 16; ++i) o[db][i] *= f;
        lsum *= f;
      }
#pragma unroll
      for (int i = 0; i < 16; ++i) { s0[i] = __builtin_amdgcn_exp2f(s0[i]); s1[i] = __builtin_amdgcn_exp2f(s1[i]); }
      float ps = 0.f;
#pragma unroll
      for (int i = 0; i < 16; ++i) ps += s0[i] + s1[i];
      lsum += ps;
#pragma unroll
      for (int s = 0; s < 2; ++s) {
        const bf16x8 pf = pack8(s0, s);
#pragma unroll
        for (int db = 0; db < 4; ++db) {
          const bf16x8 vf = cat8(lds_read_tr(bo + va[db] + (16 * s) * 256), lds_read_tr(bo + va[db] + (16 * s + 8) * 256));
          o[db] = MFMA32(vf, pf, o[db]);
        }
      }
      if (act1) {
#pragma unroll
        for (int s = 0; s < 2; ++s) {
          const bf16x8 pf = pack8(s1, s);
#pragma unroll
          for (int db = 0; db < 4; ++db) {
            const bf16x8 vf = cat8(lds_read_tr(bo + va[db] + (32 + 16 * s) * 256), lds_read_tr(bo + va[db] + (32 + 16 * s + 8) * 256));
            o[db] = MFMA32(vf, pf, o[db]);
          }
        }
      }
    }
    WAIT_VM0();
    BARRIER();
  }
  const float inv = 1.f / xhalf_sum(lsum);
  u16* op = ctx + (size_t)(q0 + r) * NB + head * DHB + 4 * h;
#pragma unroll
  for (int db = 0; db < 4; ++db)
#pragma unroll
    for (int g = 0; g < 4; ++g) {
      u32x2 v = {cvtpk(o[db][4 * g] * inv, o[db][4 * g + 1] * inv), cvtpk(o[db][4 * g + 2] * inv, o[db][4 * g + 3] * inv)};
      *(u32x2*)(op + 32 * db + 8 * g) = v;
    }
}

template <int QT>
DI void attnA_block(const u16* __restrict__ qkv, u16* __restrict__ oA, int head, int qb) {
  const int tid = threadIdx.x, lane = tid & 63, wid = __builtin_amdgcn_readfirstlane(tid >> 6);
  const int r = lane & 31, h = lane >> 5;
  const int q0 = qb * (128 * QT) + wid * (32 * QT);
  constexpr unsigned KBUF = 0, VBUF = 16384, TB = 8192;
  const u16* Qh = qkv + (size_t)(0 * HA + head) * S_ * 32;
  const u16* Kh = qkv + (size_t)(1 * HA + head) * S_ * 32;
  const u16* Vh = qkv + (size_t)(2 * HA + head) * S_ * 32;
  unsigned koff[2], voff[2];
#pragma unroll
  for (int i = 0; i < 2; ++i) {
    const int row = 16 * (wid * 2 + i) + (lane >> 2), c = lane & 3;
    koff[i] = (unsigned)(row * 64 + ((c ^ ((row >> 2) & 3)) * 16));
    voff[i] = (unsigned)(row * 64 + c * 16);
  }
  auto stage = [&](int buf, int t) {
    const u16* kb = Kh + (size_t)t * 128 * 32;
    const u16* vb = Vh + (size_t)t * 128 * 32;
#pragma unroll
    for (int i = 0; i < 2; ++i) dma16(kb, koff[i], KBUF + buf * TB + (wid * 2 + i) * 1024);
#pragma unroll
    for (int i = 0; i < 2; ++i) dma16(vb, voff[i], VBUF + buf * TB + (wid * 2 + i) * 1024);
  };
  stage(0, 0);
  bf16x8 qf[QT][2];
#pragma unroll
  for (int qt = 0; qt < QT; ++qt)
#pragma unroll
    for (int ks = 0; ks < 2; ++ks) qf[qt][ks] = *(const bf16x8*)(Qh + (size_t)(q0 + 32 * qt + r) * 32 + ks * 16 + h * 8);
  unsigned ka[2], va;
#pragma unroll
  for (int ks = 0; ks < 2; ++ks) ka[ks] = KBUF + r * 64 + (((2 * ks + h) ^ ((r >> 2) & 3)) * 16);
  {
    const int q = (lane & 15) >> 2, p = lane & 3, cb = (lane >> 4) & 1;
    va = VBUF + (4 * h + q) * 64 + cb * 32 + p * 8;
  }
  f32x16 o[QT], negm[QT];
  float mcur[QT];
#pragma unroll
  for (int qt = 0; qt < QT; ++qt) { o[qt] = f32x16{}; negm[qt] = f32x16{}; mcur[qt] = 0.f; }
  WAIT_VM0();
  BARRIER();
  constexpr int NKT = S_ / 128;
  for (int t = 0; t < NKT; ++t) {
    const unsigned bo = (t & 1) * TB;
    if (t + 1 < NKT) stage((t + 1) & 1, t + 1);
    f32x16 s[QT][4];
#pragma unroll
    for (int kb = 0; kb < 4; ++kb) {
      const bf16x8 k0 = lds_read128(bo + ka[0] + kb * 2048), k1 = lds_read128(bo + ka[1] + kb * 2048);
#pragma unroll
      for (int qt = 0; qt < QT; ++qt) {
        s[qt][kb] = MFMA32(k0, qf[qt][0], negm[qt]);
        s[qt][kb] = MFMA32(k1, qf[qt][1], s[qt][kb]);
      }
    }
    float tm[QT];
    bool need = t == 0;
#pragma unroll
    for (int qt = 0; qt < QT; ++qt) {
      tm[qt] = xhalf_max(fmaxf(fmaxf(max16(s[qt][0]), max16(s[qt][1])), fmaxf(max16(s[qt][2]), max16(s[qt][3]))));
      need = need || tm[qt] > THR;
    }
    if (__any(need)) {
#pragma unroll
      for (int qt = 0; qt < QT; ++qt) {
        const float dpos = fmaxf(tm[qt], 0.f);
        const float delta = t == 0 ? tm[qt] : dpos;
        const float f = __builtin_amdgcn_exp2f(-dpos);
        mcur[qt] += delta;
        negm[qt] = splat16(-mcur[qt]);
#pragma unroll
        for (int kb = 0; kb < 4; ++kb)
#pragma unroll
          for (int i = 0; i < 16; ++i) s[qt][kb][i] -= delta;
#pragma unroll
        for (int i = 0; i < 16; ++i) o[qt][i] *= f;
      }
    }
#pragma unroll
    for (int kb = 0; kb < 4; ++kb) {
#pragma unroll
      for (int sx = 0; sx < 2; ++sx) {
        const bf16x8 vf = cat8(lds_read_tr(bo + va + (32 * kb + 16 * sx) * 64), lds_read_tr(bo + va + (32 * kb + 16 * sx + 8) * 64));
#pragma unroll
        for (int qt = 0; qt < QT; ++qt) {
#pragma unroll
          for (int i = 0; i < 8; ++i) s[qt][kb][8 * sx + i] = __builtin_amdgcn_exp2f(s[qt][kb][8 * sx + i]);
          o[qt] = MFMA32(vf, pack8(s[qt][kb], sx), o[qt]);
        }
      }
    }
    WAIT_VM0();
    BARRIER();
  }
#pragma unroll
  for (int qt = 0; qt < QT; ++qt) {
    const float l = __shfl(o[qt][8], r + 32);
    const float inv = 1.f / l;
    u16* op = oA + (size_t)(q0 + 32 * qt + r) * DM + head * DHA + 4 * h;
#pragma unroll
    for (int g = 0; g < 3; ++g) {
      if (g < 2 || h == 0) {
        u32x2 v = {cvtpk(o[qt][4 * g] * inv, o[qt][4 * g + 1] * inv), cvtpk(o[qt][4 * g + 2] * inv, o[qt][4 * g + 3] * inv)};
        *(u32x2*)(op + 8 * g) = v;
      }
    }
  }
}

constexpr int AQT = 2;
constexpr int NBLK_B = HB * 8, NBLK_A = HA * (S_ / (128 * AQT));
__global__ __launch_bounds__(256, 2) void k_attn(const u16* __restrict__ qB, const u16* __restrict__ kB,
                                                 const u16* __restrict__ vB, u16* __restrict__ ctx,
                                                 const u16* __restrict__ qkvA, u16* __restrict__ oA) {
  const int b = blockIdx.x;
  if (b < NBLK_B) {
    const int head = (b & 7) + 8 * (b >> 6), j = (b >> 3) & 7;
    attnB_block(qB, kB, vB, ctx, head, 15 - j);
    attnB_block(qB, kB, vB, ctx, head, j);
  } else {
    const int a = b - NBLK_B;
    constexpr int PER = S_ / (128 * AQT);
    const int head = (a & 7) + 8 * (a / (8 * PER)), qb = (a >> 3) % PER;
    attnA_block<AQT>(qkvA, oA, head, qb);
  }
}

extern "C" void kernel_launch(void* const* d_in, const int* in_sizes, int n_in, void* d_out, int out_size, void* d_ws,
                              size_t ws_size, hipStream_t stream) {
  const u16* Ax = (const u16*)d_in[0];
  const u16* Awqkv = (const u16*)d_in[1];
  const u16* Awo = (const u16*)d_in[2];
  const u16* Bx = (const u16*)d_in[3];
  const u16* Bckv = (const u16*)d_in[4];
  const u16* Bwq = (const u16*)d_in[5];
  const u16* Bwuk = (const u16*)d_in[6];
  const u16* Bwuv = (const u16*)d_in[7];
  const u16* Bwo = (const u16*)d_in[8];
  u16* out = (u16*)d_out;
  char* ws = (char*)d_ws;
  u16* qkvA = (u16*)ws;                 ws += (size_t)3 * HA * S_ * 32 * 2;
  u16* qB = (u16*)ws;                   ws += (size_t)S_ * NB * 2;
  u16* kB = (u16*)ws;                   ws += (size_t)S_ * NB * 2;
  u16* vB = (u16*)ws;                   ws += (size_t)S_ * NB * 2;
  u16* ctx = (u16*)ws;                  ws += (size_t)S_ * NB * 2;
  u16* oA = (u16*)ws;                   ws += (size_t)S_ * DM * 2;
  float* part = (float*)ws;             ws += (size_t)SPLITK * S_ * DM * 4;

  (void)hipFuncSetAttribute((const void*)k_proj, hipFuncAttributeMaxDynamicSharedMemorySize, G1::LDS);
  (void)hipFuncSetAttribute((const void*)k_out, hipFuncAttributeMaxDynamicSharedMemorySize, G2::LDS);
  (void)hipFuncSetAttribute((const void*)k_attn, hipFuncAttributeMaxDynamicSharedMemorySize, 65536);
  k_proj<<<448, 512, G1::LDS, stream>>>(Ax, Awqkv, Bx, Bckv, Bwq, Bwuk, Bwuv, qkvA, qB, kB, vB);
  k_attn<<<NBLK_B + NBLK_A, 256, 65536, stream>>>(qB, kB, vB, ctx, qkvA, oA);
  k_out<<<80 * SPLITK + 80, 256, G2::LDS, stream>>>(oA, Awo, ctx, Bwo, out, part);
  k_reduce<<<S_ * DM / 4 / 256, 256, 0, stream>>>(part, out);
}
```

```cpp
#include <hip/hip_runtime.h>
#include <hip/hip_bf16.h>
#include <stdint.h>

constexpr int S_ = 2048;
constexpr int DM = 640;
constexpr int HA = 32, DHA = 20;
constexpr int HB = 32, DHB = 128, DC = 512;
constexpr int NB = HB * DHB;

typedef unsigned short u16;
typedef short bf16x8 __attribute__((ext_vector_type(8)));
typedef short s16x4 __attribute__((ext_vector_type(4)));
typedef float f32x16 __attribute__((ext_vector_type(16)));
typedef float f32x4 __attribute__((ext_vector_type(4)));
typedef float f32x2 __attribute__((ext_vector_type(2)));
typedef __bf16 bf16x2 __attribute__((ext_vector_type(2)));
typedef unsigned u32x2 __attribute__((ext_vector_type(2)));
typedef unsigned u32x4 __attribute__((ext_vector_type(4)));
typedef __attribute__((address_space(3))) s16x4 lds_s16x4;
typedef __attribute__((address_space(3))) bf16x8 lds_bf16x8;
typedef __attribute__((address_space(3))) u32x2 lds_u32x2;

#define DI __device__ __forceinline__
#define MFMA32(a, b, c) __builtin_amdgcn_mfma_f32_32x32x16_bf16((a), (b), (c), 0, 0, 0)

DI unsigned cvtpk(float lo, float hi) {
  f32x2 v = {lo, hi};
  bf16x2 b = __builtin_convertvector(v, bf16x2);
  return __builtin_bit_cast(unsigned, b);
}
DI void dma16(const void* sbase, unsigned voff, unsigned lds_addr) {
  unsigned keep;
  asm volatile("s_mov_b32 %0, m0\n\ts_mov_b32 m0, %3\n\ts_nop 0\n\tglobal_load_lds_dwordx4 %1, %2\n\ts_mov_b32 m0, %0"
               : "=&s"(keep) : "v"(voff), "s"(sbase), "s"(lds_addr) : "memory");
}
#define WAIT_VM0() asm volatile("s_waitcnt vmcnt(0)" ::: "memory")
#define BARRIER() do { asm volatile("s_waitcnt lgkmcnt(0)" ::: "memory"); __builtin_amdgcn_s_barrier(); asm volatile("" ::: "memory"); } while (0)

DI bf16x8 lds_read128(unsigned off) { return *(const lds_bf16x8*)(uintptr_t)off; }
DI s16x4 lds_read_tr(unsigned off) { return __builtin_amdgcn_ds_read_tr16_b64_v4i16((lds_s16x4*)(uintptr_t)off); }
DI bf16x8 cat8(s16x4 a, s16x4 b) { return __builtin_shufflevector(a, b, 0, 1, 2, 3, 4, 5, 6, 7); }
DI float xhalf_max(float x) {
  auto r = __builtin_amdgcn_permlane32_swap(__float_as_uint(x), __float_as_uint(x), false, false);
  return fmaxf(__uint_as_float(r[0]), __uint_as_float(r[1]));
}
DI float xhalf_sum(float x) {
  auto r = __builtin_amdgcn_permlane32_swap(__float_as_uint(x), __float_as_uint(x), false, false);
  return __uint_as_float(r[0]) + __uint_as_float(r[1]);
}

template <int BM, int BN, int WM, int WN>
struct GCfg {
  static constexpr int NW = WM * WN, TM = BM / WM, TN = BN / WN, MT = TM / 32, NT = TN / 32;
  static constexpr int XB = BM * 128, WROW = BN * 2, WB = 64 * WROW, STAGE = XB + WB;
  static constexpr int XI = BM / 8 / NW;
  static constexpr int WI = WB / 1024 / NW;
  static constexpr int WCH = BN / 8;
  static constexpr int WRPI = 64 / WCH;
  static constexpr int LDS = 2 * STAGE;
};

template <class C, class Epi>
DI void gemm_tile(const u16* __restrict__ X, int ldx, const u16* __restrict__ W, int ldw, int m0, int n0, int ncols,
                  int kbeg, int nk, const Epi& epi) {
  const int tid = threadIdx.x, lane = tid & 63, wid = __builtin_amdgcn_readfirstlane(tid >> 6);
  const int wm = wid / C::WN_, wn = wid % C::WN_;
  const int r = lane & 31, h = lane >> 5;
  unsigned xoff[C::XI], woff[C::WI];
#pragma unroll
  for (int i = 0; i < C::XI; ++i) {
    const int row = 8 * (wid * C::XI + i) + (lane >> 3);
    xoff[i] = (unsigned)(row * ldx + (((lane & 7) ^ ((row >> 1) & 7)) * 8)) * 2u;
  }
#pragma unroll
  for (int i = 0; i < C::WI; ++i) {
    const int k = C::WRPI * (wid * C::WI + i) + lane / C::WCH;
    int col = (((lane % C::WCH) ^ ((k & 3) << 2)) * 8);
    if (n0 + col > ncols - 8) col = ncols - 8 - n0;
    woff[i] = (unsigned)(k * ldw + col) * 2u;
  }
  const u16* xs = X + (size_t)m0 * ldx + kbeg;
  const u16* ws = W + (size_t)kbeg * ldw + n0;
  auto stage = [&](int buf, int kt) {
    const u16* xb = xs + kt * 64;
    const u16* wb = ws + (size_t)kt * 64 * ldw;
    const unsigned lb = buf * C::STAGE;
#pragma unroll
    for (int i = 0; i < C::XI; ++i) dma16(xb, xoff[i], lb + (wid * C::XI + i) * 1024);
#pragma unroll
    for (int i = 0; i < C::WI; ++i) dma16(wb, woff[i], lb + C::XB + (wid * C::WI + i) * 1024);
  };
  unsigned xa[4];
#pragma unroll
  for (int ks = 0; ks < 4; ++ks) xa[ks] = (wm * C::TM + r) * 128 + (((2 * ks + h) ^ ((r >> 1) & 7)) * 16);
  unsigned wa[C::NT];
  {
    const int q = (lane & 15) >> 2, p = lane & 3, cb = (lane >> 4) & 1;
#pragma unroll
    for (int nt = 0; nt < C::NT; ++nt) {
      const int nb = wn * C::NT + nt;
      wa[nt] = C::XB + (8 * h + q) * C::WROW + ((nb ^ q) * 64) + cb * 32 + p * 8;
    }
  }
  f32x16 acc[C::NT][C::MT];
#pragma unroll
  for (int nt = 0; nt < C::NT; ++nt)
#pragma unroll
    for (int mt = 0; mt < C::MT; ++mt) acc[nt][mt] = f32x16{};

  stage(0, 0);
  WAIT_VM0();
  BARRIER();
  for (int kt = 0; kt < nk; ++kt) {
    const unsigned lb = (kt & 1) * C::STAGE;
    const bool more = kt + 1 < nk;
    const u16* xb = xs + (kt + 1) * 64;
    const u16* wb = ws + (size_t)(kt + 1) * 64 * ldw;
    const unsigned nb = ((kt + 1) & 1) * C::STAGE;
#pragma unroll
    for (int ks = 0; ks < 4; ++ks) {
      bf16x8 xf[C::MT], wf[C::NT];
#pragma unroll
      for (int mt = 0; mt < C::MT; ++mt) xf[mt] = lds_read128(lb + xa[ks] + mt * 4096);
#pragma unroll
      for (int nt = 0; nt < C::NT; ++nt)
        wf[nt] = cat8(lds_read_tr(lb + wa[nt] + (16 * ks) * C::WROW), lds_read_tr(lb + wa[nt] + (16 * ks + 4) * C::WROW));
      if (more) {
        constexpr int TOT = C::XI + C::WI;
#pragma unroll
        for (int i = ks * TOT / 4; i < (ks + 1) * TOT / 4; ++i) {
          if (i < C::XI) dma16(xb, xoff[i], nb + (wid * C::XI + i) * 1024);
          else dma16(wb, woff[i - C::XI], nb + C::XB + (wid * C::WI + i - C::XI) * 1024);
        }
      }
#pragma unroll
      for (int nt = 0; nt < C::NT; ++nt)
#pragma unroll
        for (int mt = 0; mt < C::MT; ++mt) acc[nt][mt] = MFMA32(wf[nt], xf[mt], acc[nt][mt]);
    }
    WAIT_VM0();
    BARRIER();
  }
  if constexpr (Epi::DIRECT) {
#pragma unroll
    for (int nt = 0; nt < C::NT; ++nt)
#pragma unroll
      for (int mt = 0; mt < C::MT; ++mt)
#pragma unroll
        for (int g = 0; g < 4; ++g) {
          const int n = n0 + wn * C::TN + nt * 32 + 8 * g + 4 * h;
          const int m = m0 + wm * C::TM + mt * 32 + r;
          epi(m, n, acc[nt][mt][4 * g], acc[nt][mt][4 * g + 1], acc[nt][mt][4 * g + 2], acc[nt][mt][4 * g + 3]);
        }
  } else {
    static_assert(C::TN == 64, "staging layout");
    const unsigned sb = wid * (C::TM * 128);
#pragma unroll
    for (int nt = 0; nt < C::NT; ++nt)
#pragma unroll
      for (int g = 0; g < 4; ++g) {
        const int nl = nt * 32 + 8 * g + 4 * h;
        const float sc = epi.scale_of(n0 + wn * C::TN + nl);
        const unsigned a = sb + r * 128 + (((nl >> 3) ^ (r & 7)) * 16) + (nl & 4) * 2;
#pragma unroll
        for (int mt = 0; mt < C::MT; ++mt) {
          u32x2 v = {cvtpk(acc[nt][mt][4 * g] * sc, acc[nt][mt][4 * g + 1] * sc),
                     cvtpk(acc[nt][mt][4 * g + 2] * sc, acc[nt][mt][4 * g + 3] * sc)};
          *(lds_u32x2*)(uintptr_t)(a + mt * 4096) = v;
        }
      }
    epi.template store<C::TM>(sb, m0 + wm * C::TM, n0 + wn * C::TN, lane);
  }
}

template <int BM, int BN, int WM, int WN>
struct GC : GCfg<BM, BN, WM, WN> { static constexpr int WN_ = WN; };

struct EpiPlain {
  static constexpr bool DIRECT = false;
  u16* out; int ld; float scale;
  DI float scale_of(int) const { return scale; }
  template <int TM> DI void store(unsigned sb, int mb, int nb, int lane) const {
    const int rr = lane >> 3, c = lane & 7;
    u16* dst = out + (size_t)(mb + rr) * ld + nb + c * 8;
    const unsigned a = sb + rr * 128 + ((c ^ (rr & 7)) * 16);
#pragma unroll
    for (int it = 0; it < TM / 8; ++it) {
      const bf16x8 v = lds_read128(a + it * 1024);
      *(bf16x8*)(dst + (size_t)it * 8 * ld) = v;
    }
  }
};
struct EpiQKVA {
  static constexpr bool DIRECT = false;
  u16* out; float qscale;
  DI float scale_of(int n) const { return n < DM ? qscale : 1.f; }
  template <int TM> DI void store(unsigned sb, int mb, int nb, int lane) const {
    const int rr = lane >> 4, gq = lane & 15;
    const int n = nb + gq * 4;
    if (n >= 3 * DM) return;
    const int which = n / DM, rem = n - which * DM, head = rem / DHA, dd = rem - head * DHA;
    u16* dst = out + ((size_t)((which * HA + head) * S_ + mb + rr)) * 32;
#pragma unroll 4
    for (int it = 0; it < TM / 4; ++it) {
      const int row = it * 4 + rr;
      const u32x2 v = *(const lds_u32x2*)(uintptr_t)(sb + row * 128 + (((gq >> 1) ^ (row & 7)) * 16) + (gq & 1) * 8);
      u16* d = dst + it * 4 * 32;
      *(u32x2*)(d + dd) = v;
      if (dd == 16) {
        u32x2 z0 = {which == 2 ? 0x3F80u : 0u, 0u};
        u32x4 z1 = {0u, 0u, 0u, 0u};
        *(u32x2*)(d + 20) = z0;
        *(u32x4*)(d + 24) = z1;
      }
    }
  }
};
struct EpiF32 {
  static constexpr bool DIRECT = true;
  float* out; int ld;
  DI void operator()(int m, int n, float a, float b, float c, float d) const {
    f32x4 v = {a, b, c, d};
    *(f32x4*)(out + (size_t)m * ld + n) = v;
  }
};

typedef GC<256, 256, 2, 4> G1;
typedef GC<128, 128, 2, 2> G2;

constexpr float LOG2E = 1.4426950408889634f;

__global__ __launch_bounds__(512, 2) void k_proj(const u16* __restrict__ Ax, const u16* __restrict__ Awqkv,
                                                 const u16* __restrict__ Bx, const u16* __restrict__ Bckv,
                                                 const u16* __restrict__ Bwq, const u16* __restrict__ Bwuk,
                                                 const u16* __restrict__ Bwuv, u16* __restrict__ qkvA,
                                                 u16* __restrict__ qB, u16* __restrict__ kB, u16* __restrict__ vB) {
  const int b = blockIdx.x, xcd = b & 7, idx = b >> 3;
  const int tn = xcd + 8 * (idx >> 3), tm = idx & 7;
  const int m0 = tm * 256;
  if (tn < 8) {
    EpiQKVA e{qkvA, 0.22360679774997896f * LOG2E};
    gemm_tile<G1>(Ax, DM, Awqkv, 3 * DM, m0, tn * 256, 3 * DM, 0, DM / 64, e);
  } else if (tn < 24) {
    EpiPlain e{qB, NB, 0.08838834764831845f * LOG2E};
    gemm_tile<G1>(Bx, DM, Bwq, NB, m0, (tn - 8) * 256, NB, 0, DM / 64, e);
  } else {
    const bool isv = tn >= 40;
    EpiPlain e{isv ? vB : kB, NB, 1.f};
    gemm_tile<G1>(Bckv, DC, isv ? Bwuv : Bwuk, NB, m0, (tn - (isv ? 40 : 24)) * 256, NB, 0, DC / 64, e);
  }
}

constexpr int SPLITK = 4;
__global__ __launch_bounds__(256, 2) void k_out(const u16* __restrict__ oA, const u16* __restrict__ Awo,
                                                const u16* __restrict__ ctx, const u16* __restrict__ Bwo,
                                                u16* __restrict__ out, float* __restrict__ part) {
  const int b = blockIdx.x;
  if (b < 80 * SPLITK) {
    const int sp = b & 3, t = b >> 2, tn = t % 5, tm = t / 5;
    EpiF32 e{part + (size_t)sp * S_ * DM, DM};
    gemm_tile<G2>(ctx, NB, Bwo, DM, tm * 128, tn * 128, DM, sp * (NB / SPLITK), NB / SPLITK / 64, e);
  } else {
    const int t = b - 80 * SPLITK, tn = t % 5, tm = t / 5;
    EpiPlain e{out, 2 * DM, 1.f};
    gemm_tile<G2>(oA, DM, Awo, DM, tm * 128, tn * 128, DM, 0, DM / 64, e);
  }
}

__global__ __launch_bounds__(256) void k_reduce(const float* __restrict__ part, u16* __restrict__ out) {
  const int i = blockIdx.x * 256 + threadIdx.x;
  const int m = i / (DM / 4), n = (i % (DM / 4)) * 4;
  f32x4 s = *(const f32x4*)(part + (size_t)m * DM + n);
#pragma unroll
  for (int sp = 1; sp < SPLITK; ++sp) s += *(const f32x4*)(part + (size_t)sp * S_ * DM + (size_t)m * DM + n);
  u32x2 v = {cvtpk(s[0], s[1]), cvtpk(s[2], s[3])};
  *(u32x2*)(out + (size_t)m * 2 * DM + DM + n) = v;
}

constexpr float THR = 8.f;
DI bf16x8 pack8(const f32x16& x, int s) {
  u32x4 w = {cvtpk(x[8 * s], x[8 * s + 1]), cvtpk(x[8 * s + 2], x[8 * s + 3]), cvtpk(x[8 * s + 4], x[8 * s + 5]),
             cvtpk(x[8 * s + 6], x[8 * s + 7])};
  return __builtin_bit_cast(bf16x8, w);
}
DI f32x16 splat16(float v) {
  f32x16 x;
#pragma unroll
  for (int i = 0; i < 16; ++i) x[i] = v;
  return x;
}
DI float max16(const f32x16& x) {
  float a = fmaxf(fmaxf(x[0], x[1]), x[2]);
#pragma unroll
  for (int i = 3; i < 15; i += 2) a = fmaxf(fmaxf(a, x[i]), x[i + 1]);
  return fmaxf(a, x[15]);
}

#define SBAR() __builtin_amdgcn_sched_barrier(0)
constexpr float BIGSUM = 4096.f;
DI float sum16(const f32x16& x) {
  float a = (x[0] + x[1]) + (x[2] + x[3]), b = (x[4] + x[5]) + (x[6] + x[7]);
  float c = (x[8] + x[9]) + (x[10] + x[11]), d = (x[12] + x[13]) + (x[14] + x[15]);
  return (a + b) + (c + d);
}
DI void exp16(f32x16& x) {
#pragma unroll
  for (int i = 0; i < 16; ++i) x[i] = __builtin_amdgcn_exp2f(x[i]);
}
DI void attnB_block(const u16* __restrict__ Q, const u16* __restrict__ K, const u16* __restrict__ V,
                    u16* __restrict__ ctx, int head, int qb) {
  const int tid = threadIdx.x, lane = tid & 63, wid = __builtin_amdgcn_readfirstlane(tid >> 6);
  const int r = lane & 31, h = lane >> 5;
  const int q0 = qb * 128 + wid * 32;
  const int nfull = 2 * qb;
  constexpr unsigned KBUF = 0, VBUF = 32768, TB = 16384;
  unsigned koff, voff;
  {
    const int row = 4 * wid + (lane >> 4), c = lane & 15;
    koff = (unsigned)(row * NB + ((c ^ (row & 15)) * 8)) * 2u;
    voff = (unsigned)(row * NB + ((c ^ ((row & 3) << 2)) * 8)) * 2u;
  }
  const u16* Kh = K + head * DHB;
  const u16* Vh = V + head * DHB;
  auto stage = [&](int buf, int t) {
    const u16* kb = Kh + (size_t)t * 64 * NB;
    const u16* vb = Vh + (size_t)t * 64 * NB;
#pragma unroll
    for (int i = 0; i < 4; ++i) dma16(kb + i * 16 * NB, koff, KBUF + buf * TB + (4 * i + wid) * 1024);
#pragma unroll
    for (int i = 0; i < 4; ++i) dma16(vb + i * 16 * NB, voff, VBUF + buf * TB + (4 * i + wid) * 1024);
  };
  stage(0, 0);
  bf16x8 qf[8];
  {
    const u16* qp = Q + (size_t)(q0 + r) * NB + head * DHB + h * 8;
#pragma unroll
    for (int ks = 0; ks < 8; ++ks) qf[ks] = *(const bf16x8*)(qp + ks * 16);
  }
  unsigned ka[8], va[4];
#pragma unroll
  for (int ks = 0; ks < 8; ++ks) ka[ks] = KBUF + r * 256 + (((2 * ks + h) ^ (r & 15)) * 16);
  {
    const int q = (lane & 15) >> 2, p = lane & 3, cb = (lane >> 4) & 1;
#pragma unroll
    for (int db = 0; db < 4; ++db) va[db] = VBUF + (4 * h + q) * 256 + ((db ^ q) * 64) + cb * 32 + p * 8;
  }
  f32x16 o[4];
#pragma unroll
  for (int db = 0; db < 4; ++db) o[db] = f32x16{};
  float mcur = 0.f, lsum = 0.f;
  f32x16 negm = f32x16{};
#pragma unroll
  for (int ks = 0; ks < 8; ++ks) asm volatile("" : "+v"(qf[ks]));
  WAIT_VM0();
  BARRIER();

  auto qk = [&](unsigned off) {
    bf16x8 kf[8];
#pragma unroll
    for (int ks = 0; ks < 8; ++ks) kf[ks] = lds_read128(off + ka[ks]);
    f32x16 s = MFMA32(kf[0], qf[0], negm);
#pragma unroll
    for (int ks = 1; ks < 8; ++ks) s = MFMA32(kf[ks], qf[ks], s);
    return s;
  };
  auto pv = [&](unsigned off, const f32x16& p) {
#pragma unroll
    for (int sx = 0; sx < 2; ++sx) {
      const bf16x8 pf = pack8(p, sx);
#pragma unroll
      for (int db = 0; db < 4; ++db) {
        const bf16x8 vf = cat8(lds_read_tr(off + va[db] + (16 * sx) * 256), lds_read_tr(off + va[db] + (16 * sx + 8) * 256));
        o[db] = MFMA32(vf, pf, o[db]);
      }
    }
  };
  auto remax = [&](f32x16& s, bool first) {
    const float tm = xhalf_max(max16(s));
    const float dpos = fmaxf(tm, 0.f);
    const float delta = first ? tm : dpos;
    const float f = __builtin_amdgcn_exp2f(-dpos);
    mcur += delta;
    negm = splat16(-mcur);
#pragma unroll
    for (int i = 0; i < 16; ++i) s[i] -= delta;
#pragma unroll
    for (int db = 0; db < 4; ++db)
#pragma unroll
      for (int i = 0; i < 16; ++i) o[db][i] *= f;
    lsum *= f;
    return delta;
  };

  for (int t = 0; t < nfull; ++t) {
    const unsigned bo = (t & 1) * TB;
    stage((t + 1) & 1, t + 1);
    f32x16 s0 = qk(bo);
    f32x16 s1 = qk(bo + 8192);
    exp16(s0);
    float ps0 = sum16(s0);
    if (t == 0 || __any(!(ps0 <= BIGSUM))) {
      s0 = qk(bo);
      const float delta = remax(s0, t == 0);
#pragma unroll
      for (int i = 0; i < 16; ++i) s1[i] -= delta;
      exp16(s0);
      ps0 = sum16(s0);
    }
    pv(bo, s0);
    exp16(s1);
    float ps1 = sum16(s1);
    lsum += ps0;
    if (__any(!(ps1 <= BIGSUM))) {
      s1 = qk(bo + 8192);
      remax(s1, false);
      exp16(s1);
      ps1 = sum16(s1);
    }
    pv(bo + 32 * 256, s1);
    lsum += ps1;
    WAIT_VM0();
    BARRIER();
  }
  for (int tt = 0; tt < 2; ++tt) {
    const unsigned bo = ((nfull + tt) & 1) * TB;
    if (tt == 0) stage((nfull + 1) & 1, nfull + 1);
    for (int jj = 0; jj < 2; ++jj) {
      const int j = 2 * tt + jj;
      if (j <= wid) {
        f32x16 s = qk(bo + jj * 8192);
        if (j == wid) {
#pragma unroll
          for (int i = 0; i < 16; ++i) {
            const int kr = (i & 3) + 8 * (i >> 2) + 4 * h;
            if (kr > r) s[i] = -__builtin_inff();
          }
        }
        remax(s, nfull == 0 && j == 0);
        exp16(s);
        lsum += sum16(s);
        pv(bo + jj * 32 * 256, s);
      }
    }
    WAIT_VM0();
    BARRIER();
  }
  const float inv = 1.f / xhalf_sum(lsum);
  u16* op = ctx + (size_t)(q0 + r) * NB + head * DHB + 4 * h;
#pragma unroll
  for (int db = 0; db < 4; ++db)
#pragma unroll
    for (int g = 0; g < 4; ++g) {
      u32x2 v = {cvtpk(o[db][4 * g] * inv, o[db][4 * g + 1] * inv), cvtpk(o[db][4 * g + 2] * inv, o[db][4 * g + 3] * inv)};
      *(u32x2*)(op + 32 * db + 8 * g) = v;
    }
}

template <int QT>
DI void attnA_block(const u16* __restrict__ qkv, u16* __restrict__ oA, int head, int qb) {
  const int tid = threadIdx.x, lane = tid & 63, wid = __builtin_amdgcn_readfirstlane(tid >> 6);
  const int r = lane & 31, h = lane >> 5;
  const int q0 = qb * (128 * QT) + wid * (32 * QT);
  constexpr unsigned KBUF = 0, VBUF = 16384, TB = 8192;
  const u16* Qh = qkv + (size_t)(0 * HA + head) * S_ * 32;
  const u16* Kh = qkv + (size_t)(1 * HA + head) * S_ * 32;
  const u16* Vh = qkv + (size_t)(2 * HA + head) * S_ * 32;
  unsigned koff[2], voff[2];
#pragma unroll
  for (int i = 0; i < 2; ++i) {
    const int row = 16 * (wid * 2 + i) + (lane >> 2), c = lane & 3;
    koff[i] = (unsigned)(row * 64 + ((c ^ ((row >> 2) & 3)) * 16));
    voff[i] = (unsigned)(row * 64 + c * 16);
  }
  auto stage = [&](int buf, int t) {
    const u16* kb = Kh + (size_t)t * 128 * 32;
    const u16* vb = Vh + (size_t)t * 128 * 32;
#pragma unroll
    for (int i = 0; i < 2; ++i) dma16(kb, koff[i], KBUF + buf * TB + (wid * 2 + i) * 1024);
#pragma unroll
    for (int i = 0; i < 2; ++i) dma16(vb, voff[i], VBUF + buf * TB + (wid * 2 + i) * 1024);
  };
  stage(0, 0);
  bf16x8 qf[QT][2];
#pragma unroll
  for (int qt = 0; qt < QT; ++qt)
#pragma unroll
    for (int ks = 0; ks < 2; ++ks) qf[qt][ks] = *(const bf16x8*)(Qh + (size_t)(q0 + 32 * qt + r) * 32 + ks * 16 + h * 8);
  unsigned ka[2], va;
#pragma unroll
  for (int ks = 0; ks < 2; ++ks) ka[ks] = KBUF + r * 64 + (((2 * ks + h) ^ ((r >> 2) & 3)) * 16);
  {
    const int q = (lane & 15) >> 2, p = lane & 3, cb = (lane >> 4) & 1;
    va = VBUF + (4 * h + q) * 64 + cb * 32 + p * 8;
  }
  f32x16 o[QT], negm[QT];
  float mcur[QT];
#pragma unroll
  for (int qt = 0; qt < QT; ++qt) { o[qt] = f32x16{}; negm[qt] = f32x16{}; mcur[qt] = 0.f; }
#pragma unroll
  for (int qt = 0; qt < QT; ++qt) { asm volatile("" : "+v"(qf[qt][0])); asm volatile("" : "+v"(qf[qt][1])); }
  WAIT_VM0();
  BARRIER();
  constexpr int NKT = S_ / 128;
  for (int t = 0; t < NKT; ++t) {
    const unsigned bo = (t & 1) * TB;
    if (t + 1 < NKT) stage((t + 1) & 1, t + 1);
    f32x16 s[QT][4];
#pragma unroll
    for (int kb = 0; kb < 4; ++kb) {
      const bf16x8 k0 = lds_read128(bo + ka[0] + kb * 2048), k1 = lds_read128(bo + ka[1] + kb * 2048);
#pragma unroll
      for (int qt = 0; qt < QT; ++qt) {
        s[qt][kb] = MFMA32(k0, qf[qt][0], negm[qt]);
        s[qt][kb] = MFMA32(k1, qf[qt][1], s[qt][kb]);
      }
    }
    float tm[QT];
    bool need = t == 0;
#pragma unroll
    for (int qt = 0; qt < QT; ++qt) {
      tm[qt] = xhalf_max(fmaxf(fmaxf(max16(s[qt][0]), max16(s[qt][1])), fmaxf(max16(s[qt][2]), max16(s[qt][3]))));
      need = need || tm[qt] > THR;
    }
    if (__any(need)) {
#pragma unroll
      for (int qt = 0; qt < QT; ++qt) {
        const float dpos = fmaxf(tm[qt], 0.f);
        const float delta = t == 0 ? tm[qt] : dpos;
        const float f = __builtin_amdgcn_exp2f(-dpos);
        mcur[qt] += delta;
        negm[qt] = splat16(-mcur[qt]);
#pragma unroll
        for (int kb = 0; kb < 4; ++kb)
#pragma unroll
          for (int i = 0; i < 16; ++i) s[qt][kb][i] -= delta;
#pragma unroll
        for (int i = 0; i < 16; ++i) o[qt][i] *= f;
      }
    }
#pragma unroll
    for (int kb = 0; kb < 4; ++kb) {
#pragma unroll
      for (int sx = 0; sx < 2; ++sx) {
        const bf16x8 vf = cat8(lds_read_tr(bo + va + (32 * kb + 16 * sx) * 64), lds_read_tr(bo + va + (32 * kb + 16 * sx + 8) * 64));
#pragma unroll
        for (int qt = 0; qt < QT; ++qt) {
#pragma unroll
          for (int i = 0; i < 8; ++i) s[qt][kb][8 * sx + i] = __builtin_amdgcn_exp2f(s[qt][kb][8 * sx + i]);
          o[qt] = MFMA32(vf, pack8(s[qt][kb], sx), o[qt]);
        }
      }
    }
    WAIT_VM0();
    BARRIER();
  }
#pragma unroll
  for (int qt = 0; qt < QT; ++qt) {
    const float l = __shfl(o[qt][8], r + 32);
    const float inv = 1.f / l;
    u16* op = oA + (size_t)(q0 + 32 * qt + r) * DM + head * DHA + 4 * h;
#pragma unroll
    for (int g = 0; g < 3; ++g) {
      if (g < 2 || h == 0) {
        u32x2 v = {cvtpk(o[qt][4 * g] * inv, o[qt][4 * g + 1] * inv), cvtpk(o[qt][4 * g + 2] * inv, o[qt][4 * g + 3] * inv)};
        *(u32x2*)(op + 8 * g) = v;
      }
    }
  }
}

constexpr int AQT = 2;
constexpr int NBLK_B = HB * 8, NBLK_A = HA * (S_ / (128 * AQT));
__global__ __launch_bounds__(256, 2) void k_attn(const u16* __restrict__ qB, const u16* __restrict__ kB,
                                                 const u16* __restrict__ vB, u16* __restrict__ ctx,
                                                 const u16* __restrict__ qkvA, u16* __restrict__ oA) {
  const int b = blockIdx.x;
  if (b < NBLK_B) {
    const int head = (b & 7) + 8 * (b >> 6), j = (b >> 3) & 7;
#pragma unroll 1
    for (int pass = 0; pass < 2; ++pass) attnB_block(qB, kB, vB, ctx, head, pass ? j : 15 - j);
  } else {
    const int a = b - NBLK_B;
    constexpr int PER = S_ / (128 * AQT);
    const int head = (a & 7) + 8 * (a / (8 * PER)), qb = (a >> 3) % PER;
    attnA_block<AQT>(qkvA, oA, head, qb);
  }
}

extern "C" void kernel_launch(void* const* d_in, const int* in_sizes, int n_in, void* d_out, int out_size, void* d_ws,
                              size_t ws_size, hipStream_t stream) {
  const u16* Ax = (const u16*)d_in[0];
  const u16* Awqkv = (const u16*)d_in[1];
  const u16* Awo = (const u16*)d_in[2];
  const u16* Bx = (const u16*)d_in[3];
  const u16* Bckv = (const u16*)d_in[4];
  const u16* Bwq = (const u16*)d_in[5];
  const u16* Bwuk = (const u16*)d_in[6];
  const u16* Bwuv = (const u16*)d_in[7];
  const u16* Bwo = (const u16*)d_in[8];
  u16* out = (u16*)d_out;
  char* ws = (char*)d_ws;
  u16* qkvA = (u16*)ws;                 ws += (size_t)3 * HA * S_ * 32 * 2;
  u16* qB = (u16*)ws;                   ws += (size_t)S_ * NB * 2;
  u16* kB = (u16*)ws;                   ws += (size_t)S_ * NB * 2;
  u16* vB = (u16*)ws;                   ws += (size_t)S_ * NB * 2;
  u16* ctx = (u16*)ws;                  ws += (size_t)S_ * NB * 2;
  u16* oA = (u16*)ws;                   ws += (size_t)S_ * DM * 2;
  float* part = (float*)ws;             ws += (size_t)SPLITK * S_ * DM * 4;

  (void)hipFuncSetAttribute((const void*)k_proj, hipFuncAttributeMaxDynamicSharedMemorySize, G1::LDS);
  (void)hipFuncSetAttribute((const void*)k_out, hipFuncAttributeMaxDynamicSharedMemorySize, G2::LDS);
  (void)hipFuncSetAttribute((const void*)k_attn, hipFuncAttributeMaxDynamicSharedMemorySize, 65536);
  k_proj<<<448, 512, G1::LDS, stream>>>(Ax, Awqkv, Bx, Bckv, Bwq, Bwuk, Bwuv, qkvA, qB, kB, vB);
  k_attn<<<NBLK_B + NBLK_A, 256, 65536, stream>>>(qB, kB, vB, ctx, qkvA, oA);
  k_out<<<80 * SPLITK + 80, 256, G2::LDS, stream>>>(oA, Awo, ctx, Bwo, out, part);
  k_reduce<<<S_ * DM / 4 / 256, 256, 0, stream>>>(part, out);
}
```

```cpp
#include <hip/hip_runtime.h>
#include <hip/hip_bf16.h>
#include <stdint.h>
#include <type_traits>

constexpr int S_ = 2048;
constexpr int DM = 640;
constexpr int HA = 32, DHA = 20;
constexpr int HB = 32, DHB = 128, DC = 512;
constexpr int NB = HB * DHB;

typedef unsigned short u16;
typedef short bf16x8 __attribute__((ext_vector_type(8)));
typedef short s16x4 __attribute__((ext_vector_type(4)));
typedef float f32x16 __attribute__((ext_vector_type(16)));
typedef float f32x4 __attribute__((ext_vector_type(4)));
typedef float f32x2 __attribute__((ext_vector_type(2)));
typedef __bf16 bf16x2 __attribute__((ext_vector_type(2)));
typedef unsigned u32x2 __attribute__((ext_vector_type(2)));
typedef unsigned u32x4 __attribute__((ext_vector_type(4)));
typedef __attribute__((address_space(3))) s16x4 lds_s16x4;
typedef __attribute__((address_space(3))) bf16x8 lds_bf16x8;
typedef __attribute__((address_space(3))) u32x2 lds_u32x2;

#define DI __device__ __forceinline__
#define MFMA32(a, b, c) __builtin_amdgcn_mfma_f32_32x32x16_bf16((a), (b), (c), 0, 0, 0)

DI unsigned cvtpk(float lo, float hi) {
  f32x2 v = {lo, hi};
  bf16x2 b = __builtin_convertvector(v, bf16x2);
  return __builtin_bit_cast(unsigned, b);
}
DI void dma16(const void* sbase, unsigned voff, unsigned lds_addr) {
  unsigned keep;
  asm volatile("s_mov_b32 %0, m0\n\ts_mov_b32 m0, %3\n\ts_nop 0\n\tglobal_load_lds_dwordx4 %1, %2\n\ts_mov_b32 m0, %0"
               : "=&s"(keep) : "v"(voff), "s"(sbase), "s"(lds_addr) : "memory");
}
#define WAIT_VM0() asm volatile("s_waitcnt vmcnt(0)" ::: "memory")
#define BARRIER() do { asm volatile("s_waitcnt lgkmcnt(0)" ::: "memory"); __builtin_amdgcn_s_barrier(); asm volatile("" ::: "memory"); } while (0)

DI bf16x8 lds_read128(unsigned off) { return *(const lds_bf16x8*)(uintptr_t)off; }
DI s16x4 lds_read_tr(unsigned off) { return __builtin_amdgcn_ds_read_tr16_b64_v4i16((lds_s16x4*)(uintptr_t)off); }
DI bf16x8 cat8(s16x4 a, s16x4 b) { return __builtin_shufflevector(a, b, 0, 1, 2, 3, 4, 5, 6, 7); }
DI float xhalf_max(float x) {
  auto r = __builtin_amdgcn_permlane32_swap(__float_as_uint(x), __float_as_uint(x), false, false);
  return fmaxf(__uint_as_float(r[0]), __uint_as_float(r[1]));
}
DI float xhalf_sum(float x) {
  auto r = __builtin_amdgcn_permlane32_swap(__float_as_uint(x), __float_as_uint(x), false, false);
  return __uint_as_float(r[0]) + __uint_as_float(r[1]);
}

template <int BM, int BN, int WM, int WN>
struct GCfg {
  static constexpr int NW = WM * WN, TM = BM / WM, TN = BN / WN, MT = TM / 32, NT = TN / 32;
  static constexpr int XB = BM * 128, WROW = BN * 2, WB = 64 * WROW, STAGE = XB + WB;
  static constexpr int XI = BM / 8 / NW;
  static constexpr int WI = WB / 1024 / NW;
  static constexpr int WCH = BN / 8;
  static constexpr int WRPI = 64 / WCH;
  static constexpr int LDS = 2 * STAGE;
};

template <class C, class Epi, bool XHEAD = false>
DI void gemm_tile(const u16* __restrict__ X, int ldx, const u16* __restrict__ W, int ldw, int m0, int n0, int ncols,
                  int kbeg, int nk, const Epi& epi) {
  const int tid = threadIdx.x, lane = tid & 63, wid = __builtin_amdgcn_readfirstlane(tid >> 6);
  const int wm = wid / C::WN_, wn = wid % C::WN_;
  const int r = lane & 31, h = lane >> 5;
  unsigned xoff[C::XI], woff[C::WI];
#pragma unroll
  for (int i = 0; i < C::XI; ++i) {
    const int row = 8 * (wid * C::XI + i) + (lane >> 3);
    xoff[i] = (unsigned)(row * ldx + (((lane & 7) ^ ((row >> 1) & 7)) * 8)) * 2u;
  }
#pragma unroll
  for (int i = 0; i < C::WI; ++i) {
    const int k = C::WRPI * (wid * C::WI + i) + lane / C::WCH;
    int col = (((lane % C::WCH) ^ ((k & 3) << 2)) * 8);
    if (n0 + col > ncols - 8) col = ncols - 8 - n0;
    woff[i] = (unsigned)(k * ldw + col) * 2u;
  }
  const u16* xs = XHEAD ? X + (size_t)m0 * ldx + (size_t)(kbeg >> 7) * S_ * DHB : X + (size_t)m0 * ldx + kbeg;
  auto xk = [&](int kt) { return XHEAD ? xs + (size_t)(kt >> 1) * S_ * DHB + (kt & 1) * 64 : xs + kt * 64; };
  const u16* ws = W + (size_t)kbeg * ldw + n0;
  auto stage = [&](int buf, int kt) {
    const u16* xb = xk(kt);
    const u16* wb = ws + (size_t)kt * 64 * ldw;
    const unsigned lb = buf * C::STAGE;
#pragma unroll
    for (int i = 0; i < C::XI; ++i) dma16(xb, xoff[i], lb + (wid * C::XI + i) * 1024);
#pragma unroll
    for (int i = 0; i < C::WI; ++i) dma16(wb, woff[i], lb + C::XB + (wid * C::WI + i) * 1024);
  };
  unsigned xa[4];
#pragma unroll
  for (int ks = 0; ks < 4; ++ks) xa[ks] = (wm * C::TM + r) * 128 + (((2 * ks + h) ^ ((r >> 1) & 7)) * 16);
  unsigned wa[C::NT];
  {
    const int q = (lane & 15) >> 2, p = lane & 3, cb = (lane >> 4) & 1;
#pragma unroll
    for (int nt = 0; nt < C::NT; ++nt) {
      const int nb = wn * C::NT + nt;
      wa[nt] = C::XB + (8 * h + q) * C::WROW + ((nb ^ q) * 64) + cb * 32 + p * 8;
    }
  }
  f32x16 acc[C::NT][C::MT];
#pragma unroll
  for (int nt = 0; nt < C::NT; ++nt)
#pragma unroll
    for (int mt = 0; mt < C::MT; ++mt) acc[nt][mt] = f32x16{};

  stage(0, 0);
  WAIT_VM0();
  BARRIER();
  for (int kt = 0; kt < nk; ++kt) {
    const unsigned lb = (kt & 1) * C::STAGE;
    const bool more = kt + 1 < nk;
    const u16* xb = xk(kt + 1);
    const u16* wb = ws + (size_t)(kt + 1) * 64 * ldw;
    const unsigned nb = ((kt + 1) & 1) * C::STAGE;
#pragma unroll
    for (int ks = 0; ks < 4; ++ks) {
      bf16x8 xf[C::MT], wf[C::NT];
#pragma unroll
      for (int mt = 0; mt < C::MT; ++mt) xf[mt] = lds_read128(lb + xa[ks] + mt * 4096);
#pragma unroll
      for (int nt = 0; nt < C::NT; ++nt)
        wf[nt] = cat8(lds_read_tr(lb + wa[nt] + (16 * ks) * C::WROW), lds_read_tr(lb + wa[nt] + (16 * ks + 4) * C::WROW));
      if (more) {
        constexpr int TOT = C::XI + C::WI;
#pragma unroll
        for (int i = ks * TOT / 4; i < (ks + 1) * TOT / 4; ++i) {
          if (i < C::XI) dma16(xb, xoff[i], nb + (wid * C::XI + i) * 1024);
          else dma16(wb, woff[i - C::XI], nb + C::XB + (wid * C::WI + i - C::XI) * 1024);
        }
      }
#pragma unroll
      for (int nt = 0; nt < C::NT; ++nt)
#pragma unroll
        for (int mt = 0; mt < C::MT; ++mt) acc[nt][mt] = MFMA32(wf[nt], xf[mt], acc[nt][mt]);
    }
    WAIT_VM0();
    BARRIER();
  }
  if constexpr (Epi::DIRECT) {
#pragma unroll
    for (int nt = 0; nt < C::NT; ++nt)
#pragma unroll
      for (int mt = 0; mt < C::MT; ++mt)
#pragma unroll
        for (int g = 0; g < 4; ++g) {
          const int n = n0 + wn * C::TN + nt * 32 + 8 * g + 4 * h;
          const int m = m0 + wm * C::TM + mt * 32 + r;
          epi(m, n, acc[nt][mt][4 * g], acc[nt][mt][4 * g + 1], acc[nt][mt][4 * g + 2], acc[nt][mt][4 * g + 3]);
        }
  } else {
    static_assert(C::TN == 64, "staging layout");
    const unsigned sb = wid * (C::TM * 128);
#pragma unroll
    for (int nt = 0; nt < C::NT; ++nt)
#pragma unroll
      for (int g = 0; g < 4; ++g) {
        const int nl = nt * 32 + 8 * g + 4 * h;
        const float sc = epi.scale_of(n0 + wn * C::TN + nl);
        const unsigned a = sb + r * 128 + (((nl >> 3) ^ (r & 7)) * 16) + (nl & 4) * 2;
#pragma unroll
        for (int mt = 0; mt < C::MT; ++mt) {
          u32x2 v = {cvtpk(acc[nt][mt][4 * g] * sc, acc[nt][mt][4 * g + 1] * sc),
                     cvtpk(acc[nt][mt][4 * g + 2] * sc, acc[nt][mt][4 * g + 3] * sc)};
          *(lds_u32x2*)(uintptr_t)(a + mt * 4096) = v;
        }
      }
    epi.template store<C::TM>(sb, m0 + wm * C::TM, n0 + wn * C::TN, lane);
  }
}

template <int BM, int BN, int WM, int WN>
struct GC : GCfg<BM, BN, WM, WN> { static constexpr int WN_ = WN; };

struct EpiPlain {
  static constexpr bool DIRECT = false;
  u16* out; int ld; float scale;
  DI float scale_of(int) const { return scale; }
  template <int TM> DI void store(unsigned sb, int mb, int nb, int lane) const {
    const int rr = lane >> 3, c = lane & 7;
    u16* dst = out + (size_t)(mb + rr) * ld + nb + c * 8;
    const unsigned a = sb + rr * 128 + ((c ^ (rr & 7)) * 16);
#pragma unroll
    for (int it = 0; it < TM / 8; ++it) {
      const bf16x8 v = lds_read128(a + it * 1024);
      *(bf16x8*)(dst + (size_t)it * 8 * ld) = v;
    }
  }
};
struct EpiHead {
  static constexpr bool DIRECT = false;
  u16* out; float scale;
  DI float scale_of(int) const { return scale; }
  template <int TM> DI void store(unsigned sb, int mb, int nb, int lane) const {
    const int rr = lane >> 3, c = lane & 7;
    u16* dst = out + ((size_t)(nb >> 7) * S_ + mb + rr) * DHB + (nb & 127) + c * 8;
    const unsigned a = sb + rr * 128 + ((c ^ (rr & 7)) * 16);
#pragma unroll
    for (int it = 0; it < TM / 8; ++it) {
      const bf16x8 v = lds_read128(a + it * 1024);
      *(bf16x8*)(dst + (size_t)it * 8 * DHB) = v;
    }
  }
};
struct EpiQKVA {
  static constexpr bool DIRECT = false;
  u16* out; float qscale;
  DI float scale_of(int n) const { return n < DM ? qscale : 1.f; }
  template <int TM> DI void store(unsigned sb, int mb, int nb, int lane) const {
    const int rr = lane >> 4, gq = lane & 15;
    const int n = nb + gq * 4;
    if (n >= 3 * DM) return;
    const int which = n / DM, rem = n - which * DM, head = rem / DHA, dd = rem - head * DHA;
    u16* dst = out + ((size_t)((which * HA + head) * S_ + mb + rr)) * 32;
#pragma unroll 4
    for (int it = 0; it < TM / 4; ++it) {
      const int row = it * 4 + rr;
      const u32x2 v = *(const lds_u32x2*)(uintptr_t)(sb + row * 128 + (((gq >> 1) ^ (row & 7)) * 16) + (gq & 1) * 8);
      u16* d = dst + it * 4 * 32;
      *(u32x2*)(d + dd) = v;
      if (dd == 16) {
        u32x2 z0 = {which == 2 ? 0x3F80u : 0u, 0u};
        u32x4 z1 = {0u, 0u, 0u, 0u};
        *(u32x2*)(d + 20) = z0;
        *(u32x4*)(d + 24) = z1;
      }
    }
  }
};
struct EpiF32 {
  static constexpr bool DIRECT = true;
  float* out; int ld;
  DI void operator()(int m, int n, float a, float b, float c, float d) const {
    f32x4 v = {a, b, c, d};
    *(f32x4*)(out + (size_t)m * ld + n) = v;
  }
};

typedef GC<256, 256, 2, 4> G1;
typedef GC<128, 128, 2, 2> G2;

constexpr float LOG2E = 1.4426950408889634f;

__global__ __launch_bounds__(512, 2) void k_proj(const u16* __restrict__ Ax, const u16* __restrict__ Awqkv,
                                                 const u16* __restrict__ Bx, const u16* __restrict__ Bckv,
                                                 const u16* __restrict__ Bwq, const u16* __restrict__ Bwuk,
                                                 const u16* __restrict__ Bwuv, u16* __restrict__ qkvA,
                                                 u16* __restrict__ qB, u16* __restrict__ kB, u16* __restrict__ vB) {
  const int b = blockIdx.x, xcd = b & 7, idx = b >> 3;
  const int tn = xcd + 8 * (idx >> 3), tm = idx & 7;
  const int m0 = tm * 256;
  if (tn < 8) {
    EpiQKVA e{qkvA, 0.22360679774997896f * LOG2E};
    gemm_tile<G1>(Ax, DM, Awqkv, 3 * DM, m0, tn * 256, 3 * DM, 0, DM / 64, e);
  } else if (tn < 24) {
    EpiHead e{qB, 0.08838834764831845f * LOG2E};
    gemm_tile<G1>(Bx, DM, Bwq, NB, m0, (tn - 8) * 256, NB, 0, DM / 64, e);
  } else {
    const bool isv = tn >= 40;
    EpiHead e{isv ? vB : kB, 1.f};
    gemm_tile<G1>(Bckv, DC, isv ? Bwuv : Bwuk, NB, m0, (tn - (isv ? 40 : 24)) * 256, NB, 0, DC / 64, e);
  }
}

constexpr int SPLITK = 4;
__global__ __launch_bounds__(256, 2) void k_out(const u16* __restrict__ oA, const u16* __restrict__ Awo,
                                                const u16* __restrict__ ctx, const u16* __restrict__ Bwo,
                                                u16* __restrict__ out, float* __restrict__ part) {
  const int b = blockIdx.x;
  if (b < 80 * SPLITK) {
    const int sp = b & 3, t = b >> 2, tn = t % 5, tm = t / 5;
    EpiF32 e{part + (size_t)sp * S_ * DM, DM};
    gemm_tile<G2, EpiF32, true>(ctx, DHB, Bwo, DM, tm * 128, tn * 128, DM, sp * (NB / SPLITK), NB / SPLITK / 64, e);
  } else {
    const int t = b - 80 * SPLITK, tn = t % 5, tm = t / 5;
    EpiPlain e{out, 2 * DM, 1.f};
    gemm_tile<G2>(oA, DM, Awo, DM, tm * 128, tn * 128, DM, 0, DM / 64, e);
  }
}

__global__ __launch_bounds__(256) void k_reduce(const float* __restrict__ part, u16* __restrict__ out) {
  const int i = blockIdx.x * 256 + threadIdx.x;
  const int m = i / (DM / 4), n = (i % (DM / 4)) * 4;
  f32x4 s = *(const f32x4*)(part + (size_t)m * DM + n);
#pragma unroll
  for (int sp = 1; sp < SPLITK; ++sp) s += *(const f32x4*)(part + (size_t)sp * S_ * DM + (size_t)m * DM + n);
  u32x2 v = {cvtpk(s[0], s[1]), cvtpk(s[2], s[3])};
  *(u32x2*)(out + (size_t)m * 2 * DM + DM + n) = v;
}

constexpr float THR = 8.f;
DI bf16x8 pack8(const f32x16& x, int s) {
  u32x4 w = {cvtpk(x[8 * s], x[8 * s + 1]), cvtpk(x[8 * s + 2], x[8 * s + 3]), cvtpk(x[8 * s + 4], x[8 * s + 5]),
             cvtpk(x[8 * s + 6], x[8 * s + 7])};
  return __builtin_bit_cast(bf16x8, w);
}
DI f32x16 splat16(float v) {
  f32x16 x;
#pragma unroll
  for (int i = 0; i < 16; ++i) x[i] = v;
  return x;
}
DI float max16(const f32x16& x) {
  float a = fmaxf(fmaxf(x[0], x[1]), x[2]);
#pragma unroll
  for (int i = 3; i < 15; i += 2) a = fmaxf(fmaxf(a, x[i]), x[i + 1]);
  return fmaxf(a, x[15]);
}

#define SBAR() __builtin_amdgcn_sched_barrier(0)
constexpr float BIGSUM = 4096.f;
DI float sum16(const f32x16& x) {
  float a = (x[0] + x[1]) + (x[2] + x[3]), b = (x[4] + x[5]) + (x[6] + x[7]);
  float c = (x[8] + x[9]) + (x[10] + x[11]), d = (x[12] + x[13]) + (x[14] + x[15]);
  return (a + b) + (c + d);
}
DI void exp16(f32x16& x) {
#pragma unroll
  for (int i = 0; i < 16; ++i) x[i] = __builtin_amdgcn_exp2f(x[i]);
}
DI void attnB_item(const u16* __restrict__ Q, const u16* __restrict__ K, const u16* __restrict__ V,
                   u16* __restrict__ ctx, int head, int jitem) {
  const int tid = threadIdx.x, lane = tid & 63, wid = __builtin_amdgcn_readfirstlane(tid >> 6);
  const int r = lane & 31, h = lane >> 5;
  constexpr unsigned KBUF = 0, VBUF = 32768, TB = 16384, STG = 65536;
  unsigned koff, voff;
  {
    const int row = 4 * wid + (lane >> 4), c = lane & 15;
    koff = (unsigned)(row * DHB + ((c ^ (row & 15)) * 8)) * 2u;
    voff = (unsigned)(row * DHB + ((c ^ ((row & 3) << 2)) * 8)) * 2u;
  }
  const u16* Kh = K + (size_t)head * S_ * DHB;
  const u16* Vh = V + (size_t)head * S_ * DHB;
  auto stageK = [&](int buf, int t) {
#pragma unroll
    for (int i = 0; i < 4; ++i) dma16(Kh + (size_t)t * 64 * DHB + i * 16 * DHB, koff, KBUF + buf * TB + (4 * i + wid) * 1024);
  };
  auto stageV = [&](int buf, int t) {
#pragma unroll
    for (int i = 0; i < 4; ++i) dma16(Vh + (size_t)t * 64 * DHB + i * 16 * DHB, voff, VBUF + buf * TB + (4 * i + wid) * 1024);
  };
  unsigned ka[8], va[4];
#pragma unroll
  for (int ks = 0; ks < 8; ++ks) ka[ks] = KBUF + r * 256 + (((2 * ks + h) ^ (r & 15)) * 16);
  {
    const int q = (lane & 15) >> 2, p = lane & 3, cb = (lane >> 4) & 1;
#pragma unroll
    for (int db = 0; db < 4; ++db) va[db] = VBUF + (4 * h + q) * 256 + ((db ^ q) * 64) + cb * 32 + p * 8;
  }
  bf16x8 qf[8];
  f32x16 o[4], negm;
  float mcur, lsum;
  auto loadQ = [&](int qb) {
    int lz = threadIdx.x;
    asm volatile("" : "+v"(lz));
    const u16* qp = Q + ((size_t)head * S_ + qb * 128) * DHB + (unsigned)((wid * 32 + (lz & 31)) * DHB + ((lz >> 5) & 1) * 8);
#pragma unroll
    for (int ks = 0; ks < 8; ++ks) qf[ks] = *(const bf16x8*)(qp + ks * 16);
  };
  stageK(0, 0); stageK(1, 1); stageV(0, 0);
  loadQ(15 - jitem);

  auto qk_abs = [&](unsigned off) {
    bf16x8 kf[8];
#pragma unroll
    for (int ks = 0; ks < 8; ++ks) kf[ks] = lds_read128(off + ka[ks]);
    f32x16 s = MFMA32(kf[0], qf[0], f32x16{});
#pragma unroll
    for (int ks = 1; ks < 8; ++ks) s = MFMA32(kf[ks], qf[ks], s);
    return s;
  };
  auto remax = [&](const f32x16& st, bool first, f32x16& s) {
    const float tm = xhalf_max(max16(st));
    const float mnew = first ? tm : fmaxf(tm, mcur);
    const float delta = mnew - mcur;
    const float f = __builtin_amdgcn_exp2f(-fmaxf(delta, 0.f));
    mcur = mnew;
    negm = splat16(-mnew);
#pragma unroll
    for (int i = 0; i < 16; ++i) s[i] = __builtin_amdgcn_exp2f(st[i] - mnew);
#pragma unroll
    for (int db = 0; db < 4; ++db)
#pragma unroll
      for (int i = 0; i < 16; ++i) o[db][i] *= f;
    lsum *= f;
    return delta;
  };
  auto slow = [&](int blk, f32x16& s) {
    int lz = threadIdx.x;
    asm volatile("" : "+v"(lz));
    const u16* kp = Kh + (size_t)blk * 32 * DHB + (unsigned)((lz & 31) * DHB + ((lz >> 5) & 1) * 8);
    f32x16 st = f32x16{};
#pragma unroll
    for (int ks = 0; ks < 8; ++ks) {
      st = MFMA32(*(const bf16x8*)(kp + ks * 16), qf[ks], st);
      asm volatile("" ::: "memory");
      SBAR();
    }
    return remax(st, false, s);
  };
  auto pv = [&](unsigned off, const f32x16& p) {
#pragma unroll
    for (int sx = 0; sx < 2; ++sx) {
      const bf16x8 pf = pack8(p, sx);
#pragma unroll
      for (int db = 0; db < 4; ++db) {
        const bf16x8 vf = cat8(lds_read_tr(off + va[db] + (16 * sx) * 256), lds_read_tr(off + va[db] + (16 * sx + 8) * 256));
        o[db] = MFMA32(vf, pf, o[db]);
      }
    }
  };
#define EXP2(x) x = __builtin_amdgcn_exp2f(x)
#define VRD(dst, off) dst = cat8(lds_read_tr(off), lds_read_tr((off) + 2048))

#pragma unroll 1
  for (int pass = 0; pass < 2; ++pass) {
    const int qb = pass ? jitem : 15 - jitem;
    const int nfull = 2 * qb;
    auto init = [&]() {
      float z = 0.f;
      asm volatile("" : "+v"(z));
#pragma unroll
      for (int db = 0; db < 4; ++db) o[db] = splat16(z);
      mcur = z; lsum = z;
    };
#pragma unroll
    for (int ks = 0; ks < 8; ++ks) asm volatile("" : "+v"(qf[ks]));
    WAIT_VM0();
    BARRIER();
    if (nfull == 0) init();
    else {
      init();
      f32x16 sa, sb;
      bf16x8 kg[8];
      float pa;
      {
        const f32x16 st = qk_abs(0);
        const float m = xhalf_max(max16(st));
        mcur = m;
        negm = splat16(-m);
#pragma unroll
        for (int i = 0; i < 8; ++i) sa[i] = __builtin_amdgcn_exp2f(st[i] - m);
#pragma unroll
        for (int i = 8; i < 16; ++i) sa[i] = st[i] - m;
        pa = ((sa[0] + sa[1]) + (sa[2] + sa[3])) + ((sa[4] + sa[5]) + (sa[6] + sa[7]));
#pragma unroll
        for (int ks = 0; ks < 8; ++ks) kg[ks] = lds_read128(ka[ks] + 8192);
      }
      auto tile = [&](auto BUFC, auto LASTC, int t) {
        constexpr unsigned B_ = decltype(BUFC)::value;
        constexpr bool LAST = decltype(LASTC)::value;
        constexpr unsigned vo = B_ * TB, vn = TB - vo;
        constexpr unsigned kn = TB - vo, kd = vo;
        const u16* kbn = Kh + (size_t)(t + 2) * 64 * DHB;
        const u16* vbn = Vh + (size_t)(t + 1) * 64 * DHB;
#define DMAK(i) dma16(kbn + (i) * 16 * DHB, koff, KBUF + kd + (4 * (i) + wid) * 1024)
#define DMAV(i) dma16(vbn + (i) * 16 * DHB, voff, VBUF + vn + (4 * (i) + wid) * 1024)
        bf16x8 kf[8], vf[8], vg[8];
        sb = MFMA32(kg[0], qf[0], negm);
        EXP2(sa[8]); EXP2(sa[9]);
        SBAR();
        sb = MFMA32(kg[1], qf[1], sb);
        EXP2(sa[10]); EXP2(sa[11]);
        SBAR();
        sb = MFMA32(kg[2], qf[2], sb);
        DMAV(0);
        EXP2(sa[12]); EXP2(sa[13]);
        float pa2 = (sa[8] + sa[9]) + (sa[10] + sa[11]);
        SBAR();
        sb = MFMA32(kg[3], qf[3], sb);
        EXP2(sa[14]); EXP2(sa[15]);
        pa += sa[12] + sa[13];
        SBAR();
        sb = MFMA32(kg[4], qf[4], sb);
        DMAV(1);
        pa2 += sa[14] + sa[15];
        pa += pa2;
        if (__any(!(pa <= BIGSUM))) {
          const float delta = slow(2 * t, sa);
#pragma unroll
          for (int i = 0; i < 16; ++i) sb[i] -= delta;
          pa = sum16(sa);
        }
        sb = MFMA32(kg[5], qf[5], sb);
        VRD(vf[0], vo + va[0]); VRD(vf[1], vo + va[1]);
        const bf16x8 p0 = pack8(sa, 0);
        lsum += pa;
        SBAR();
        sb = MFMA32(kg[6], qf[6], sb);
        VRD(vf[2], vo + va[2]); VRD(vf[3], vo + va[3]);
        DMAV(2);
        const bf16x8 p1 = pack8(sa, 1);
        SBAR();
        sb = MFMA32(kg[7], qf[7], sb);
        VRD(vf[4], vo + va[0] + 4096); VRD(vf[5], vo + va[1] + 4096);
        SBAR();
        o[0] = MFMA32(vf[0], p0, o[0]);
        VRD(vf[6], vo + va[2] + 4096); VRD(vf[7], vo + va[3] + 4096);
        DMAV(3);
        SBAR();
        o[1] = MFMA32(vf[1], p0, o[1]);
        EXP2(sb[0]); EXP2(sb[1]); EXP2(sb[2]);
        SBAR();
        o[2] = MFMA32(vf[2], p0, o[2]);
        DMAK(0);
        EXP2(sb[3]); EXP2(sb[4]);
        SBAR();
        o[3] = MFMA32(vf[3], p0, o[3]);
        if (!LAST) { kf[0] = lds_read128(kn + ka[0]); kf[1] = lds_read128(kn + ka[1]); }
        EXP2(sb[5]); EXP2(sb[6]);
        float pb = (sb[0] + sb[1]) + (sb[2] + sb[3]);
        SBAR();
        o[0] = MFMA32(vf[4], p1, o[0]);
        DMAK(1);
        EXP2(sb[7]); EXP2(sb[8]);
        SBAR();
        o[1] = MFMA32(vf[5], p1, o[1]);
        if (!LAST) { kf[2] = lds_read128(kn + ka[2]); kf[3] = lds_read128(kn + ka[3]); }
        EXP2(sb[9]); EXP2(sb[10]);
        float pb2 = (sb[4] + sb[5]) + (sb[6] + sb[7]);
        SBAR();
        o[2] = MFMA32(vf[6], p1, o[2]);
        DMAK(2);
        if (!LAST) { kf[4] = lds_read128(kn + ka[4]); kf[5] = lds_read128(kn + ka[5]); }
        EXP2(sb[11]); EXP2(sb[12]);
        SBAR();
        o[3] = MFMA32(vf[7], p1, o[3]);
        if (!LAST) { kf[6] = lds_read128(kn + ka[6]); kf[7] = lds_read128(kn + ka[7]); }
        EXP2(sb[13]); EXP2(sb[14]);
        pb += (sb[8] + sb[9]) + sb[10];
        SBAR();
        if (!LAST) sa = MFMA32(kf[0], qf[0], negm);
        DMAK(3);
        EXP2(sb[15]);
        pb2 += (sb[11] + sb[12]) + sb[13];
        VRD(vg[0], vo + va[0] + 8192); VRD(vg[1], vo + va[1] + 8192);
        SBAR();
        if (!LAST) sa = MFMA32(kf[1], qf[1], sa);
        pb += sb[14] + sb[15];
        pb += pb2;
        VRD(vg[2], vo + va[2] + 8192); VRD(vg[3], vo + va[3] + 8192);
        if (__any(!(pb <= BIGSUM))) {
          const float delta = slow(2 * t + 1, sb);
          if (!LAST) {
#pragma unroll
            for (int i = 0; i < 16; ++i) sa[i] -= delta;
          }
          pb = sum16(sb);
        }
        if (!LAST) sa = MFMA32(kf[2], qf[2], sa);
        const bf16x8 r0 = pack8(sb, 0);
        lsum += pb;
        SBAR();
        if (!LAST) sa = MFMA32(kf[3], qf[3], sa);
        const bf16x8 r1 = pack8(sb, 1);
        SBAR();
        if (!LAST) sa = MFMA32(kf[4], qf[4], sa);
        VRD(vg[4], vo + va[0] + 12288); VRD(vg[5], vo + va[1] + 12288);
        SBAR();
        if (!LAST) sa = MFMA32(kf[5], qf[5], sa);
        VRD(vg[6], vo + va[2] + 12288); VRD(vg[7], vo + va[3] + 12288);
        SBAR();
        if (!LAST) sa = MFMA32(kf[6], qf[6], sa);
        SBAR();
        if (!LAST) sa = MFMA32(kf[7], qf[7], sa);
        SBAR();
        o[0] = MFMA32(vg[0], r0, o[0]);
        if (!LAST) { kg[0] = lds_read128(kn + ka[0] + 8192); kg[1] = lds_read128(kn + ka[1] + 8192); }
        SBAR();
        o[1] = MFMA32(vg[1], r0, o[1]);
        if (!LAST) { kg[2] = lds_read128(kn + ka[2] + 8192); kg[3] = lds_read128(kn + ka[3] + 8192); EXP2(sa[0]); EXP2(sa[1]); }
        SBAR();
        o[2] = MFMA32(vg[2], r0, o[2]);
        if (!LAST) { kg[4] = lds_read128(kn + ka[4] + 8192); kg[5] = lds_read128(kn + ka[5] + 8192); EXP2(sa[2]); EXP2(sa[3]); }
        SBAR();
        o[3] = MFMA32(vg[3], r0, o[3]);
        if (!LAST) { kg[6] = lds_read128(kn + ka[6] + 8192); kg[7] = lds_read128(kn + ka[7] + 8192); EXP2(sa[4]); EXP2(sa[5]); }
        SBAR();
        o[0] = MFMA32(vg[4], r1, o[0]);
        if (!LAST) { EXP2(sa[6]); EXP2(sa[7]); pa = (sa[0] + sa[1]) + (sa[2] + sa[3]); }
        SBAR();
        o[1] = MFMA32(vg[5], r1, o[1]);
        if (!LAST) { pa += (sa[4] + sa[5]) + (sa[6] + sa[7]); }
        SBAR();
        o[2] = MFMA32(vg[6], r1, o[2]);
        o[3] = MFMA32(vg[7], r1, o[3]);
        WAIT_VM0();
        BARRIER();
#undef DMAK
#undef DMAV
      };
      typedef std::integral_constant<unsigned, 0> C0;
      typedef std::integral_constant<unsigned, 1> C1;
      int t = 0;
      do {
        tile(C0{}, std::false_type{}, t);
        tile(C1{}, std::false_type{}, t + 1);
        t += 2;
      } while (t < nfull);
    }
    stageV(1, nfull + 1);
#pragma unroll 1
    for (int tt = 0; tt < 2; ++tt) {
#pragma unroll 1
      for (int jj = 0; jj < 2; ++jj) {
        const int j = 2 * tt + jj;
        if (j <= wid) {
          f32x16 st = qk_abs(tt * TB + jj * 8192), s;
          if (j == wid) {
            int lz = threadIdx.x;
            asm volatile("" : "+v"(lz));
            const int dq = (lz & 31) - 4 * ((lz >> 5) & 1);
#pragma unroll
            for (int i = 0; i < 16; ++i) {
              const int kr = (i & 3) + 8 * (i >> 2);
              if (kr > dq) st[i] = -__builtin_inff();
            }
          }
          remax(st, nfull == 0 && j == 0, s);
          lsum += sum16(s);
          pv(tt * TB + jj * 32 * 256, s);
        }
      }
      WAIT_VM0();
      BARRIER();
    }
    if (pass == 0) { stageK(0, 0); stageK(1, 1); stageV(0, 0); }
    const float inv = 1.f / xhalf_sum(lsum);
    {
      int lz = threadIdx.x;
      asm volatile("" : "+v"(lz));
      const int lane = lz & 63, r = lz & 31, h = (lz >> 5) & 1;
      const unsigned sb_ = STG + wid * 4096;
      u16* op = ctx + ((size_t)head * S_ + qb * 128 + wid * 32 + (lane >> 3)) * DHB + (lane & 7) * 8;
#pragma unroll
      for (int hf = 0; hf < 2; ++hf) {
#pragma unroll
        for (int d2 = 0; d2 < 2; ++d2)
#pragma unroll
          for (int g = 0; g < 4; ++g) {
            const f32x16& x = o[2 * hf + d2];
            u32x2 v = {cvtpk(x[4 * g] * inv, x[4 * g + 1] * inv), cvtpk(x[4 * g + 2] * inv, x[4 * g + 3] * inv)};
            *(lds_u32x2*)(uintptr_t)(sb_ + r * 128 + (((4 * d2 + g) ^ (r & 7)) * 16) + h * 8) = v;
          }
#pragma unroll
        for (int it = 0; it < 4; ++it) {
          const int rr = it * 8 + (lane >> 3);
          const bf16x8 v = lds_read128(sb_ + rr * 128 + (((lane & 7) ^ (rr & 7)) * 16));
          *(bf16x8*)(op + (size_t)it * 8 * DHB + hf * 64) = v;
        }
      }
    }
    if (pass == 0) loadQ(jitem);
  }
#undef EXP2
#undef VRD
}

template <int QT>
DI void attnA_block(const u16* __restrict__ qkv, u16* __restrict__ oA, int head, int qb) {
  const int tid = threadIdx.x, lane = tid & 63, wid = __builtin_amdgcn_readfirstlane(tid >> 6);
  const int r = lane & 31, h = lane >> 5;
  const int q0 = qb * (128 * QT) + wid * (32 * QT);
  constexpr unsigned KBUF = 0, VBUF = 16384, TB = 8192;
  const u16* Qh = qkv + (size_t)(0 * HA + head) * S_ * 32;
  const u16* Kh = qkv + (size_t)(1 * HA + head) * S_ * 32;
  const u16* Vh = qkv + (size_t)(2 * HA + head) * S_ * 32;
  unsigned koff[2], voff[2];
#pragma unroll
  for (int i = 0; i < 2; ++i) {
    const int row = 16 * (wid * 2 + i) + (lane >> 2), c = lane & 3;
    koff[i] = (unsigned)(row * 64 + ((c ^ ((row >> 2) & 3)) * 16));
    voff[i] = (unsigned)(row * 64 + c * 16);
  }
  auto stage = [&](int buf, int t) {
    const u16* kb = Kh + (size_t)t * 128 * 32;
    const u16* vb = Vh + (size_t)t * 128 * 32;
#pragma unroll
    for (int i = 0; i < 2; ++i) dma16(kb, koff[i], KBUF + buf * TB + (wid * 2 + i) * 1024);
#pragma unroll
    for (int i = 0; i < 2; ++i) dma16(vb, voff[i], VBUF + buf * TB + (wid * 2 + i) * 1024);
  };
  stage(0, 0);
  bf16x8 qf[QT][2];
#pragma unroll
  for (int qt = 0; qt < QT; ++qt)
#pragma unroll
    for (int ks = 0; ks < 2; ++ks) qf[qt][ks] = *(const bf16x8*)(Qh + (size_t)(q0 + 32 * qt + r) * 32 + ks * 16 + h * 8);
  unsigned ka[2], va;
#pragma unroll
  for (int ks = 0; ks < 2; ++ks) ka[ks] = KBUF + r * 64 + (((2 * ks + h) ^ ((r >> 2) & 3)) * 16);
  {
    const int q = (lane & 15) >> 2, p = lane & 3, cb = (lane >> 4) & 1;
    va = VBUF + (4 * h + q) * 64 + cb * 32 + p * 8;
  }
  f32x16 o[QT], negm[QT];
  float mcur[QT];
#pragma unroll
  for (int qt = 0; qt < QT; ++qt) { o[qt] = f32x16{}; negm[qt] = f32x16{}; mcur[qt] = 0.f; }
#pragma unroll
  for (int qt = 0; qt < QT; ++qt) { asm volatile("" : "+v"(qf[qt][0])); asm volatile("" : "+v"(qf[qt][1])); }
  WAIT_VM0();
  BARRIER();
  constexpr int NKT = S_ / 128;
  for (int t = 0; t < NKT; ++t) {
    const unsigned bo = (t & 1) * TB;
    if (t + 1 < NKT) stage((t + 1) & 1, t + 1);
    f32x16 s[QT][4];
#pragma unroll
    for (int kb = 0; kb < 4; ++kb) {
      const bf16x8 k0 = lds_read128(bo + ka[0] + kb * 2048), k1 = lds_read128(bo + ka[1] + kb * 2048);
#pragma unroll
      for (int qt = 0; qt < QT; ++qt) {
        s[qt][kb] = MFMA32(k0, qf[qt][0], negm[qt]);
        s[qt][kb] = MFMA32(k1, qf[qt][1], s[qt][kb]);
      }
    }
    float tm[QT];
    bool need = t == 0;
#pragma unroll
    for (int qt = 0; qt < QT; ++qt) {
      tm[qt] = xhalf_max(fmaxf(fmaxf(max16(s[qt][0]), max16(s[qt][1])), fmaxf(max16(s[qt][2]), max16(s[qt][3]))));
      need = need || tm[qt] > THR;
    }
    if (__any(need)) {
#pragma unroll
      for (int qt = 0; qt < QT; ++qt) {
        const float dpos = fmaxf(tm[qt], 0.f);
        const float delta = t == 0 ? tm[qt] : dpos;
        const float f = __builtin_amdgcn_exp2f(-dpos);
        mcur[qt] += delta;
        negm[qt] = splat16(-mcur[qt]);
#pragma unroll
        for (int kb = 0; kb < 4; ++kb)
#pragma unroll
          for (int i = 0; i < 16; ++i) s[qt][kb][i] -= delta;
#pragma unroll
        for (int i = 0; i < 16; ++i) o[qt][i] *= f;
      }
    }
#pragma unroll
    for (int kb = 0; kb < 4; ++kb) {
#pragma unroll
      for (int sx = 0; sx < 2; ++sx) {
        const bf16x8 vf = cat8(lds_read_tr(bo + va + (32 * kb + 16 * sx) * 64), lds_read_tr(bo + va + (32 * kb + 16 * sx + 8) * 64));
#pragma unroll
        for (int qt = 0; qt < QT; ++qt) {
#pragma unroll
          for (int i = 0; i < 8; ++i) s[qt][kb][8 * sx + i] = __builtin_amdgcn_exp2f(s[qt][kb][8 * sx + i]);
          o[qt] = MFMA32(vf, pack8(s[qt][kb], sx), o[qt]);
        }
      }
    }
    WAIT_VM0();
    BARRIER();
  }
#pragma unroll
  for (int qt = 0; qt < QT; ++qt) {
    const float l = __shfl(o[qt][8], r + 32);
    const float inv = 1.f / l;
    u16* op = oA + (size_t)(q0 + 32 * qt + r) * DM + head * DHA + 4 * h;
#pragma unroll
    for (int g = 0; g < 3; ++g) {
      if (g < 2 || h == 0) {
        u32x2 v = {cvtpk(o[qt][4 * g] * inv, o[qt][4 * g + 1] * inv), cvtpk(o[qt][4 * g + 2] * inv, o[qt][4 * g + 3] * inv)};
        *(u32x2*)(op + 8 * g) = v;
      }
    }
  }
}

constexpr int AQT = 2;
constexpr int NBLK_B = HB * 8, NBLK_A = HA * (S_ / (128 * AQT));
__global__ __launch_bounds__(256, 2) void k_attn(const u16* __restrict__ qB, const u16* __restrict__ kB,
                                                 const u16* __restrict__ vB, u16* __restrict__ ctx,
                                                 const u16* __restrict__ qkvA, u16* __restrict__ oA) {
  const int b = blockIdx.x;
  if (b < NBLK_B) {
    const int head = (b & 7) + 8 * (b >> 6), j = (b >> 3) & 7;
    attnB_item(qB, kB, vB, ctx, head, j);
  } else {
    const int a = b - NBLK_B;
    constexpr int PER = S_ / (128 * AQT);
    const int head = (a & 7) + 8 * (a / (8 * PER)), qb = (a >> 3) % PER;
    attnA_block<AQT>(qkvA, oA, head, qb);
  }
}

extern "C" void kernel_launch(void* const* d_in, const int* in_sizes, int n_in, void* d_out, int out_size, void* d_ws,
                              size_t ws_size, hipStream_t stream) {
  const u16* Ax = (const u16*)d_in[0];
  const u16* Awqkv = (const u16*)d_in[1];
  const u16* Awo = (const u16*)d_in[2];
  const u16* Bx = (const u16*)d_in[3];
  const u16* Bckv = (const u16*)d_in[4];
  const u16* Bwq = (const u16*)d_in[5];
  const u16* Bwuk = (const u16*)d_in[6];
  const u16* Bwuv = (const u16*)d_in[7];
  const u16* Bwo = (const u16*)d_in[8];
  u16* out = (u16*)d_out;
  char* ws = (char*)d_ws;
  u16* qkvA = (u16*)ws;                 ws += (size_t)3 * HA * S_ * 32 * 2;
  u16* qB = (u16*)ws;                   ws += (size_t)S_ * NB * 2;
  u16* kB = (u16*)ws;                   ws += (size_t)S_ * NB * 2;
  u16* vB = (u16*)ws;                   ws += (size_t)S_ * NB * 2;
  u16* ctx = (u16*)ws;                  ws += (size_t)S_ * NB * 2;
  u16* oA = (u16*)ws;                   ws += (size_t)S_ * DM * 2;
  float* part = (float*)ws;             ws += (size_t)SPLITK * S_ * DM * 4;

  (void)hipFuncSetAttribute((const void*)k_proj, hipFuncAttributeMaxDynamicSharedMemorySize, G1::LDS);
  (void)hipFuncSetAttribute((const void*)k_out, hipFuncAttributeMaxDynamicSharedMemorySize, G2::LDS);
  (void)hipFuncSetAttribute((const void*)k_attn, hipFuncAttributeMaxDynamicSharedMemorySize, 81920);
  k_proj<<<448, 512, G1::LDS, stream>>>(Ax, Awqkv, Bx, Bckv, Bwq, Bwuk, Bwuv, qkvA, qB, kB, vB);
  k_attn<<<NBLK_B + NBLK_A, 256, 81920, stream>>>(qB, kB, vB, ctx, qkvA, oA);
  k_out<<<80 * SPLITK + 80, 256, G2::LDS, stream>>>(oA, Awo, ctx, Bwo, out, part);
  k_reduce<<<S_ * DM / 4 / 256, 256, 0, stream>>>(part, out);
}
```

```cpp
#include <hip/hip_runtime.h>
#include <hip/hip_bf16.h>
#include <stdint.h>
#include <type_traits>

constexpr int S_ = 2048;
constexpr int DM = 640;
constexpr int HA = 32, DHA = 20;
constexpr int HB = 32, DHB = 128, DC = 512;
constexpr int NB = HB * DHB;

typedef unsigned short u16;
typedef short bf16x8 __attribute__((ext_vector_type(8)));
typedef short s16x4 __attribute__((ext_vector_type(4)));
typedef float f32x16 __attribute__((ext_vector_type(16)));
typedef float f32x4 __attribute__((ext_vector_type(4)));
typedef float f32x2 __attribute__((ext_vector_type(2)));
typedef __bf16 bf16x2 __attribute__((ext_vector_type(2)));
typedef unsigned u32x2 __attribute__((ext_vector_type(2)));
typedef unsigned u32x4 __attribute__((ext_vector_type(4)));
typedef __attribute__((address_space(3))) s16x4 lds_s16x4;
typedef __attribute__((address_space(3))) bf16x8 lds_bf16x8;
typedef __attribute__((address_space(3))) u32x2 lds_u32x2;

#define DI __device__ __forceinline__
#define MFMA32(a, b, c) __builtin_amdgcn_mfma_f32_32x32x16_bf16((a), (b), (c), 0, 0, 0)

DI unsigned cvtpk(float lo, float hi) {
  f32x2 v = {lo, hi};
  bf16x2 b = __builtin_convertvector(v, bf16x2);
  return __builtin_bit_cast(unsigned, b);
}
DI void dma16(const void* sbase, unsigned voff, unsigned lds_addr) {
  unsigned keep;
  asm volatile("s_mov_b32 %0, m0\n\ts_mov_b32 m0, %3\n\ts_nop 0\n\tglobal_load_lds_dwordx4 %1, %2\n\ts_mov_b32 m0, %0"
               : "=&s"(keep) : "v"(voff), "s"(sbase), "s"(lds_addr) : "memory");
}
DI void dma_base(unsigned lds_addr) { asm volatile("s_mov_b32 m0, %0\n\ts_nop 0" :: "s"(lds_addr) : "memory"); }
template <int OFF> DI void dma_piece(const void* sbase, unsigned voff) {
  asm volatile("global_load_lds_dwordx4 %0, %1 offset:%2" :: "v"(voff), "s"(sbase), "i"(OFF) : "memory");
}
template <int OFF, int X> DI void dma_piece_x(const void* sbase, unsigned voff) {
  unsigned t;
  asm volatile("v_xor_b32 %0, %4, %1\n\tglobal_load_lds_dwordx4 %0, %2 offset:%3" : "=&v"(t) : "v"(voff), "s"(sbase), "i"(OFF), "i"(X) : "memory");
}
#define WAIT_VM0() asm volatile("s_waitcnt vmcnt(0)" ::: "memory")
#define BARRIER() do { asm volatile("s_waitcnt lgkmcnt(0)" ::: "memory"); __builtin_amdgcn_s_barrier(); asm volatile("" ::: "memory"); } while (0)

DI bf16x8 lds_read128(unsigned off) { return *(const lds_bf16x8*)(uintptr_t)off; }
DI s16x4 lds_read_tr(unsigned off) { return __builtin_amdgcn_ds_read_tr16_b64_v4i16((lds_s16x4*)(uintptr_t)off); }
DI bf16x8 cat8(s16x4 a, s16x4 b) { return __builtin_shufflevector(a, b, 0, 1, 2, 3, 4, 5, 6, 7); }
DI float xhalf_max(float x) {
  auto r = __builtin_amdgcn_permlane32_swap(__float_as_uint(x), __float_as_uint(x), false, false);
  return fmaxf(__uint_as_float(r[0]), __uint_as_float(r[1]));
}
DI float xhalf_sum(float x) {
  auto r = __builtin_amdgcn_permlane32_swap(__float_as_uint(x), __float_as_uint(x), false, false);
  return __uint_as_float(r[0]) + __uint_as_float(r[1]);
}

template <int BM, int BN, int WM, int WN>
struct GCfg {
  static constexpr int NW = WM * WN, TM = BM / WM, TN = BN / WN, MT = TM / 32, NT = TN / 32;
  static constexpr int XB = BM * 128, WROW = BN * 2, WB = 64 * WROW, STAGE = XB + WB;
  static constexpr int XI = BM / 8 / NW;
  static constexpr int WI = WB / 1024 / NW;
  static constexpr int WCH = BN / 8;
  static constexpr int WRPI = 64 / WCH;
  static constexpr int LDS = 2 * STAGE;
};

template <class C, class Epi, bool XHEAD = false>
DI void gemm_tile(const u16* __restrict__ X, int ldx, const u16* __restrict__ W, int ldw, int m0, int n0, int ncols,
                  int kbeg, int nk, const Epi& epi) {
  const int tid = threadIdx.x, lane = tid & 63, wid = __builtin_amdgcn_readfirstlane(tid >> 6);
  const int wm = wid / C::WN_, wn = wid % C::WN_;
  const int r = lane & 31, h = lane >> 5;
  unsigned xoff[C::XI], woff[C::WI];
#pragma unroll
  for (int i = 0; i < C::XI; ++i) {
    const int row = 8 * (wid * C::XI + i) + (lane >> 3);
    xoff[i] = (unsigned)(row * ldx + (((lane & 7) ^ ((row >> 1) & 7)) * 8)) * 2u;
  }
#pragma unroll
  for (int i = 0; i < C::WI; ++i) {
    const int k = C::WRPI * (wid * C::WI + i) + lane / C::WCH;
    int col = (((lane % C::WCH) ^ ((k & 3) << 2)) * 8);
    if (n0 + col > ncols - 8) col = ncols - 8 - n0;
    woff[i] = (unsigned)(k * ldw + col) * 2u;
  }
  const u16* xs = XHEAD ? X + (size_t)m0 * ldx : X + (size_t)m0 * ldx + kbeg;
  const int kt0 = kbeg >> 6;
  auto xk = [&](int kt) { return XHEAD ? xs + (size_t)((kt + kt0) >> 1) * S_ * DHB + ((kt + kt0) & 1) * 64 : xs + kt * 64; };
  const u16* ws = W + (size_t)kbeg * ldw + n0;
  auto stage = [&](int buf, int kt) {
    const u16* xb = xk(kt);
    const u16* wb = ws + (size_t)kt * 64 * ldw;
    const unsigned lb = buf * C::STAGE;
#pragma unroll
    for (int i = 0; i < C::XI; ++i) dma16(xb, xoff[i], lb + (wid * C::XI + i) * 1024);
#pragma unroll
    for (int i = 0; i < C::WI; ++i) dma16(wb, woff[i], lb + C::XB + (wid * C::WI + i) * 1024);
  };
  unsigned xa[4];
#pragma unroll
  for (int ks = 0; ks < 4; ++ks) xa[ks] = (wm * C::TM + r) * 128 + (((2 * ks + h) ^ ((r >> 1) & 7)) * 16);
  unsigned wa[C::NT];
  {
    const int q = (lane & 15) >> 2, p = lane & 3, cb = (lane >> 4) & 1;
#pragma unroll
    for (int nt = 0; nt < C::NT; ++nt) {
      const int nb = wn * C::NT + nt;
      wa[nt] = C::XB + (8 * h + q) * C::WROW + ((nb ^ q) * 64) + cb * 32 + p * 8;
    }
  }
  f32x16 acc[C::NT][C::MT];
#pragma unroll
  for (int nt = 0; nt < C::NT; ++nt)
#pragma unroll
    for (int mt = 0; mt < C::MT; ++mt) acc[nt][mt] = f32x16{};

  stage(0, 0);
  WAIT_VM0();
  BARRIER();
  for (int kt = 0; kt < nk; ++kt) {
    const unsigned lb = (kt & 1) * C::STAGE;
    const bool more = kt + 1 < nk;
    const u16* xb = xk(kt + 1);
    const u16* wb = ws + (size_t)(kt + 1) * 64 * ldw;
    const unsigned nb = ((kt + 1) & 1) * C::STAGE;
#pragma unroll
    for (int ks = 0; ks < 4; ++ks) {
      bf16x8 xf[C::MT], wf[C::NT];
#pragma unroll
      for (int mt = 0; mt < C::MT; ++mt) xf[mt] = lds_read128(lb + xa[ks] + mt * 4096);
#pragma unroll
      for (int nt = 0; nt < C::NT; ++nt)
        wf[nt] = cat8(lds_read_tr(lb + wa[nt] + (16 * ks) * C::WROW), lds_read_tr(lb + wa[nt] + (16 * ks + 4) * C::WROW));
      if (more) {
        constexpr int TOT = C::XI + C::WI;
#pragma unroll
        for (int i = ks * TOT / 4; i < (ks + 1) * TOT / 4; ++i) {
          if (i < C::XI) dma16(xb, xoff[i], nb + (wid * C::XI + i) * 1024);
          else dma16(wb, woff[i - C::XI], nb + C::XB + (wid * C::WI + i - C::XI) * 1024);
        }
      }
#pragma unroll
      for (int nt = 0; nt < C::NT; ++nt)
#pragma unroll
        for (int mt = 0; mt < C::MT; ++mt) acc[nt][mt] = MFMA32(wf[nt], xf[mt], acc[nt][mt]);
    }
    WAIT_VM0();
    BARRIER();
  }
  if constexpr (Epi::DIRECT) {
    f32x4* dst = (f32x4*)epi.out + (size_t)wid * (C::NT * C::MT * 4 * 64) + lane;
#pragma unroll
    for (int nt = 0; nt < C::NT; ++nt)
#pragma unroll
      for (int mt = 0; mt < C::MT; ++mt)
#pragma unroll
        for (int g = 0; g < 4; ++g) {
          f32x4 v = {acc[nt][mt][4 * g], acc[nt][mt][4 * g + 1], acc[nt][mt][4 * g + 2], acc[nt][mt][4 * g + 3]};
          dst[((nt * C::MT + mt) * 4 + g) * 64] = v;
        }
  } else {
    static_assert(C::TN == 64, "staging layout");
    const unsigned sb = wid * (C::TM * 128);
#pragma unroll
    for (int nt = 0; nt < C::NT; ++nt)
#pragma unroll
      for (int g = 0; g < 4; ++g) {
        const int nl = nt * 32 + 8 * g + 4 * h;
        const float sc = epi.scale_of(n0 + wn * C::TN + nl);
        const unsigned a = sb + r * 128 + (((nl >> 3) ^ (r & 7)) * 16) + (nl & 4) * 2;
#pragma unroll
        for (int mt = 0; mt < C::MT; ++mt) {
          u32x2 v = {cvtpk(acc[nt][mt][4 * g] * sc, acc[nt][mt][4 * g + 1] * sc),
                     cvtpk(acc[nt][mt][4 * g + 2] * sc, acc[nt][mt][4 * g + 3] * sc)};
          *(lds_u32x2*)(uintptr_t)(a + mt * 4096) = v;
        }
      }
    epi.template store<C::TM>(sb, m0 + wm * C::TM, n0 + wn * C::TN, lane);
  }
}

template <int BM, int BN, int WM, int WN>
struct GC : GCfg<BM, BN, WM, WN> { static constexpr int WN_ = WN; };

struct EpiPlain {
  static constexpr bool DIRECT = false;
  u16* out; int ld; float scale;
  DI float scale_of(int) const { return scale; }
  template <int TM> DI void store(unsigned sb, int mb, int nb, int lane) const {
    const int rr = lane >> 3, c = lane & 7;
    u16* dst = out + (size_t)(mb + rr) * ld + nb + c * 8;
    const unsigned a = sb + rr * 128 + ((c ^ (rr & 7)) * 16);
#pragma unroll
    for (int it = 0; it < TM / 8; ++it) {
      const bf16x8 v = lds_read128(a + it * 1024);
      *(bf16x8*)(dst + (size_t)it * 8 * ld) = v;
    }
  }
};
struct EpiHead {
  static constexpr bool DIRECT = false;
  u16* out; float scale;
  DI float scale_of(int) const { return scale; }
  template <int TM> DI void store(unsigned sb, int mb, int nb, int lane) const {
    const int rr = lane >> 3, c = lane & 7;
    u16* dst = out + ((size_t)(nb >> 7) * S_ + mb + rr) * DHB + (nb & 127) + c * 8;
    const unsigned a = sb + rr * 128 + ((c ^ (rr & 7)) * 16);
#pragma unroll
    for (int it = 0; it < TM / 8; ++it) {
      const bf16x8 v = lds_read128(a + it * 1024);
      *(bf16x8*)(dst + (size_t)it * 8 * DHB) = v;
    }
  }
};
struct EpiQKVA {
  static constexpr bool DIRECT = false;
  u16* out; float qscale;
  DI float scale_of(int n) const { return n < DM ? qscale : 1.f; }
  template <int TM> DI void store(unsigned sb, int mb, int nb, int lane) const {
    const int rr = lane >> 4, gq = lane & 15;
    const int n = nb + gq * 4;
    if (n >= 3 * DM) return;
    const int which = n / DM, rem = n - which * DM, head = rem / DHA, dd = rem - head * DHA;
    u16* dst = out + ((size_t)((which * HA + head) * S_ + mb + rr)) * 32;
#pragma unroll 4
    for (int it = 0; it < TM / 4; ++it) {
      const int row = it * 4 + rr;
      const u32x2 v = *(const lds_u32x2*)(uintptr_t)(sb + row * 128 + (((gq >> 1) ^ (row & 7)) * 16) + (gq & 1) * 8);
      u16* d = dst + it * 4 * 32;
      *(u32x2*)(d + dd) = v;
      if (dd == 16) {
        u32x2 z0 = {which == 2 ? 0x3F80u : 0u, 0u};
        u32x4 z1 = {0u, 0u, 0u, 0u};
        *(u32x2*)(d + 20) = z0;
        *(u32x4*)(d + 24) = z1;
      }
    }
  }
};
struct EpiF32 {
  static constexpr bool DIRECT = true;
  float* out;
};

typedef GC<256, 256, 2, 4> G1;
typedef GC<256, 128, 4, 2> G2;

constexpr float LOG2E = 1.4426950408889634f;

__global__ __launch_bounds__(512, 2) void k_proj(const u16* __restrict__ Ax, const u16* __restrict__ Awqkv,
                                                 const u16* __restrict__ Bx, const u16* __restrict__ Bckv,
                                                 const u16* __restrict__ Bwq, const u16* __restrict__ Bwuk,
                                                 const u16* __restrict__ Bwuv, u16* __restrict__ qkvA,
                                                 u16* __restrict__ qB, u16* __restrict__ kB, u16* __restrict__ vB) {
  const int b = blockIdx.x, xcd = b & 7, idx = b >> 3;
  const int tn = xcd + 8 * (idx >> 3), tm = idx & 7;
  const int m0 = tm * 256;
  if (tn < 8) {
    EpiQKVA e{qkvA, 0.22360679774997896f * LOG2E};
    gemm_tile<G1>(Ax, DM, Awqkv, 3 * DM, m0, tn * 256, 3 * DM, 0, DM / 64, e);
  } else if (tn < 24) {
    EpiHead e{qB, 0.08838834764831845f * LOG2E};
    gemm_tile<G1>(Bx, DM, Bwq, NB, m0, (tn - 8) * 256, NB, 0, DM / 64, e);
  } else {
    const bool isv = tn >= 40;
    EpiHead e{isv ? vB : kB, 1.f};
    gemm_tile<G1>(Bckv, DC, isv ? Bwuv : Bwuk, NB, m0, (tn - (isv ? 40 : 24)) * 256, NB, 0, DC / 64, e);
  }
}

constexpr int SPLITK = 5;
__global__ __launch_bounds__(512, 2) void k_out(const u16* __restrict__ oA, const u16* __restrict__ Awo,
                                                const u16* __restrict__ ctx, const u16* __restrict__ Bwo,
                                                u16* __restrict__ out, float* __restrict__ part) {
  const int b = blockIdx.x;
  if (b < 40 * SPLITK) {
    const int tm = b & 7, rest = b >> 3, tn = rest % 5, sp = rest / 5;
    EpiF32 e{part + ((size_t)sp * 40 + tm * 5 + tn) * (256 * 128)};
    gemm_tile<G2, EpiF32, true>(ctx, DHB, Bwo, DM, tm * 256, tn * 128, DM, sp * 13 * 64, sp == 4 ? 12 : 13, e);
  } else {
    const int t = b - 40 * SPLITK, tm = t & 7, tn = t >> 3;
    EpiPlain e{out, 2 * DM, 1.f};
    gemm_tile<G2>(oA, DM, Awo, DM, tm * 256, tn * 128, DM, 0, DM / 64, e);
  }
}

__global__ __launch_bounds__(256) void k_reduce(const float* __restrict__ part, u16* __restrict__ out) {
  const int i = blockIdx.x * 256 + threadIdx.x;
  const int tile = i >> 13, w = (i >> 10) & 7, j = (i >> 6) & 15, lane = i & 63;
  const int nt = j >> 3, mt = (j >> 2) & 1, g = j & 3;
  const int tm = tile / 5, tn = tile - tm * 5;
  const int m = tm * 256 + (w >> 1) * 64 + mt * 32 + (lane & 31);
  const int n = tn * 128 + (w & 1) * 64 + nt * 32 + 8 * g + 4 * (lane >> 5);
  const f32x4* p = (const f32x4*)part + i;
  f32x4 s = p[0];
#pragma unroll
  for (int sp = 1; sp < SPLITK; ++sp) s += p[(size_t)sp * 40 * 8192];
  u32x2 v = {cvtpk(s[0], s[1]), cvtpk(s[2], s[3])};
  *(u32x2*)(out + (size_t)m * 2 * DM + DM + n) = v;
}

constexpr float THR = 8.f;
DI bf16x8 pack8(const f32x16& x, int s) {
  u32x4 w = {cvtpk(x[8 * s], x[8 * s + 1]), cvtpk(x[8 * s + 2], x[8 * s + 3]), cvtpk(x[8 * s + 4], x[8 * s + 5]),
             cvtpk(x[8 * s + 6], x[8 * s + 7])};
  return __builtin_bit_cast(bf16x8, w);
}
DI f32x16 splat16(float v) {
  f32x16 x;
#pragma unroll
  for (int i = 0; i < 16; ++i) x[i] = v;
  return x;
}
DI float max16(const f32x16& x) {
  float a = fmaxf(fmaxf(x[0], x[1]), x[2]);
#pragma unroll
  for (int i = 3; i < 15; i += 2) a = fmaxf(fmaxf(a, x[i]), x[i + 1]);
  return fmaxf(a, x[15]);
}

#define SBAR() __builtin_amdgcn_sched_barrier(0)
constexpr float BIGSUM = 4096.f;
DI float sum16(const f32x16& x) {
  float a = (x[0] + x[1]) + (x[2] + x[3]), b = (x[4] + x[5]) + (x[6] + x[7]);
  float c = (x[8] + x[9]) + (x[10] + x[11]), d = (x[12] + x[13]) + (x[14] + x[15]);
  return (a + b) + (c + d);
}
DI void exp16(f32x16& x) {
#pragma unroll
  for (int i = 0; i < 16; ++i) x[i] = __builtin_amdgcn_exp2f(x[i]);
}
DI void attnB_item(const u16* __restrict__ Q, const u16* __restrict__ K, const u16* __restrict__ V,
                   u16* __restrict__ ctx, int head, int jitem) {
  const int tid = threadIdx.x, lane = tid & 63, wid = __builtin_amdgcn_readfirstlane(tid >> 6);
  const int r = lane & 31, h = lane >> 5;
  constexpr unsigned KBUF = 0, VBUF = 32768, TB = 16384, STG = 65536;
  unsigned koff, voff;
  {
    const int l4 = lane >> 4, c = lane & 15;
    koff = (unsigned)((16 * wid + l4) * DHB + ((c ^ l4) * 8)) * 2u;
    voff = (unsigned)((16 * wid + l4) * DHB + ((c ^ (l4 << 2)) * 8)) * 2u;
  }
  const u16* Kh = K + (size_t)head * S_ * DHB;
  const u16* Vh = V + (size_t)head * S_ * DHB;
  auto stageK = [&](int buf, int t) {
    const u16* g = Kh + (size_t)t * 64 * DHB;
    dma_base(KBUF + buf * TB + wid * 4096);
    dma_piece<0>(g, koff); dma_piece_x<1024, 64>(g, koff); dma_piece_x<2048, 128>(g, koff); dma_piece_x<3072, 192>(g, koff);
  };
  auto stageV = [&](int buf, int t) {
    const u16* g = Vh + (size_t)t * 64 * DHB;
    dma_base(VBUF + buf * TB + wid * 4096);
    dma_piece<0>(g, voff); dma_piece<1024>(g, voff); dma_piece<2048>(g, voff); dma_piece<3072>(g, voff);
  };
  unsigned ka[8], va[4];
#pragma unroll
  for (int ks = 0; ks < 8; ++ks) {
    ka[ks] = KBUF + r * 256 + (((2 * ks + h) ^ (r & 15)) * 16);
    asm volatile("" : "+v"(ka[ks]));
  }
  {
    const int q = (lane & 15) >> 2, p = lane & 3, cb = (lane >> 4) & 1;
#pragma unroll
    for (int db = 0; db < 4; ++db) va[db] = VBUF + (4 * h + q) * 256 + ((db ^ q) * 64) + cb * 32 + p * 8;
  }
  bf16x8 qf[8];
  f32x16 o[4], negm;
  float mcur, lsum;
  auto loadQ = [&](int qb) {
    int lz = threadIdx.x;
    asm volatile("" : "+v"(lz));
    const u16* qp = Q + ((size_t)head * S_ + qb * 128) * DHB + (unsigned)((wid * 32 + (lz & 31)) * DHB + ((lz >> 5) & 1) * 8);
#pragma unroll
    for (int ks = 0; ks < 8; ++ks) qf[ks] = *(const bf16x8*)(qp + ks * 16);
  };
  stageK(0, 0); stageK(1, 1); stageV(0, 0);
  loadQ(15 - jitem);

  auto qk_abs = [&](unsigned off) {
    bf16x8 kf[8];
#pragma unroll
    for (int ks = 0; ks < 8; ++ks) kf[ks] = lds_read128(off + ka[ks]);
    f32x16 s = MFMA32(kf[0], qf[0], f32x16{});
#pragma unroll
    for (int ks = 1; ks < 8; ++ks) s = MFMA32(kf[ks], qf[ks], s);
    return s;
  };
  auto remax = [&](const f32x16& st, bool first, f32x16& s) {
    const float tm = xhalf_max(max16(st));
    const float mnew = first ? tm : fmaxf(tm, mcur);
    const float delta = mnew - mcur;
    const float f = __builtin_amdgcn_exp2f(-fmaxf(delta, 0.f));
    mcur = mnew;
    negm = splat16(-mnew);
#pragma unroll
    for (int i = 0; i < 16; ++i) s[i] = __builtin_amdgcn_exp2f(st[i] - mnew);
#pragma unroll
    for (int db = 0; db < 4; ++db)
#pragma unroll
      for (int i = 0; i < 16; ++i) o[db][i] *= f;
    lsum *= f;
    return delta;
  };
  auto slow = [&](int blk, f32x16& s) {
    int lz = threadIdx.x;
    asm volatile("" : "+v"(lz));
    const u16* kp = Kh + (size_t)blk * 32 * DHB + (unsigned)((lz & 31) * DHB + ((lz >> 5) & 1) * 8);
    f32x16 st = f32x16{};
#pragma unroll
    for (int ks = 0; ks < 8; ++ks) {
      st = MFMA32(*(const bf16x8*)(kp + ks * 16), qf[ks], st);
      asm volatile("" ::: "memory");
      SBAR();
    }
    return remax(st, false, s);
  };
  auto pv = [&](unsigned off, const f32x16& p) {
#pragma unroll
    for (int sx = 0; sx < 2; ++sx) {
      const bf16x8 pf = pack8(p, sx);
#pragma unroll
      for (int db = 0; db < 4; ++db) {
        const bf16x8 vf = cat8(lds_read_tr(off + va[db] + (16 * sx) * 256), lds_read_tr(off + va[db] + (16 * sx + 8) * 256));
        o[db] = MFMA32(vf, pf, o[db]);
      }
    }
  };
#define EXP2(x) x = __builtin_amdgcn_exp2f(x)
#define VRD(dst, off) dst = cat8(lds_read_tr(off), lds_read_tr((off) + 2048))

#pragma unroll 1
  for (int pass = 0; pass < 2; ++pass) {
    const int qb = pass ? jitem : 15 - jitem;
    const int nfull = 2 * qb;
    auto init = [&]() {
      float z = 0.f;
      asm volatile("" : "+v"(z));
#pragma unroll
      for (int db = 0; db < 4; ++db) o[db] = splat16(z);
      mcur = z; lsum = z;
    };
#pragma unroll
    for (int ks = 0; ks < 8; ++ks) asm volatile("" : "+v"(qf[ks]));
    WAIT_VM0();
    BARRIER();
    if (nfull == 0) init();
    else {
      init();
      f32x16 sa, sb;
      bf16x8 kg[8];
      float pa;
      {
        const f32x16 st = qk_abs(0);
        const float m = xhalf_max(max16(st));
        mcur = m;
        negm = splat16(-m);
#pragma unroll
        for (int i = 0; i < 8; ++i) sa[i] = __builtin_amdgcn_exp2f(st[i] - m);
#pragma unroll
        for (int i = 8; i < 16; ++i) sa[i] = st[i] - m;
        pa = ((sa[0] + sa[1]) + (sa[2] + sa[3])) + ((sa[4] + sa[5]) + (sa[6] + sa[7]));
#pragma unroll
        for (int ks = 0; ks < 8; ++ks) kg[ks] = lds_read128(ka[ks] + 8192);
      }
      auto tile = [&](auto BUFC, auto LASTC, int t) {
        constexpr unsigned B_ = decltype(BUFC)::value;
        constexpr bool LAST = decltype(LASTC)::value;
        constexpr unsigned vo = B_ * TB, vn = TB - vo;
        constexpr unsigned kn = TB - vo, kd = vo;
        const u16* kbn = Kh + (size_t)(t + 2) * 64 * DHB;
        const u16* vbn = Vh + (size_t)(t + 1) * 64 * DHB;
#define DMAK(i) do { if ((i) == 0) { dma_base(KBUF + kd + wid * 4096); dma_piece<0>(kbn, koff); } else dma_piece_x<(i) * 1024, (i) * 64>(kbn, koff); } while (0)
#define DMAV(i) do { if ((i) == 0) dma_base(VBUF + vn + wid * 4096); dma_piece<(i) * 1024>(vbn, voff); } while (0)
        bf16x8 kf[8], vf[8], vg[8];
#define KF(i) kf[i] = lds_read128(kn + ka[i])
#define KG(i) kg[i] = lds_read128(kn + ka[i] + 8192)
        sb = MFMA32(kg[0], qf[0], negm);
        VRD(vf[0], vo + va[0]); DMAV(0);
        EXP2(sa[8]); EXP2(sa[9]);
        SBAR();
        sb = MFMA32(kg[1], qf[1], sb);
        VRD(vf[1], vo + va[1]); DMAV(1);
        EXP2(sa[10]); EXP2(sa[11]);
        SBAR();
        sb = MFMA32(kg[2], qf[2], sb);
        VRD(vf[2], vo + va[2]); DMAV(2);
        EXP2(sa[12]); EXP2(sa[13]);
        float pa2 = (sa[8] + sa[9]) + (sa[10] + sa[11]);
        SBAR();
        sb = MFMA32(kg[3], qf[3], sb);
        VRD(vf[3], vo + va[3]); DMAV(3);
        EXP2(sa[14]); EXP2(sa[15]);
        pa += sa[12] + sa[13];
        SBAR();
        sb = MFMA32(kg[4], qf[4], sb);
        DMAK(0);
        pa2 += sa[14] + sa[15];
        pa += pa2;
        if (__any(!(pa <= BIGSUM))) {
          const float delta = slow(2 * t, sa);
#pragma unroll
          for (int i = 0; i < 16; ++i) sb[i] -= delta;
          pa = sum16(sa);
        }
        sb = MFMA32(kg[5], qf[5], sb);
        VRD(vf[4], vo + va[0] + 4096); VRD(vf[5], vo + va[1] + 4096);
        const bf16x8 p0 = pack8(sa, 0);
        lsum += pa;
        SBAR();
        sb = MFMA32(kg[6], qf[6], sb);
        VRD(vf[6], vo + va[2] + 4096); DMAK(1);
        const bf16x8 p1 = pack8(sa, 1);
        SBAR();
        sb = MFMA32(kg[7], qf[7], sb);
        VRD(vf[7], vo + va[3] + 4096); DMAK(2);
        SBAR();
        o[0] = MFMA32(vf[0], p0, o[0]);
        KF(0); DMAK(3);
        SBAR();
        o[1] = MFMA32(vf[1], p0, o[1]);
        KF(1);
        EXP2(sb[0]); EXP2(sb[1]); EXP2(sb[2]);
        SBAR();
        o[2] = MFMA32(vf[2], p0, o[2]);
        KF(2);
        EXP2(sb[3]); EXP2(sb[4]);
        SBAR();
        o[3] = MFMA32(vf[3], p0, o[3]);
        KF(3);
        EXP2(sb[5]); EXP2(sb[6]);
        float pb = (sb[0] + sb[1]) + (sb[2] + sb[3]);
        SBAR();
        o[0] = MFMA32(vf[4], p1, o[0]);
        KF(4);
        EXP2(sb[7]); EXP2(sb[8]);
        SBAR();
        o[1] = MFMA32(vf[5], p1, o[1]);
        KF(5);
        EXP2(sb[9]); EXP2(sb[10]);
        float pb2 = (sb[4] + sb[5]) + (sb[6] + sb[7]);
        SBAR();
        o[2] = MFMA32(vf[6], p1, o[2]);
        KF(6);
        EXP2(sb[11]); EXP2(sb[12]);
        SBAR();
        o[3] = MFMA32(vf[7], p1, o[3]);
        KF(7);
        EXP2(sb[13]); EXP2(sb[14]);
        pb += (sb[8] + sb[9]) + sb[10];
        SBAR();
        sa = MFMA32(kf[0], qf[0], negm);
        VRD(vg[0], vo + va[0] + 8192);
        EXP2(sb[15]);
        pb2 += (sb[11] + sb[12]) + sb[13];
        SBAR();
        sa = MFMA32(kf[1], qf[1], sa);
        VRD(vg[1], vo + va[1] + 8192);
        pb += sb[14] + sb[15];
        pb += pb2;
        if (__any(!(pb <= BIGSUM))) {
          const float delta = slow(2 * t + 1, sb);
#pragma unroll
          for (int i = 0; i < 16; ++i) sa[i] -= delta;
          pb = sum16(sb);
        }
        sa = MFMA32(kf[2], qf[2], sa);
        VRD(vg[2], vo + va[2] + 8192);
        const bf16x8 r0 = pack8(sb, 0);
        lsum += pb;
        SBAR();
        sa = MFMA32(kf[3], qf[3], sa);
        VRD(vg[3], vo + va[3] + 8192);
        const bf16x8 r1 = pack8(sb, 1);
        SBAR();
        sa = MFMA32(kf[4], qf[4], sa);
        VRD(vg[4], vo + va[0] + 12288);
        SBAR();
        sa = MFMA32(kf[5], qf[5], sa);
        VRD(vg[5], vo + va[1] + 12288);
        SBAR();
        sa = MFMA32(kf[6], qf[6], sa);
        VRD(vg[6], vo + va[2] + 12288);
        SBAR();
        sa = MFMA32(kf[7], qf[7], sa);
        VRD(vg[7], vo + va[3] + 12288);
        SBAR();
        o[0] = MFMA32(vg[0], r0, o[0]);
        KG(0);
        SBAR();
        o[1] = MFMA32(vg[1], r0, o[1]);
        KG(1); EXP2(sa[0]); EXP2(sa[1]);
        SBAR();
        o[2] = MFMA32(vg[2], r0, o[2]);
        KG(2); EXP2(sa[2]); EXP2(sa[3]);
        SBAR();
        o[3] = MFMA32(vg[3], r0, o[3]);
        KG(3); EXP2(sa[4]); EXP2(sa[5]);
        SBAR();
        o[0] = MFMA32(vg[4], r1, o[0]);
        KG(4); EXP2(sa[6]); EXP2(sa[7]);
        pa = (sa[0] + sa[1]) + (sa[2] + sa[3]);
        SBAR();
        o[1] = MFMA32(vg[5], r1, o[1]);
        KG(5);
        pa += (sa[4] + sa[5]) + (sa[6] + sa[7]);
        SBAR();
        o[2] = MFMA32(vg[6], r1, o[2]);
        KG(6); KG(7);
        SBAR();
        o[3] = MFMA32(vg[7], r1, o[3]);
#undef KF
#undef KG
        WAIT_VM0();
        BARRIER();
#undef DMAK
#undef DMAV
      };
      typedef std::integral_constant<unsigned, 0> C0;
      typedef std::integral_constant<unsigned, 1> C1;
      int t = 0;
      do {
        tile(C0{}, std::false_type{}, t);
        tile(C1{}, std::false_type{}, t + 1);
        t += 2;
      } while (t < nfull);
    }
    stageV(1, nfull + 1);
#pragma unroll 1
    for (int tt = 0; tt < 2; ++tt) {
#pragma unroll 1
      for (int jj = 0; jj < 2; ++jj) {
        const int j = 2 * tt + jj;
        if (j <= wid) {
          f32x16 st = qk_abs(tt * TB + jj * 8192), s;
          if (j == wid) {
            int lz = threadIdx.x;
            asm volatile("" : "+v"(lz));
            const int dq = (lz & 31) - 4 * ((lz >> 5) & 1);
#pragma unroll
            for (int i = 0; i < 16; ++i) {
              const int kr = (i & 3) + 8 * (i >> 2);
              if (kr > dq) st[i] = -__builtin_inff();
            }
          }
          remax(st, nfull == 0 && j == 0, s);
          lsum += sum16(s);
          pv(tt * TB + jj * 32 * 256, s);
        }
      }
      WAIT_VM0();
      BARRIER();
    }
    if (pass == 0) { stageK(0, 0); stageK(1, 1); stageV(0, 0); }
    const float inv = 1.f / xhalf_sum(lsum);
    {
      int lz = threadIdx.x;
      asm volatile("" : "+v"(lz));
      const int lane = lz & 63, r = lz & 31, h = (lz >> 5) & 1;
      const unsigned sb_ = STG + wid * 4096;
      u16* op = ctx + ((size_t)head * S_ + qb * 128 + wid * 32 + (lane >> 3)) * DHB + (lane & 7) * 8;
#pragma unroll
      for (int hf = 0; hf < 2; ++hf) {
#pragma unroll
        for (int d2 = 0; d2 < 2; ++d2)
#pragma unroll
          for (int g = 0; g < 4; ++g) {
            const f32x16& x = o[2 * hf + d2];
            u32x2 v = {cvtpk(x[4 * g] * inv, x[4 * g + 1] * inv), cvtpk(x[4 * g + 2] * inv, x[4 * g + 3] * inv)};
            *(lds_u32x2*)(uintptr_t)(sb_ + r * 128 + (((4 * d2 + g) ^ (r & 7)) * 16) + h * 8) = v;
          }
#pragma unroll
        for (int it = 0; it < 4; ++it) {
          const int rr = it * 8 + (lane >> 3);
          const bf16x8 v = lds_read128(sb_ + rr * 128 + (((lane & 7) ^ (rr & 7)) * 16));
          *(bf16x8*)(op + (size_t)it * 8 * DHB + hf * 64) = v;
        }
      }
    }
    if (pass == 0) loadQ(jitem);
  }
#undef EXP2
#undef VRD
}

template <int QT>
DI void attnA_block(const u16* __restrict__ qkv, u16* __restrict__ oA, int head, int qb) {
  const int tid = threadIdx.x, lane = tid & 63, wid = __builtin_amdgcn_readfirstlane(tid >> 6);
  const int r = lane & 31, h = lane >> 5;
  const int q0 = qb * (128 * QT) + wid * (32 * QT);
  constexpr unsigned KBUF = 0, VBUF = 16384, TB = 8192;
  const u16* Qh = qkv + (size_t)(0 * HA + head) * S_ * 32;
  const u16* Kh = qkv + (size_t)(1 * HA + head) * S_ * 32;
  const u16* Vh = qkv + (size_t)(2 * HA + head) * S_ * 32;
  unsigned koff[2], voff[2];
#pragma unroll
  for (int i = 0; i < 2; ++i) {
    const int row = 16 * (wid * 2 + i) + (lane >> 2), c = lane & 3;
    koff[i] = (unsigned)(row * 64 + ((c ^ ((row >> 2) & 3)) * 16));
    voff[i] = (unsigned)(row * 64 + c * 16);
  }
  auto stage = [&](int buf, int t) {
    const u16* kb = Kh + (size_t)t * 128 * 32;
    const u16* vb = Vh + (size_t)t * 128 * 32;
#pragma unroll
    for (int i = 0; i < 2; ++i) dma16(kb, koff[i], KBUF + buf * TB + (wid * 2 + i) * 1024);
#pragma unroll
    for (int i = 0; i < 2; ++i) dma16(vb, voff[i], VBUF + buf * TB + (wid * 2 + i) * 1024);
  };
  stage(0, 0);
  bf16x8 qf[QT][2];
#pragma unroll
  for (int qt = 0; qt < QT; ++qt)
#pragma unroll
    for (int ks = 0; ks < 2; ++ks) qf[qt][ks] = *(const bf16x8*)(Qh + (size_t)(q0 + 32 * qt + r) * 32 + ks * 16 + h * 8);
  unsigned ka[2], va;
#pragma unroll
  for (int ks = 0; ks < 2; ++ks) ka[ks] = KBUF + r * 64 + (((2 * ks + h) ^ ((r >> 2) & 3)) * 16);
  {
    const int q = (lane & 15) >> 2, p = lane & 3, cb = (lane >> 4) & 1;
    va = VBUF + (4 * h + q) * 64 + cb * 32 + p * 8;
  }
  f32x16 o[QT], negm[QT];
  float mcur[QT];
#pragma unroll
  for (int qt = 0; qt < QT; ++qt) { o[qt] = f32x16{}; negm[qt] = f32x16{}; mcur[qt] = 0.f; }
#pragma unroll
  for (int qt = 0; qt < QT; ++qt) { asm volatile("" : "+v"(qf[qt][0])); asm volatile("" : "+v"(qf[qt][1])); }
  WAIT_VM0();
  BARRIER();
  constexpr int NKT = S_ / 128;
  for (int t = 0; t < NKT; ++t) {
    const unsigned bo = (t & 1) * TB;
    if (t + 1 < NKT) stage((t + 1) & 1, t + 1);
    f32x16 s[QT][4];
#pragma unroll
    for (int kb = 0; kb < 4; ++kb) {
      const bf16x8 k0 = lds_read128(bo + ka[0] + kb * 2048), k1 = lds_read128(bo + ka[1] + kb * 2048);
#pragma unroll
      for (int qt = 0; qt < QT; ++qt) {
        s[qt][kb] = MFMA32(k0, qf[qt][0], negm[qt]);
        s[qt][kb] = MFMA32(k1, qf[qt][1], s[qt][kb]);
      }
    }
    float tm[QT];
    bool need = t == 0;
#pragma unroll
    for (int qt = 0; qt < QT; ++qt) {
      tm[qt] = xhalf_max(fmaxf(fmaxf(max16(s[qt][0]), max16(s[qt][1])), fmaxf(max16(s[qt][2]), max16(s[qt][3]))));
      need = need || tm[qt] > THR;
    }
    if (__any(need)) {
#pragma unroll
      for (int qt = 0; qt < QT; ++qt) {
        const float dpos = fmaxf(tm[qt], 0.f);
        const float delta = t == 0 ? tm[qt] : dpos;
        const float f = __builtin_amdgcn_exp2f(-dpos);
        mcur[qt] += delta;
        negm[qt] = splat16(-mcur[qt]);
#pragma unroll
        for (int kb = 0; kb < 4; ++kb)
#pragma unroll
          for (int i = 0; i < 16; ++i) s[qt][kb][i] -= delta;
#pragma unroll
        for (int i = 0; i < 16; ++i) o[qt][i] *= f;
      }
    }
#pragma unroll
    for (int kb = 0; kb < 4; ++kb) {
#pragma unroll
      for (int sx = 0; sx < 2; ++sx) {
        const bf16x8 vf = cat8(lds_read_tr(bo + va + (32 * kb + 16 * sx) * 64), lds_read_tr(bo + va + (32 * kb + 16 * sx + 8) * 64));
#pragma unroll
        for (int qt = 0; qt < QT; ++qt) {
#pragma unroll
          for (int i = 0; i < 8; ++i) s[qt][kb][8 * sx + i] = __builtin_amdgcn_exp2f(s[qt][kb][8 * sx + i]);
          o[qt] = MFMA32(vf, pack8(s[qt][kb], sx), o[qt]);
        }
      }
    }
    WAIT_VM0();
    BARRIER();
  }
#pragma unroll
  for (int qt = 0; qt < QT; ++qt) {
    const float l = __shfl(o[qt][8], r + 32);
    const float inv = 1.f / l;
    u16* op = oA + (size_t)(q0 + 32 * qt + r) * DM + head * DHA + 4 * h;
#pragma unroll
    for (int g = 0; g < 3; ++g) {
      if (g < 2 || h == 0) {
        u32x2 v = {cvtpk(o[qt][4 * g] * inv, o[qt][4 * g + 1] * inv), cvtpk(o[qt][4 * g + 2] * inv, o[qt][4 * g + 3] * inv)};
        *(u32x2*)(op + 8 * g) = v;
      }
    }
  }
}

constexpr int AQT = 2;
constexpr int NBLK_B = HB * 8, NBLK_A = HA * (S_ / (128 * AQT));
__global__ __launch_bounds__(256, 2) void k_attn(const u16* __restrict__ qB, const u16* __restrict__ kB,
                                                 const u16* __restrict__ vB, u16* __restrict__ ctx,
                                                 const u16* __restrict__ qkvA, u16* __restrict__ oA) {
  const int b = blockIdx.x;
  if (b < NBLK_B) {
    const int head = (b & 7) + 8 * (b >> 6), j = (b >> 3) & 7;
    attnB_item(qB, kB, vB, ctx, head, j);
  } else {
    const int a = b - NBLK_B;
    constexpr int PER = S_ / (128 * AQT);
    const int head = (a & 7) + 8 * (a / (8 * PER)), qb = (a >> 3) % PER;
    attnA_block<AQT>(qkvA, oA, head, qb);
  }
}

extern "C" void kernel_launch(void* const* d_in, const int* in_sizes, int n_in, void* d_out, int out_size, void* d_ws,
                              size_t ws_size, hipStream_t stream) {
  const u16* Ax = (const u16*)d_in[0];
  const u16* Awqkv = (const u16*)d_in[1];
  const u16* Awo = (const u16*)d_in[2];
  const u16* Bx = (const u16*)d_in[3];
  const u16* Bckv = (const u16*)d_in[4];
  const u16* Bwq = (const u16*)d_in[5];
  const u16* Bwuk = (const u16*)d_in[6];
  const u16* Bwuv = (const u16*)d_in[7];
  const u16* Bwo = (const u16*)d_in[8];
  u16* out = (u16*)d_out;
  char* ws = (char*)d_ws;
  u16* qkvA = (u16*)ws;                 ws += (size_t)3 * HA * S_ * 32 * 2;
  u16* qB = (u16*)ws;                   ws += (size_t)S_ * NB * 2;
  u16* kB = (u16*)ws;                   ws += (size_t)S_ * NB * 2;
  u16* vB = (u16*)ws;                   ws += (size_t)S_ * NB * 2;
  u16* ctx = (u16*)ws;                  ws += (size_t)S_ * NB * 2;
  u16* oA = (u16*)ws;                   ws += (size_t)S_ * DM * 2;
  float* part = (float*)ws;             ws += (size_t)SPLITK * S_ * DM * 4;

  (void)hipFuncSetAttribute((const void*)k_proj, hipFuncAttributeMaxDynamicSharedMemorySize, G1::LDS);
  (void)hipFuncSetAttribute((const void*)k_out, hipFuncAttributeMaxDynamicSharedMemorySize, G2::LDS);
  (void)hipFuncSetAttribute((const void*)k_attn, hipFuncAttributeMaxDynamicSharedMemorySize, 81920);
  k_proj<<<448, 512, G1::LDS, stream>>>(Ax, Awqkv, Bx, Bckv, Bwq, Bwuk, Bwuv, qkvA, qB, kB, vB);
  k_attn<<<NBLK_B + NBLK_A, 256, 81920, stream>>>(qB, kB, vB, ctx, qkvA, oA);
  k_out<<<40 * SPLITK + 40, 512, G2::LDS, stream>>>(oA, Awo, ctx, Bwo, out, part);
  k_reduce<<<S_ * DM / 4 / 256, 256, 0, stream>>>(part, out);
}
```

```cpp
#include <hip/hip_runtime.h>
#include <hip/hip_bf16.h>
#include <stdint.h>
#include <type_traits>

constexpr int S_ = 2048;
constexpr int DM = 640;
constexpr int HA = 32, DHA = 20;
constexpr int HB = 32, DHB = 128, DC = 512;
constexpr int NB = HB * DHB;

typedef unsigned short u16;
typedef short bf16x8 __attribute__((ext_vector_type(8)));
typedef short s16x4 __attribute__((ext_vector_type(4)));
typedef float f32x16 __attribute__((ext_vector_type(16)));
typedef float f32x4 __attribute__((ext_vector_type(4)));
typedef float f32x2 __attribute__((ext_vector_type(2)));
typedef __bf16 bf16x2 __attribute__((ext_vector_type(2)));
typedef unsigned u32x2 __attribute__((ext_vector_type(2)));
typedef unsigned u32x4 __attribute__((ext_vector_type(4)));
typedef __attribute__((address_space(3))) s16x4 lds_s16x4;
typedef __attribute__((address_space(3))) bf16x8 lds_bf16x8;
typedef __attribute__((address_space(3))) u32x2 lds_u32x2;

#define DI __device__ __forceinline__
#define MFMA32(a, b, c) __builtin_amdgcn_mfma_f32_32x32x16_bf16((a), (b), (c), 0, 0, 0)

DI unsigned cvtpk(float lo, float hi) {
  f32x2 v = {lo, hi};
  bf16x2 b = __builtin_convertvector(v, bf16x2);
  return __builtin_bit_cast(unsigned, b);
}
DI void dma16(const void* sbase, unsigned voff, unsigned lds_addr) {
  unsigned keep;
  asm volatile("s_mov_b32 %0, m0\n\ts_mov_b32 m0, %3\n\ts_nop 0\n\tglobal_load_lds_dwordx4 %1, %2\n\ts_mov_b32 m0, %0"
               : "=&s"(keep) : "v"(voff), "s"(sbase), "s"(lds_addr) : "memory");
}
DI void dma_base(unsigned lds_addr) { asm volatile("s_mov_b32 m0, %0\n\ts_nop 0" :: "s"(lds_addr) : "memory"); }
template <int OFF> DI void dma_piece(const void* sbase, unsigned voff) {
  asm volatile("global_load_lds_dwordx4 %0, %1 offset:%2" :: "v"(voff), "s"(sbase), "i"(OFF) : "memory");
}
template <int OFF, int X> DI void dma_piece_x(const void* sbase, unsigned voff) {
  unsigned t;
  asm volatile("v_xor_b32 %0, %4, %1\n\tglobal_load_lds_dwordx4 %0, %2 offset:%3" : "=&v"(t) : "v"(voff), "s"(sbase), "i"(OFF), "i"(X) : "memory");
}
#define WAIT_VM0() asm volatile("s_waitcnt vmcnt(0)" ::: "memory")
#define BARRIER() do { asm volatile("s_waitcnt lgkmcnt(0)" ::: "memory"); __builtin_amdgcn_s_barrier(); asm volatile("" ::: "memory"); } while (0)

DI bf16x8 lds_read128(unsigned off) { return *(const lds_bf16x8*)(uintptr_t)off; }
DI s16x4 lds_read_tr(unsigned off) { return __builtin_amdgcn_ds_read_tr16_b64_v4i16((lds_s16x4*)(uintptr_t)off); }
DI bf16x8 cat8(s16x4 a, s16x4 b) { return __builtin_shufflevector(a, b, 0, 1, 2, 3, 4, 5, 6, 7); }
DI float xhalf_max(float x) {
  auto r = __builtin_amdgcn_permlane32_swap(__float_as_uint(x), __float_as_uint(x), false, false);
  return fmaxf(__uint_as_float(r[0]), __uint_as_float(r[1]));
}
DI float xhalf_sum(float x) {
  auto r = __builtin_amdgcn_permlane32_swap(__float_as_uint(x), __float_as_uint(x), false, false);
  return __uint_as_float(r[0]) + __uint_as_float(r[1]);
}

template <int BM, int BN, int WM, int WN, int NST_>
struct GCfg {
  static constexpr int NST = NST_;
  static constexpr int NW = WM * WN, TM = BM / WM, TN = BN / WN, MT = TM / 32, NT = TN / 32;
  static constexpr int XB = BM * 128, WROW = BN * 2, WB = 64 * WROW, STAGE = XB + WB;
  static constexpr int XI = BM / 8 / NW;
  static constexpr int WI = WB / 1024 / NW;
  static constexpr int WCH = BN / 8;
  static constexpr int WRPI = 64 / WCH;
  static constexpr int LDS = NST * STAGE;
};

template <class C, class Epi, bool XHEAD = false>
DI void gemm_tile(const u16* __restrict__ X, int ldx, const u16* __restrict__ W, int ldw, int m0, int n0, int ncols,
                  int kbeg, int nk, const Epi& epi) {
  const int tid = threadIdx.x, lane = tid & 63, wid = __builtin_amdgcn_readfirstlane(tid >> 6);
  const int wm = wid / C::WN_, wn = wid % C::WN_;
  const int r = lane & 31, h = lane >> 5;
  unsigned xoff[C::XI], woff[C::WI];
#pragma unroll
  for (int i = 0; i < C::XI; ++i) {
    const int row = 8 * (wid * C::XI + i) + (lane >> 3);
    xoff[i] = (unsigned)(row * ldx + (((lane & 7) ^ ((row >> 1) & 7)) * 8)) * 2u;
  }
#pragma unroll
  for (int i = 0; i < C::WI; ++i) {
    const int k = C::WRPI * (wid * C::WI + i) + lane / C::WCH;
    int col = (((lane % C::WCH) ^ ((k & 3) << 2)) * 8);
    if (n0 + col > ncols - 8) col = ncols - 8 - n0;
    woff[i] = (unsigned)(k * ldw + col) * 2u;
  }
  const u16* xs = XHEAD ? X + (size_t)m0 * ldx : X + (size_t)m0 * ldx + kbeg;
  const int kt0 = kbeg >> 6;
  auto xk = [&](int kt) { return XHEAD ? xs + (size_t)((kt + kt0) >> 1) * S_ * DHB + ((kt + kt0) & 1) * 64 : xs + kt * 64; };
  const u16* ws = W + (size_t)kbeg * ldw + n0;
  auto stage = [&](int buf, int kt) {
    const u16* xb = xk(kt);
    const u16* wb = ws + (size_t)kt * 64 * ldw;
    const unsigned lb = buf * C::STAGE;
#pragma unroll
    for (int i = 0; i < C::XI; ++i) dma16(xb, xoff[i], lb + (wid * C::XI + i) * 1024);
#pragma unroll
    for (int i = 0; i < C::WI; ++i) dma16(wb, woff[i], lb + C::XB + (wid * C::WI + i) * 1024);
  };
  unsigned xa[4];
#pragma unroll
  for (int ks = 0; ks < 4; ++ks) xa[ks] = (wm * C::TM + r) * 128 + (((2 * ks + h) ^ ((r >> 1) & 7)) * 16);
  unsigned wa[C::NT];
  {
    const int q = (lane & 15) >> 2, p = lane & 3, cb = (lane >> 4) & 1;
#pragma unroll
    for (int nt = 0; nt < C::NT; ++nt) {
      const int nb = wn * C::NT + nt;
      wa[nt] = C::XB + (8 * h + q) * C::WROW + ((nb ^ q) * 64) + cb * 32 + p * 8;
    }
  }
  f32x16 acc[C::NT][C::MT];
#pragma unroll
  for (int nt = 0; nt < C::NT; ++nt)
#pragma unroll
    for (int mt = 0; mt < C::MT; ++mt) acc[nt][mt] = f32x16{};

  constexpr int AHEAD = C::NST - 1, PER = C::XI + C::WI;
  stage(0, 0);
  if constexpr (AHEAD == 2) { stage(1, 1); asm volatile("s_waitcnt vmcnt(%0)" :: "i"(PER) : "memory"); }
  else WAIT_VM0();
  BARRIER();
  int cur = 0, nxt = AHEAD;
  for (int kt = 0; kt < nk; ++kt) {
    const unsigned lb = cur * C::STAGE;
    const bool more = kt + AHEAD < nk;
    const u16* xb = xk(kt + AHEAD);
    const u16* wb = ws + (size_t)(kt + AHEAD) * 64 * ldw;
    const unsigned nb = nxt * C::STAGE;
    cur = cur + 1 == C::NST ? 0 : cur + 1;
    nxt = nxt + 1 == C::NST ? 0 : nxt + 1;
#pragma unroll
    for (int ks = 0; ks < 4; ++ks) {
      bf16x8 xf[C::MT], wf[C::NT];
#pragma unroll
      for (int mt = 0; mt < C::MT; ++mt) xf[mt] = lds_read128(lb + xa[ks] + mt * 4096);
#pragma unroll
      for (int nt = 0; nt < C::NT; ++nt)
        wf[nt] = cat8(lds_read_tr(lb + wa[nt] + (16 * ks) * C::WROW), lds_read_tr(lb + wa[nt] + (16 * ks + 4) * C::WROW));
      if (more) {
        constexpr int TOT = C::XI + C::WI;
#pragma unroll
        for (int i = ks * TOT / 4; i < (ks + 1) * TOT / 4; ++i) {
          if (i < C::XI) dma16(xb, xoff[i], nb + (wid * C::XI + i) * 1024);
          else dma16(wb, woff[i - C::XI], nb + C::XB + (wid * C::WI + i - C::XI) * 1024);
        }
      }
#pragma unroll
      for (int nt = 0; nt < C::NT; ++nt)
#pragma unroll
        for (int mt = 0; mt < C::MT; ++mt) acc[nt][mt] = MFMA32(wf[nt], xf[mt], acc[nt][mt]);
    }
    if (AHEAD == 2 && more) asm volatile("s_waitcnt vmcnt(%0)" :: "i"(PER) : "memory");
    else WAIT_VM0();
    BARRIER();
  }
  if constexpr (Epi::DIRECT) {
    f32x4* dst = (f32x4*)epi.out + (size_t)wid * (C::NT * C::MT * 4 * 64) + lane;
#pragma unroll
    for (int nt = 0; nt < C::NT; ++nt)
#pragma unroll
      for (int mt = 0; mt < C::MT; ++mt)
#pragma unroll
        for (int g = 0; g < 4; ++g) {
          f32x4 v = {acc[nt][mt][4 * g], acc[nt][mt][4 * g + 1], acc[nt][mt][4 * g + 2], acc[nt][mt][4 * g + 3]};
          __builtin_nontemporal_store(v, dst + ((nt * C::MT + mt) * 4 + g) * 64);
        }
  } else {
    static_assert(C::TN == 64, "staging layout");
    const unsigned sb = wid * (C::TM * 128);
#pragma unroll
    for (int nt = 0; nt < C::NT; ++nt)
#pragma unroll
      for (int g = 0; g < 4; ++g) {
        const int nl = nt * 32 + 8 * g + 4 * h;
        const float sc = epi.scale_of(n0 + wn * C::TN + nl);
        const unsigned a = sb + r * 128 + (((nl >> 3) ^ (r & 7)) * 16) + (nl & 4) * 2;
#pragma unroll
        for (int mt = 0; mt < C::MT; ++mt) {
          u32x2 v = {cvtpk(acc[nt][mt][4 * g] * sc, acc[nt][mt][4 * g + 1] * sc),
                     cvtpk(acc[nt][mt][4 * g + 2] * sc, acc[nt][mt][4 * g + 3] * sc)};
          *(lds_u32x2*)(uintptr_t)(a + mt * 4096) = v;
        }
      }
    epi.template store<C::TM>(sb, m0 + wm * C::TM, n0 + wn * C::TN, lane);
  }
}

template <int BM, int BN, int WM, int WN, int NST>
struct GC : GCfg<BM, BN, WM, WN, NST> { static constexpr int WN_ = WN; };

struct EpiPlain {
  static constexpr bool DIRECT = false, PREFW = false;
  u16* out; int ld; float scale;
  DI float scale_of(int) const { return scale; }
  template <int TM> DI void store(unsigned sb, int mb, int nb, int lane) const {
    const int rr = lane >> 3, c = lane & 7;
    u16* dst = out + (size_t)(mb + rr) * ld + nb + c * 8;
    const unsigned a = sb + rr * 128 + ((c ^ (rr & 7)) * 16);
#pragma unroll
    for (int it = 0; it < TM / 8; ++it) {
      const bf16x8 v = lds_read128(a + it * 1024);
      *(bf16x8*)(dst + (size_t)it * 8 * ld) = v;
    }
  }
};
struct EpiHead {
  static constexpr bool DIRECT = false, PREFW = true;
  u16* out; float scale;
  DI float scale_of(int) const { return scale; }
  template <int TM> DI void store(unsigned sb, int mb, int nb, int lane) const {
    const int rr = lane >> 3, c = lane & 7;
    u16* dst = out + ((size_t)(nb >> 7) * S_ + mb + rr) * DHB + (nb & 127) + c * 8;
    const unsigned a = sb + rr * 128 + ((c ^ (rr & 7)) * 16);
#pragma unroll
    for (int it = 0; it < TM / 8; ++it) {
      const bf16x8 v = lds_read128(a + it * 1024);
      __builtin_nontemporal_store(v, (bf16x8*)(dst + (size_t)it * 8 * DHB));
    }
  }
};
struct EpiQKVA {
  static constexpr bool DIRECT = false, PREFW = true;
  u16* out; float qscale;
  DI float scale_of(int n) const { return n < DM ? qscale : 1.f; }
  template <int TM> DI void store(unsigned sb, int mb, int nb, int lane) const {
    const int rr = lane >> 4, gq = lane & 15;
    const int n = nb + gq * 4;
    if (n >= 3 * DM) return;
    const int which = n / DM, rem = n - which * DM, head = rem / DHA, dd = rem - head * DHA;
    u16* dst = out + ((size_t)((which * HA + head) * S_ + mb + rr)) * 32;
#pragma unroll 4
    for (int it = 0; it < TM / 4; ++it) {
      const int row = it * 4 + rr;
      const u32x2 v = *(const lds_u32x2*)(uintptr_t)(sb + row * 128 + (((gq >> 1) ^ (row & 7)) * 16) + (gq & 1) * 8);
      u16* d = dst + it * 4 * 32;
      *(u32x2*)(d + dd) = v;
      if (dd == 16) {
        u32x2 z0 = {which == 2 ? 0x3F80u : 0u, 0u};
        u32x4 z1 = {0u, 0u, 0u, 0u};
        *(u32x2*)(d + 20) = z0;
        *(u32x4*)(d + 24) = z1;
      }
    }
  }
};
struct EpiF32 {
  static constexpr bool DIRECT = true, PREFW = false;
  float* out;
};

typedef GC<256, 256, 2, 4, 2> G1;
typedef GC<256, 128, 4, 2, 3> G2;

constexpr float LOG2E = 1.4426950408889634f;

__global__ __launch_bounds__(512, 2) void k_proj(const u16* __restrict__ Ax, const u16* __restrict__ Awqkv,
                                                 const u16* __restrict__ Bx, const u16* __restrict__ Bckv,
                                                 const u16* __restrict__ Bwq, const u16* __restrict__ Bwuk,
                                                 const u16* __restrict__ Bwuv, u16* __restrict__ qkvA,
                                                 u16* __restrict__ qB, u16* __restrict__ kB, u16* __restrict__ vB) {
  const int b = blockIdx.x, xcd = b & 7, idx = b >> 3;
  const int tn = xcd + 8 * (idx >> 3), tm = idx & 7;
  const int m0 = tm * 256;
  if (tn < 8) {
    EpiQKVA e{qkvA, 0.22360679774997896f * LOG2E};
    gemm_tile<G1>(Ax, DM, Awqkv, 3 * DM, m0, tn * 256, 3 * DM, 0, DM / 64, e);
  } else if (tn < 24) {
    EpiHead e{qB, 0.08838834764831845f * LOG2E};
    gemm_tile<G1>(Bx, DM, Bwq, NB, m0, (tn - 8) * 256, NB, 0, DM / 64, e);
  } else {
    const bool isv = tn >= 40;
    EpiHead e{isv ? vB : kB, 1.f};
    gemm_tile<G1>(Bckv, DC, isv ? Bwuv : Bwuk, NB, m0, (tn - (isv ? 40 : 24)) * 256, NB, 0, DC / 64, e);
  }
}

constexpr int SPLITK = 5;
__global__ __launch_bounds__(512, 2) void k_out(const u16* __restrict__ oA, const u16* __restrict__ Awo,
                                                const u16* __restrict__ ctx, const u16* __restrict__ Bwo,
                                                u16* __restrict__ out, float* __restrict__ part) {
  const int b = blockIdx.x;
  if (b < 40 * SPLITK) {
    const int tm = b & 7, rest = b >> 3, tn = rest % 5, sp = rest / 5;
    EpiF32 e{part + ((size_t)sp * 40 + tm * 5 + tn) * (256 * 128)};
    gemm_tile<G2, EpiF32, true>(ctx, DHB, Bwo, DM, tm * 256, tn * 128, DM, sp * 13 * 64, sp == 4 ? 12 : 13, e);
  } else {
    const int t = b - 40 * SPLITK, tm = t & 7, tn = t >> 3;
    EpiPlain e{out, 2 * DM, 1.f};
    gemm_tile<G2>(oA, DM, Awo, DM, tm * 256, tn * 128, DM, 0, DM / 64, e);
  }
}

__global__ __launch_bounds__(256) void k_reduce(const float* __restrict__ part, u16* __restrict__ out) {
  const int i = blockIdx.x * 256 + threadIdx.x;
  const int tile = i >> 13, w = (i >> 10) & 7, j = (i >> 6) & 15, lane = i & 63;
  const int nt = j >> 3, mt = (j >> 2) & 1, g = j & 3;
  const int tm = tile / 5, tn = tile - tm * 5;
  const int m = tm * 256 + (w >> 1) * 64 + mt * 32 + (lane & 31);
  const int n = tn * 128 + (w & 1) * 64 + nt * 32 + 8 * g + 4 * (lane >> 5);
  const f32x4* p = (const f32x4*)part + i;
  f32x4 s = p[0];
#pragma unroll
  for (int sp = 1; sp < SPLITK; ++sp) s += p[(size_t)sp * 40 * 8192];
  u32x2 v = {cvtpk(s[0], s[1]), cvtpk(s[2], s[3])};
  *(u32x2*)(out + (size_t)m * 2 * DM + DM + n) = v;
}

constexpr float THR = 8.f;
DI bf16x8 pack8(const f32x16& x, int s) {
  u32x4 w = {cvtpk(x[8 * s], x[8 * s + 1]), cvtpk(x[8 * s + 2], x[8 * s + 3]), cvtpk(x[8 * s + 4], x[8 * s + 5]),
             cvtpk(x[8 * s + 6], x[8 * s + 7])};
  return __builtin_bit_cast(bf16x8, w);
}
DI f32x16 splat16(float v) {
  f32x16 x;
#pragma unroll
  for (int i = 0; i < 16; ++i) x[i] = v;
  return x;
}
DI float max16(const f32x16& x) {
  float a = fmaxf(fmaxf(x[0], x[1]), x[2]);
#pragma unroll
  for (int i = 3; i < 15; i += 2) a = fmaxf(fmaxf(a, x[i]), x[i + 1]);
  return fmaxf(a, x[15]);
}

#define SBAR() __builtin_amdgcn_sched_barrier(0)
constexpr float BIGSUM = 4096.f;
DI float sum16(const f32x16& x) {
  float a = (x[0] + x[1]) + (x[2] + x[3]), b = (x[4] + x[5]) + (x[6] + x[7]);
  float c = (x[8] + x[9]) + (x[10] + x[11]), d = (x[12] + x[13]) + (x[14] + x[15]);
  return (a + b) + (c + d);
}
DI void exp16(f32x16& x) {
#pragma unroll
  for (int i = 0; i < 16; ++i) x[i] = __builtin_amdgcn_exp2f(x[i]);
}
DI void attnB_item(const u16* __restrict__ Q, const u16* __restrict__ K, const u16* __restrict__ V,
                   u16* __restrict__ ctx, int head, int jitem) {
  const int tid = threadIdx.x, lane = tid & 63, wid = __builtin_amdgcn_readfirstlane(tid >> 6);
  const int r = lane & 31, h = lane >> 5;
  constexpr unsigned KBUF = 0, VBUF = 32768, TB = 16384, STG = 65536;
  unsigned koff, voff;
  {
    const int l4 = lane >> 4, c = lane & 15;
    koff = (unsigned)((16 * wid + l4) * DHB + ((c ^ l4) * 8)) * 2u;
    voff = (unsigned)((16 * wid + l4) * DHB + ((c ^ (l4 << 2)) * 8)) * 2u;
  }
  const u16* Kh = K + (size_t)head * S_ * DHB;
  const u16* Vh = V + (size_t)head * S_ * DHB;
  auto stageK = [&](int buf, int t) {
    const u16* g = Kh + (size_t)t * 64 * DHB;
    dma_base(KBUF + buf * TB + wid * 4096);
    dma_piece<0>(g, koff); dma_piece_x<1024, 64>(g, koff); dma_piece_x<2048, 128>(g, koff); dma_piece_x<3072, 192>(g, koff);
  };
  auto stageV = [&](int buf, int t) {
    const u16* g = Vh + (size_t)t * 64 * DHB;
    dma_base(VBUF + buf * TB + wid * 4096);
    dma_piece<0>(g, voff); dma_piece<1024>(g, voff); dma_piece<2048>(g, voff); dma_piece<3072>(g, voff);
  };
  unsigned ka[8], va[4];
#pragma unroll
  for (int ks = 0; ks < 8; ++ks) {
    ka[ks] = KBUF + r * 256 + (((2 * ks + h) ^ (r & 15)) * 16);
    asm volatile("" : "+v"(ka[ks]));
  }
  {
    const int q = (lane & 15) >> 2, p = lane & 3, cb = (lane >> 4) & 1;
#pragma unroll
    for (int db = 0; db < 4; ++db) va[db] = VBUF + (4 * h + q) * 256 + ((db ^ q) * 64) + cb * 32 + p * 8;
  }
  bf16x8 qf[8];
  f32x16 o[4], negm;
  float mcur, lsum;
  auto loadQ = [&](int qb) {
    int lz = threadIdx.x;
    asm volatile("" : "+v"(lz));
    const u16* qp = Q + ((size_t)head * S_ + qb * 128) * DHB + (unsigned)((wid * 32 + (lz & 31)) * DHB + ((lz >> 5) & 1) * 8);
#pragma unroll
    for (int ks = 0; ks < 8; ++ks) qf[ks] = *(const bf16x8*)(qp + ks * 16);
  };
  stageK(0, 0); stageK(1, 1); stageV(0, 0);
  loadQ(15 - jitem);

  auto qk_abs = [&](unsigned off) {
    bf16x8 kf[8];
#pragma unroll
    for (int ks = 0; ks < 8; ++ks) kf[ks] = lds_read128(off + ka[ks]);
    f32x16 s = MFMA32(kf[0], qf[0], f32x16{});
#pragma unroll
    for (int ks = 1; ks < 8; ++ks) s = MFMA32(kf[ks], qf[ks], s);
    return s;
  };
  auto remax = [&](const f32x16& st, bool first, f32x16& s) {
    const float tm = xhalf_max(max16(st));
    const float mnew = first ? tm : fmaxf(tm, mcur);
    const float delta = mnew - mcur;
    const float f = __builtin_amdgcn_exp2f(-fmaxf(delta, 0.f));
    mcur = mnew;
    negm = splat16(-mnew);
#pragma unroll
    for (int i = 0; i < 16; ++i) s[i] = __builtin_amdgcn_exp2f(st[i] - mnew);
#pragma unroll
    for (int db = 0; db < 4; ++db)
#pragma unroll
      for (int i = 0; i < 16; ++i) o[db][i] *= f;
    lsum *= f;
    return delta;
  };
  auto slow = [&](int blk, f32x16& s) {
    int lz = threadIdx.x;
    asm volatile("" : "+v"(lz));
    const u16* kp = Kh + (size_t)blk * 32 * DHB + (unsigned)((lz & 31) * DHB + ((lz >> 5) & 1) * 8);
    f32x16 st = f32x16{};
#pragma unroll
    for (int ks = 0; ks < 8; ++ks) {
      st = MFMA32(*(const bf16x8*)(kp + ks * 16), qf[ks], st);
      asm volatile("" ::: "memory");
      SBAR();
    }
    return remax(st, false, s);
  };
  auto pv = [&](unsigned off, const f32x16& p) {
#pragma unroll
    for (int sx = 0; sx < 2; ++sx) {
      const bf16x8 pf = pack8(p, sx);
#pragma unroll
      for (int db = 0; db < 4; ++db) {
        const bf16x8 vf = cat8(lds_read_tr(off + va[db] + (16 * sx) * 256), lds_read_tr(off + va[db] + (16 * sx + 8) * 256));
        o[db] = MFMA32(vf, pf, o[db]);
      }
    }
  };
#define EXP2(x) x = __builtin_amdgcn_exp2f(x)
#define VRD(dst, off) dst = cat8(lds_read_tr(off), lds_read_tr((off) + 2048))

#pragma unroll 1
  for (int pass = 0; pass < 2; ++pass) {
    const int qb = pass ? jitem : 15 - jitem;
    const int nfull = 2 * qb;
    auto init = [&]() {
      float z = 0.f;
      asm volatile("" : "+v"(z));
#pragma unroll
      for (int db = 0; db < 4; ++db) o[db] = splat16(z);
      mcur = z; lsum = z;
    };
#pragma unroll
    for (int ks = 0; ks < 8; ++ks) asm volatile("" : "+v"(qf[ks]));
    WAIT_VM0();
    BARRIER();
    if (nfull == 0) init();
    else {
      init();
      f32x16 sa, sb;
      bf16x8 kg[8];
      float pa;
      {
        const f32x16 st = qk_abs(0);
        const float m = xhalf_max(max16(st));
        mcur = m;
        negm = splat16(-m);
#pragma unroll
        for (int i = 0; i < 8; ++i) sa[i] = __builtin_amdgcn_exp2f(st[i] - m);
#pragma unroll
        for (int i = 8; i < 16; ++i) sa[i] = st[i] - m;
        pa = ((sa[0] + sa[1]) + (sa[2] + sa[3])) + ((sa[4] + sa[5]) + (sa[6] + sa[7]));
#pragma unroll
        for (int ks = 0; ks < 8; ++ks) kg[ks] = lds_read128(ka[ks] + 8192);
      }
      auto tile = [&](auto BUFC, auto LASTC, int t) {
        constexpr unsigned B_ = decltype(BUFC)::value;
        constexpr bool LAST = decltype(LASTC)::value;
        constexpr unsigned vo = B_ * TB, vn = TB - vo;
        constexpr unsigned kn = TB - vo, kd = vo;
        const u16* kbn = Kh + (size_t)(t + 2) * 64 * DHB;
        const u16* vbn = Vh + (size_t)(t + 1) * 64 * DHB;
#define DMAK(i) do { if ((i) == 0) { dma_base(KBUF + kd + wid * 4096); dma_piece<0>(kbn, koff); } else dma_piece_x<(i) * 1024, (i) * 64>(kbn, koff); } while (0)
#define DMAV(i) do { if ((i) == 0) dma_base(VBUF + vn + wid * 4096); dma_piece<(i) * 1024>(vbn, voff); } while (0)
        bf16x8 kf[8], vf[8], vg[8];
#define KF(i) kf[i] = lds_read128(kn + ka[i])
#define KG(i) kg[i] = lds_read128(kn + ka[i] + 8192)
        sb = MFMA32(kg[0], qf[0], negm);
        VRD(vf[0], vo + va[0]); DMAV(0);
        EXP2(sa[8]); EXP2(sa[9]);
        SBAR();
        sb = MFMA32(kg[1], qf[1], sb);
        VRD(vf[1], vo + va[1]); DMAV(1);
        EXP2(sa[10]); EXP2(sa[11]);
        SBAR();
        sb = MFMA32(kg[2], qf[2], sb);
        VRD(vf[2], vo + va[2]); DMAV(2);
        EXP2(sa[12]); EXP2(sa[13]);
        float pa2 = (sa[8] + sa[9]) + (sa[10] + sa[11]);
        SBAR();
        sb = MFMA32(kg[3], qf[3], sb);
        VRD(vf[3], vo + va[3]); DMAV(3);
        EXP2(sa[14]); EXP2(sa[15]);
        pa += sa[12] + sa[13];
        SBAR();
        sb = MFMA32(kg[4], qf[4], sb);
        DMAK(0);
        pa2 += sa[14] + sa[15];
        pa += pa2;
        if (__any(!(pa <= BIGSUM))) {
          const float delta = slow(2 * t, sa);
#pragma unroll
          for (int i = 0; i < 16; ++i) sb[i] -= delta;
          pa = sum16(sa);
        }
        sb = MFMA32(kg[5], qf[5], sb);
        VRD(vf[4], vo + va[0] + 4096); VRD(vf[5], vo + va[1] + 4096);
        const bf16x8 p0 = pack8(sa, 0);
        lsum += pa;
        SBAR();
        sb = MFMA32(kg[6], qf[6], sb);
        VRD(vf[6], vo + va[2] + 4096); DMAK(1);
        const bf16x8 p1 = pack8(sa, 1);
        SBAR();
        sb = MFMA32(kg[7], qf[7], sb);
        VRD(vf[7], vo + va[3] + 4096); DMAK(2);
        SBAR();
        o[0] = MFMA32(vf[0], p0, o[0]);
        KF(0); DMAK(3);
        SBAR();
        o[1] = MFMA32(vf[1], p0, o[1]);
        KF(1);
        EXP2(sb[0]); EXP2(sb[1]); EXP2(sb[2]);
        SBAR();
        o[2] = MFMA32(vf[2], p0, o[2]);
        KF(2);
        EXP2(sb[3]); EXP2(sb[4]);
        SBAR();
        o[3] = MFMA32(vf[3], p0, o[3]);
        KF(3);
        EXP2(sb[5]); EXP2(sb[6]);
        float pb = (sb[0] + sb[1]) + (sb[2] + sb[3]);
        SBAR();
        o[0] = MFMA32(vf[4], p1, o[0]);
        KF(4);
        EXP2(sb[7]); EXP2(sb[8]);
        SBAR();
        o[1] = MFMA32(vf[5], p1, o[1]);
        KF(5);
        EXP2(sb[9]); EXP2(sb[10]);
        float pb2 = (sb[4] + sb[5]) + (sb[6] + sb[7]);
        SBAR();
        o[2] = MFMA32(vf[6], p1, o[2]);
        KF(6);
        EXP2(sb[11]); EXP2(sb[12]);
        SBAR();
        o[3] = MFMA32(vf[7], p1, o[3]);
        KF(7);
        EXP2(sb[13]); EXP2(sb[14]);
        pb += (sb[8] + sb[9]) + sb[10];
        SBAR();
        sa = MFMA32(kf[0], qf[0], negm);
        VRD(vg[0], vo + va[0] + 8192);
        EXP2(sb[15]);
        pb2 += (sb[11] + sb[12]) + sb[13];
        SBAR();
        sa = MFMA32(kf[1], qf[1], sa);
        VRD(vg[1], vo + va[1] + 8192);
        pb += sb[14] + sb[15];
        pb += pb2;
        if (__any(!(pb <= BIGSUM))) {
          const float delta = slow(2 * t + 1, sb);
#pragma unroll
          for (int i = 0; i < 16; ++i) sa[i] -= delta;
          pb = sum16(sb);
        }
        sa = MFMA32(kf[2], qf[2], sa);
        VRD(vg[2], vo + va[2] + 8192);
        const bf16x8 r0 = pack8(sb, 0);
        lsum += pb;
        SBAR();
        sa = MFMA32(kf[3], qf[3], sa);
        VRD(vg[3], vo + va[3] + 8192);
        const bf16x8 r1 = pack8(sb, 1);
        SBAR();
        sa = MFMA32(kf[4], qf[4], sa);
        VRD(vg[4], vo + va[0] + 12288);
        SBAR();
        sa = MFMA32(kf[5], qf[5], sa);
        VRD(vg[5], vo + va[1] + 12288);
        SBAR();
        sa = MFMA32(kf[6], qf[6], sa);
        VRD(vg[6], vo + va[2] + 12288);
        SBAR();
        sa = MFMA32(kf[7], qf[7], sa);
        VRD(vg[7], vo + va[3] + 12288);
        SBAR();
        o[0] = MFMA32(vg[0], r0, o[0]);
        KG(0);
        SBAR();
        o[1] = MFMA32(vg[1], r0, o[1]);
        KG(1); EXP2(sa[0]); EXP2(sa[1]);
        SBAR();
        o[2] = MFMA32(vg[2], r0, o[2]);
        KG(2); EXP2(sa[2]); EXP2(sa[3]);
        SBAR();
        o[3] = MFMA32(vg[3], r0, o[3]);
        KG(3); EXP2(sa[4]); EXP2(sa[5]);
        SBAR();
        o[0] = MFMA32(vg[4], r1, o[0]);
        KG(4); EXP2(sa[6]); EXP2(sa[7]);
        pa = (sa[0] + sa[1]) + (sa[2] + sa[3]);
        SBAR();
        o[1] = MFMA32(vg[5], r1, o[1]);
        KG(5);
        pa += (sa[4] + sa[5]) + (sa[6] + sa[7]);
        SBAR();
        o[2] = MFMA32(vg[6], r1, o[2]);
        KG(6); KG(7);
        SBAR();
        o[3] = MFMA32(vg[7], r1, o[3]);
#undef KF
#undef KG
        WAIT_VM0();
        BARRIER();
#undef DMAK
#undef DMAV
      };
      typedef std::integral_constant<unsigned, 0> C0;
      typedef std::integral_constant<unsigned, 1> C1;
      int t = 0;
      do {
        tile(C0{}, std::false_type{}, t);
        tile(C1{}, std::false_type{}, t + 1);
        t += 2;
      } while (t < nfull);
    }
    stageV(1, nfull + 1);
#pragma unroll 1
    for (int tt = 0; tt < 2; ++tt) {
#pragma unroll 1
      for (int jj = 0; jj < 2; ++jj) {
        const int j = 2 * tt + jj;
        if (j <= wid) {
          f32x16 st = qk_abs(tt * TB + jj * 8192), s;
          if (j == wid) {
            int lz = threadIdx.x;
            asm volatile("" : "+v"(lz));
            const int dq = (lz & 31) - 4 * ((lz >> 5) & 1);
#pragma unroll
            for (int i = 0; i < 16; ++i) {
              const int kr = (i & 3) + 8 * (i >> 2);
              if (kr > dq) st[i] = -__builtin_inff();
            }
          }
          remax(st, nfull == 0 && j == 0, s);
          lsum += sum16(s);
          pv(tt * TB + jj * 32 * 256, s);
        }
      }
      WAIT_VM0();
      BARRIER();
    }
    if (pass == 0) { stageK(0, 0); stageK(1, 1); stageV(0, 0); }
    const float inv = 1.f / xhalf_sum(lsum);
    {
      int lz = threadIdx.x;
      asm volatile("" : "+v"(lz));
      const int lane = lz & 63, r = lz & 31, h = (lz >> 5) & 1;
      const unsigned sb_ = STG + wid * 4096;
      u16* op = ctx + ((size_t)head * S_ + qb * 128 + wid * 32 + (lane >> 3)) * DHB + (lane & 7) * 8;
#pragma unroll
      for (int hf = 0; hf < 2; ++hf) {
#pragma unroll
        for (int d2 = 0; d2 < 2; ++d2)
#pragma unroll
          for (int g = 0; g < 4; ++g) {
            const f32x16& x = o[2 * hf + d2];
            u32x2 v = {cvtpk(x[4 * g] * inv, x[4 * g + 1] * inv), cvtpk(x[4 * g + 2] * inv, x[4 * g + 3] * inv)};
            *(lds_u32x2*)(uintptr_t)(sb_ + r * 128 + (((4 * d2 + g) ^ (r & 7)) * 16) + h * 8) = v;
          }
#pragma unroll
        for (int it = 0; it < 4; ++it) {
          const int rr = it * 8 + (lane >> 3);
          const bf16x8 v = lds_read128(sb_ + rr * 128 + (((lane & 7) ^ (rr & 7)) * 16));
          __builtin_nontemporal_store(v, (bf16x8*)(op + (size_t)it * 8 * DHB + hf * 64));
        }
      }
    }
    if (pass == 0) loadQ(jitem);
  }
#undef EXP2
#undef VRD
}

template <int QT>
DI void attnA_block(const u16* __restrict__ qkv, u16* __restrict__ oA, int head, int qb) {
  const int tid = threadIdx.x, lane = tid & 63, wid = __builtin_amdgcn_readfirstlane(tid >> 6);
  const int r = lane & 31, h = lane >> 5;
  const int q0 = qb * (128 * QT) + wid * (32 * QT);
  constexpr unsigned KBUF = 0, VBUF = 16384, TB = 8192;
  const u16* Qh = qkv + (size_t)(0 * HA + head) * S_ * 32;
  const u16* Kh = qkv + (size_t)(1 * HA + head) * S_ * 32;
  const u16* Vh = qkv + (size_t)(2 * HA + head) * S_ * 32;
  unsigned koff[2], voff[2];
#pragma unroll
  for (int i = 0; i < 2; ++i) {
    const int row = 16 * (wid * 2 + i) + (lane >> 2), c = lane & 3;
    koff[i] = (unsigned)(row * 64 + ((c ^ ((row >> 2) & 3)) * 16));
    voff[i] = (unsigned)(row * 64 + c * 16);
  }
  auto stage = [&](int buf, int t) {
    const u16* kb = Kh + (size_t)t * 128 * 32;
    const u16* vb = Vh + (size_t)t * 128 * 32;
#pragma unroll
    for (int i = 0; i < 2; ++i) dma16(kb, koff[i], KBUF + buf * TB + (wid * 2 + i) * 1024);
#pragma unroll
    for (int i = 0; i < 2; ++i) dma16(vb, voff[i], VBUF + buf * TB + (wid * 2 + i) * 1024);
  };
  stage(0, 0);
  bf16x8 qf[QT][2];
#pragma unroll
  for (int qt = 0; qt < QT; ++qt)
#pragma unroll
    for (int ks = 0; ks < 2; ++ks) qf[qt][ks] = *(const bf16x8*)(Qh + (size_t)(q0 + 32 * qt + r) * 32 + ks * 16 + h * 8);
  unsigned ka[2], va;
#pragma unroll
  for (int ks = 0; ks < 2; ++ks) ka[ks] = KBUF + r * 64 + (((2 * ks + h) ^ ((r >> 2) & 3)) * 16);
  {
    const int q = (lane & 15) >> 2, p = lane & 3, cb = (lane >> 4) & 1;
    va = VBUF + (4 * h + q) * 64 + cb * 32 + p * 8;
  }
  f32x16 o[QT], negm[QT];
  float mcur[QT];
#pragma unroll
  for (int qt = 0; qt < QT; ++qt) { o[qt] = f32x16{}; negm[qt] = f32x16{}; mcur[qt] = 0.f; }
#pragma unroll
  for (int qt = 0; qt < QT; ++qt) { asm volatile("" : "+v"(qf[qt][0])); asm volatile("" : "+v"(qf[qt][1])); }
  WAIT_VM0();
  BARRIER();
  constexpr int NKT = S_ / 128;
  for (int t = 0; t < NKT; ++t) {
    const unsigned bo = (t & 1) * TB;
    if (t + 1 < NKT) stage((t + 1) & 1, t + 1);
    f32x16 s[QT][4];
#pragma unroll
    for (int kb = 0; kb < 4; ++kb) {
      const bf16x8 k0 = lds_read128(bo + ka[0] + kb * 2048), k1 = lds_read128(bo + ka[1] + kb * 2048);
#pragma unroll
      for (int qt = 0; qt < QT; ++qt) {
        s[qt][kb] = MFMA32(k0, qf[qt][0], negm[qt]);
        s[qt][kb] = MFMA32(k1, qf[qt][1], s[qt][kb]);
      }
    }
    float tm[QT];
    bool need = t == 0;
#pragma unroll
    for (int qt = 0; qt < QT; ++qt) {
      tm[qt] = xhalf_max(fmaxf(fmaxf(max16(s[qt][0]), max16(s[qt][1])), fmaxf(max16(s[qt][2]), max16(s[qt][3]))));
      need = need || tm[qt] > THR;
    }
    if (__any(need)) {
#pragma unroll
      for (int qt = 0; qt < QT; ++qt) {
        const float dpos = fmaxf(tm[qt], 0.f);
        const float delta = t == 0 ? tm[qt] : dpos;
        const float f = __builtin_amdgcn_exp2f(-dpos);
        mcur[qt] += delta;
        negm[qt] = splat16(-mcur[qt]);
#pragma unroll
        for (int kb = 0; kb < 4; ++kb)
#pragma unroll
          for (int i = 0; i < 16; ++i) s[qt][kb][i] -= delta;
#pragma unroll
        for (int i = 0; i < 16; ++i) o[qt][i] *= f;
      }
    }
#pragma unroll
    for (int kb = 0; kb < 4; ++kb) {
#pragma unroll
      for (int sx = 0; sx < 2; ++sx) {
        const bf16x8 vf = cat8(lds_read_tr(bo + va + (32 * kb + 16 * sx) * 64), lds_read_tr(bo + va + (32 * kb + 16 * sx + 8) * 64));
#pragma unroll
        for (int qt = 0; qt < QT; ++qt) {
#pragma unroll
          for (int i = 0; i < 8; ++i) s[qt][kb][8 * sx + i] = __builtin_amdgcn_exp2f(s[qt][kb][8 * sx + i]);
          o[qt] = MFMA32(vf, pack8(s[qt][kb], sx), o[qt]);
        }
      }
    }
    WAIT_VM0();
    BARRIER();
  }
#pragma unroll
  for (int qt = 0; qt < QT; ++qt) {
    const float l = __shfl(o[qt][8], r + 32);
    const float inv = 1.f / l;
    u16* op = oA + (size_t)(q0 + 32 * qt + r) * DM + head * DHA + 4 * h;
#pragma unroll
    for (int g = 0; g < 3; ++g) {
      if (g < 2 || h == 0) {
        u32x2 v = {cvtpk(o[qt][4 * g] * inv, o[qt][4 * g + 1] * inv), cvtpk(o[qt][4 * g + 2] * inv, o[qt][4 * g + 3] * inv)};
        *(u32x2*)(op + 8 * g) = v;
      }
    }
  }
}

constexpr int AQT = 2;
constexpr int NBLK_B = HB * 8, NBLK_A = HA * (S_ / (128 * AQT));
__global__ __launch_bounds__(256, 2) void k_attn(const u16* __restrict__ qB, const u16* __restrict__ kB,
                                                 const u16* __restrict__ vB, u16* __restrict__ ctx,
                                                 const u16* __restrict__ qkvA, u16* __restrict__ oA) {
  const int b = blockIdx.x;
  if (b < NBLK_B) {
    const int head = (b & 7) + 8 * (b >> 6), j = (b >> 3) & 7;
    attnB_item(qB, kB, vB, ctx, head, j);
  } else {
    const int a = b - NBLK_B;
    constexpr int PER = S_ / (128 * AQT);
    const int head = (a & 7) + 8 * (a / (8 * PER)), qb = (a >> 3) % PER;
    attnA_block<AQT>(qkvA, oA, head, qb);
  }
}

extern "C" void kernel_launch(void* const* d_in, const int* in_sizes, int n_in, void* d_out, int out_size, void* d_ws,
                              size_t ws_size, hipStream_t stream) {
  const u16* Ax = (const u16*)d_in[0];
  const u16* Awqkv = (const u16*)d_in[1];
  const u16* Awo = (const u16*)d_in[2];
  const u16* Bx = (const u16*)d_in[3];
  const u16* Bckv = (const u16*)d_in[4];
  const u16* Bwq = (const u16*)d_in[5];
  const u16* Bwuk = (const u16*)d_in[6];
  const u16* Bwuv = (const u16*)d_in[7];
  const u16* Bwo = (const u16*)d_in[8];
  u16* out = (u16*)d_out;
  char* ws = (char*)d_ws;
  u16* qkvA = (u16*)ws;                 ws += (size_t)3 * HA * S_ * 32 * 2;
  u16* qB = (u16*)ws;                   ws += (size_t)S_ * NB * 2;
  u16* kB = (u16*)ws;                   ws += (size_t)S_ * NB * 2;
  u16* vB = (u16*)ws;                   ws += (size_t)S_ * NB * 2;
  u16* ctx = (u16*)ws;                  ws += (size_t)S_ * NB * 2;
  u16* oA = (u16*)ws;                   ws += (size_t)S_ * DM * 2;
  float* part = (float*)ws;             ws += (size_t)SPLITK * S_ * DM * 4;

  (void)hipFuncSetAttribute((const void*)k_proj, hipFuncAttributeMaxDynamicSharedMemorySize, G1::LDS);
  (void)hipFuncSetAttribute((const void*)k_out, hipFuncAttributeMaxDynamicSharedMemorySize, G2::LDS);
  (void)hipFuncSetAttribute((const void*)k_attn, hipFuncAttributeMaxDynamicSharedMemorySize, 81920);
  k_proj<<<448, 512, G1::LDS, stream>>>(Ax, Awqkv, Bx, Bckv, Bwq, Bwuk, Bwuv, qkvA, qB, kB, vB);
  k_attn<<<NBLK_B + NBLK_A, 256, 81920, stream>>>(qB, kB, vB, ctx, qkvA, oA);
  k_out<<<40 * SPLITK + 40, 512, G2::LDS, stream>>>(oA, Awo, ctx, Bwo, out, part);
  k_reduce<<<S_ * DM / 4 / 256, 256, 0, stream>>>(part, out);
}
```

```cpp
#include <hip/hip_runtime.h>
#include <hip/hip_bf16.h>
#include <stdint.h>
#include <type_traits>

constexpr int S_ = 2048;
constexpr int DM = 640;
constexpr int HA = 32, DHA = 20;
constexpr int HB = 32, DHB = 128, DC = 512;
constexpr int NB = HB * DHB;

typedef unsigned short u16;
typedef short bf16x8 __attribute__((ext_vector_type(8)));
typedef short s16x4 __attribute__((ext_vector_type(4)));
typedef float f32x16 __attribute__((ext_vector_type(16)));
typedef float f32x4 __attribute__((ext_vector_type(4)));
typedef float f32x2 __attribute__((ext_vector_type(2)));
typedef __bf16 bf16x2 __attribute__((ext_vector_type(2)));
typedef unsigned u32x2 __attribute__((ext_vector_type(2)));
typedef unsigned u32x4 __attribute__((ext_vector_type(4)));
typedef __attribute__((address_space(3))) s16x4 lds_s16x4;
typedef __attribute__((address_space(3))) bf16x8 lds_bf16x8;
typedef __attribute__((address_space(3))) u32x2 lds_u32x2;
typedef __attribute__((address_space(3))) f32x4 lds_f32x4;
typedef __attribute__((address_space(3))) f32x2 lds_f32x2;

#define DI __device__ __forceinline__
#define MFMA32(a, b, c) __builtin_amdgcn_mfma_f32_32x32x16_bf16((a), (b), (c), 0, 0, 0)

DI unsigned cvtpk(float lo, float hi) {
  f32x2 v = {lo, hi};
  bf16x2 b = __builtin_convertvector(v, bf16x2);
  return __builtin_bit_cast(unsigned, b);
}
DI void dma16(const void* sbase, unsigned voff, unsigned lds_addr) {
  unsigned keep;
  asm volatile("s_mov_b32 %0, m0\n\ts_mov_b32 m0, %3\n\ts_nop 0\n\tglobal_load_lds_dwordx4 %1, %2\n\ts_mov_b32 m0, %0"
               : "=&s"(keep) : "v"(voff), "s"(sbase), "s"(lds_addr) : "memory");
}
DI void dma_base(unsigned lds_addr) { asm volatile("s_mov_b32 m0, %0\n\ts_nop 0" :: "s"(lds_addr) : "memory"); }
template <int OFF> DI void dma_piece(const void* sbase, unsigned voff) {
  asm volatile("global_load_lds_dwordx4 %0, %1 offset:%2" :: "v"(voff), "s"(sbase), "i"(OFF) : "memory");
}
template <int OFF, int X> DI void dma_piece_x(const void* sbase, unsigned voff) {
  unsigned t;
  asm volatile("v_xor_b32 %0, %4, %1\n\tglobal_load_lds_dwordx4 %0, %2 offset:%3" : "=&v"(t) : "v"(voff), "s"(sbase), "i"(OFF), "i"(X) : "memory");
}
#define WAIT_VM0() asm volatile("s_waitcnt vmcnt(0)" ::: "memory")
#define BARRIER() do { asm volatile("s_waitcnt lgkmcnt(0)" ::: "memory"); __builtin_amdgcn_s_barrier(); asm volatile("" ::: "memory"); } while (0)

DI bf16x8 lds_read128(unsigned off) { return *(const lds_bf16x8*)(uintptr_t)off; }
DI s16x4 lds_read_tr(unsigned off) { return __builtin_amdgcn_ds_read_tr16_b64_v4i16((lds_s16x4*)(uintptr_t)off); }
DI bf16x8 cat8(s16x4 a, s16x4 b) { return __builtin_shufflevector(a, b, 0, 1, 2, 3, 4, 5, 6, 7); }
DI float xhalf_max(float x) {
  auto r = __builtin_amdgcn_permlane32_swap(__float_as_uint(x), __float_as_uint(x), false, false);
  return fmaxf(__uint_as_float(r[0]), __uint_as_float(r[1]));
}
DI float xhalf_sum(float x) {
  auto r = __builtin_amdgcn_permlane32_swap(__float_as_uint(x), __float_as_uint(x), false, false);
  return __uint_as_float(r[0]) + __uint_as_float(r[1]);
}

template <int BM, int BN, int WM, int WN, int NST_>
struct GCfg {
  static constexpr int NST = NST_;
  static constexpr int NW = WM * WN, TM = BM / WM, TN = BN / WN, MT = TM / 32, NT = TN / 32;
  static constexpr int XB = BM * 128, WROW = BN * 2, WB = 64 * WROW, STAGE = XB + WB;
  static constexpr int XI = BM / 8 / NW;
  static constexpr int WI = WB / 1024 / NW;
  static constexpr int WCH = BN / 8;
  static constexpr int WRPI = 64 / WCH;
  static constexpr int LDS = NST * STAGE;
};

template <class C, class Epi, bool XHEAD = false>
DI void gemm_tile(const u16* __restrict__ X, int ldx, const u16* __restrict__ W, int ldw, int m0, int n0, int ncols,
                  int kbeg, int nk, const Epi& epi) {
  const int tid = threadIdx.x, lane = tid & 63, wid = __builtin_amdgcn_readfirstlane(tid >> 6);
  const int wm = wid / C::WN_, wn = wid % C::WN_;
  const int r = lane & 31, h = lane >> 5;
  unsigned xoff[C::XI], woff[C::WI];
#pragma unroll
  for (int i = 0; i < C::XI; ++i) {
    const int row = 8 * (wid * C::XI + i) + (lane >> 3);
    xoff[i] = (unsigned)(row * ldx + (((lane & 7) ^ ((row >> 1) & 7)) * 8)) * 2u;
  }
#pragma unroll
  for (int i = 0; i < C::WI; ++i) {
    const int k = C::WRPI * (wid * C::WI + i) + lane / C::WCH;
    int col = (((lane % C::WCH) ^ ((k & 3) << 2)) * 8);
    if (n0 + col > ncols - 8) col = ncols - 8 - n0;
    woff[i] = (unsigned)(k * ldw + col) * 2u;
  }
  const u16* xs = XHEAD ? X + (size_t)m0 * ldx : X + (size_t)m0 * ldx + kbeg;
  const int kt0 = kbeg >> 6;
  auto xk = [&](int kt) { return XHEAD ? xs + (size_t)((kt + kt0) >> 1) * S_ * DHB + ((kt + kt0) & 1) * 64 : xs + kt * 64; };
  const u16* ws = W + (size_t)kbeg * ldw + n0;
  auto stage = [&](int buf, int kt) {
    const u16* xb = xk(kt);
    const u16* wb = ws + (size_t)kt * 64 * ldw;
    const unsigned lb = buf * C::STAGE;
#pragma unroll
    for (int i = 0; i < C::XI; ++i) dma16(xb, xoff[i], lb + (wid * C::XI + i) * 1024);
#pragma unroll
    for (int i = 0; i < C::WI; ++i) dma16(wb, woff[i], lb + C::XB + (wid * C::WI + i) * 1024);
  };
  unsigned xa[4];
#pragma unroll
  for (int ks = 0; ks < 4; ++ks) xa[ks] = (wm * C::TM + r) * 128 + (((2 * ks + h) ^ ((r >> 1) & 7)) * 16);
  unsigned wa[C::NT];
  {
    const int q = (lane & 15) >> 2, p = lane & 3, cb = (lane >> 4) & 1;
#pragma unroll
    for (int nt = 0; nt < C::NT; ++nt) {
      const int nb = wn * C::NT + nt;
      wa[nt] = C::XB + (8 * h + q) * C::WROW + ((nb ^ q) * 64) + cb * 32 + p * 8;
    }
  }
  f32x16 acc[C::NT][C::MT];
#pragma unroll
  for (int nt = 0; nt < C::NT; ++nt)
#pragma unroll
    for (int mt = 0; mt < C::MT; ++mt) acc[nt][mt] = f32x16{};

  constexpr int AHEAD = C::NST - 1, PER = C::XI + C::WI;
  stage(0, 0);
  if constexpr (AHEAD == 2) { stage(1, 1); asm volatile("s_waitcnt vmcnt(%0)" :: "i"(PER) : "memory"); }
  else WAIT_VM0();
  BARRIER();
  int cur = 0, nxt = AHEAD;
  for (int kt = 0; kt < nk; ++kt) {
    const unsigned lb = cur * C::STAGE;
    const bool more = kt + AHEAD < nk;
    const u16* xb = xk(kt + AHEAD);
    const u16* wb = ws + (size_t)(kt + AHEAD) * 64 * ldw;
    const unsigned nb = nxt * C::STAGE;
    cur = cur + 1 == C::NST ? 0 : cur + 1;
    nxt = nxt + 1 == C::NST ? 0 : nxt + 1;
#pragma unroll
    for (int ks = 0; ks < 4; ++ks) {
      bf16x8 xf[C::MT], wf[C::NT];
#pragma unroll
      for (int mt = 0; mt < C::MT; ++mt) xf[mt] = lds_read128(lb + xa[ks] + mt * 4096);
#pragma unroll
      for (int nt = 0; nt < C::NT; ++nt)
        wf[nt] = cat8(lds_read_tr(lb + wa[nt] + (16 * ks) * C::WROW), lds_read_tr(lb + wa[nt] + (16 * ks + 4) * C::WROW));
      if (more) {
        constexpr int TOT = C::XI + C::WI;
#pragma unroll
        for (int i = ks * TOT / 4; i < (ks + 1) * TOT / 4; ++i) {
          if (i < C::XI) dma16(xb, xoff[i], nb + (wid * C::XI + i) * 1024);
          else dma16(wb, woff[i - C::XI], nb + C::XB + (wid * C::WI + i - C::XI) * 1024);
        }
      }
#pragma unroll
      for (int nt = 0; nt < C::NT; ++nt)
#pragma unroll
        for (int mt = 0; mt < C::MT; ++mt) acc[nt][mt] = MFMA32(wf[nt], xf[mt], acc[nt][mt]);
    }
    if (AHEAD == 2 && more) asm volatile("s_waitcnt vmcnt(%0)" :: "i"(PER) : "memory");
    else WAIT_VM0();
    BARRIER();
  }
  if constexpr (Epi::DIRECT) {
    f32x4* dst = (f32x4*)epi.out + (size_t)wid * (C::NT * C::MT * 4 * 64) + lane;
#pragma unroll
    for (int nt = 0; nt < C::NT; ++nt)
#pragma unroll
      for (int mt = 0; mt < C::MT; ++mt)
#pragma unroll
        for (int g = 0; g < 4; ++g) {
          f32x4 v = {acc[nt][mt][4 * g], acc[nt][mt][4 * g + 1], acc[nt][mt][4 * g + 2], acc[nt][mt][4 * g + 3]};
          dst[((nt * C::MT + mt) * 4 + g) * 64] = v;
        }
  } else {
    static_assert(C::TN == 64, "staging layout");
    const unsigned sb = wid * (C::TM * 128);
#pragma unroll
    for (int nt = 0; nt < C::NT; ++nt)
#pragma unroll
      for (int g = 0; g < 4; ++g) {
        const int nl = nt * 32 + 8 * g + 4 * h;
        const float sc = epi.scale_of(n0 + wn * C::TN + nl);
        const unsigned a = sb + r * 128 + (((nl >> 3) ^ (r & 7)) * 16) + (nl & 4) * 2;
#pragma unroll
        for (int mt = 0; mt < C::MT; ++mt) {
          u32x2 v = {cvtpk(acc[nt][mt][4 * g] * sc, acc[nt][mt][4 * g + 1] * sc),
                     cvtpk(acc[nt][mt][4 * g + 2] * sc, acc[nt][mt][4 * g + 3] * sc)};
          *(lds_u32x2*)(uintptr_t)(a + mt * 4096) = v;
        }
      }
    epi.template store<C::TM>(sb, m0 + wm * C::TM, n0 + wn * C::TN, lane);
  }
}

template <int BM, int BN, int WM, int WN, int NST>
struct GC : GCfg<BM, BN, WM, WN, NST> { static constexpr int WN_ = WN; };

struct EpiPlain {
  static constexpr bool DIRECT = false, PREFW = false;
  u16* out; int ld; float scale;
  DI float scale_of(int) const { return scale; }
  template <int TM> DI void store(unsigned sb, int mb, int nb, int lane) const {
    const int rr = lane >> 3, c = lane & 7;
    u16* dst = out + (size_t)(mb + rr) * ld + nb + c * 8;
    const unsigned a = sb + rr * 128 + ((c ^ (rr & 7)) * 16);
#pragma unroll
    for (int it = 0; it < TM / 8; ++it) {
      const bf16x8 v = lds_read128(a + it * 1024);
      *(bf16x8*)(dst + (size_t)it * 8 * ld) = v;
    }
  }
};
struct EpiHead {
  static constexpr bool DIRECT = false, PREFW = true;
  u16* out; float scale;
  DI float scale_of(int) const { return scale; }
  template <int TM> DI void store(unsigned sb, int mb, int nb, int lane) const {
    const int rr = lane >> 3, c = lane & 7;
    u16* dst = out + ((size_t)(nb >> 7) * S_ + mb + rr) * DHB + (nb & 127) + c * 8;
    const unsigned a = sb + rr * 128 + ((c ^ (rr & 7)) * 16);
#pragma unroll
    for (int it = 0; it < TM / 8; ++it) {
      const bf16x8 v = lds_read128(a + it * 1024);
      *(bf16x8*)(dst + (size_t)it * 8 * DHB) = v;
    }
  }
};
struct EpiQKVA {
  static constexpr bool DIRECT = false, PREFW = true;
  u16* out; float qscale;
  DI float scale_of(int n) const { return n < DM ? qscale : 1.f; }
  template <int TM> DI void store(unsigned sb, int mb, int nb, int lane) const {
    const int rr = lane >> 4, gq = lane & 15;
    const int n = nb + gq * 4;
    if (n >= 3 * DM) return;
    const int which = n / DM, rem = n - which * DM, head = rem / DHA, dd = rem - head * DHA;
    u16* dst = out + ((size_t)((which * HA + head) * S_ + mb + rr)) * 32;
#pragma unroll 4
    for (int it = 0; it < TM / 4; ++it) {
      const int row = it * 4 + rr;
      const u32x2 v = *(const lds_u32x2*)(uintptr_t)(sb + row * 128 + (((gq >> 1) ^ (row & 7)) * 16) + (gq & 1) * 8);
      u16* d = dst + it * 4 * 32;
      *(u32x2*)(d + dd) = v;
      if (dd == 16) {
        u32x2 z0 = {which == 2 ? 0x3F80u : 0u, 0u};
        u32x4 z1 = {0u, 0u, 0u, 0u};
        *(u32x2*)(d + 20) = z0;
        *(u32x4*)(d + 24) = z1;
      }
    }
  }
};
struct EpiF32 {
  static constexpr bool DIRECT = true, PREFW = false;
  float* out;
};

typedef GC<256, 256, 2, 4, 2> G1;
typedef GC<256, 128, 4, 2, 3> G2;

constexpr float LOG2E = 1.4426950408889634f;

__global__ __launch_bounds__(512, 2) void k_proj(const u16* __restrict__ Ax, const u16* __restrict__ Awqkv,
                                                 const u16* __restrict__ Bx, const u16* __restrict__ Bckv,
                                                 const u16* __restrict__ Bwq, const u16* __restrict__ Bwuk,
                                                 const u16* __restrict__ Bwuv, u16* __restrict__ qkvA,
                                                 u16* __restrict__ qB, u16* __restrict__ kB, u16* __restrict__ vB) {
  const int b = blockIdx.x, xcd = b & 7, idx = b >> 3;
  const int tn = xcd + 8 * (idx >> 3), tm = idx & 7;
  const int m0 = tm * 256;
  if (tn < 8) {
    EpiQKVA e{qkvA, 0.22360679774997896f * LOG2E};
    gemm_tile<G1>(Ax, DM, Awqkv, 3 * DM, m0, tn * 256, 3 * DM, 0, DM / 64, e);
  } else if (tn < 24) {
    EpiHead e{qB, 0.08838834764831845f * LOG2E};
    gemm_tile<G1>(Bx, DM, Bwq, NB, m0, (tn - 8) * 256, NB, 0, DM / 64, e);
  } else {
    const bool isv = tn >= 40;
    EpiHead e{isv ? vB : kB, 1.f};
    gemm_tile<G1>(Bckv, DC, isv ? Bwuv : Bwuk, NB, m0, (tn - (isv ? 40 : 24)) * 256, NB, 0, DC / 64, e);
  }
}

constexpr int SPLITK = 5;
__global__ __launch_bounds__(512, 2) void k_out(const u16* __restrict__ oA, const u16* __restrict__ Awo,
                                                const u16* __restrict__ ctx, const u16* __restrict__ Bwo,
                                                u16* __restrict__ out, float* __restrict__ part) {
  const int b = blockIdx.x;
  if (b < 40 * SPLITK) {
    const int tm = b & 7, rest = b >> 3, tn = rest % 5, sp = rest / 5;
    EpiF32 e{part + ((size_t)sp * 40 + tm * 5 + tn) * (256 * 128)};
    gemm_tile<G2, EpiF32, true>(ctx, DHB, Bwo, DM, tm * 256, tn * 128, DM, sp * 13 * 64, sp == 4 ? 12 : 13, e);
  } else {
    const int t = b - 40 * SPLITK, tm = t & 7, tn = t >> 3;
    EpiPlain e{out, 2 * DM, 1.f};
    gemm_tile<G2>(oA, DM, Awo, DM, tm * 256, tn * 128, DM, 0, DM / 64, e);
  }
}

__global__ __launch_bounds__(256) void k_reduce(const float* __restrict__ part, u16* __restrict__ out) {
  const int i = blockIdx.x * 256 + threadIdx.x;
  const int tile = i >> 13, w = (i >> 10) & 7, j = (i >> 6) & 15, lane = i & 63;
  const int nt = j >> 3, mt = (j >> 2) & 1, g = j & 3;
  const int tm = tile / 5, tn = tile - tm * 5;
  const int m = tm * 256 + (w >> 1) * 64 + mt * 32 + (lane & 31);
  const int n = tn * 128 + (w & 1) * 64 + nt * 32 + 8 * g + 4 * (lane >> 5);
  const f32x4* p = (const f32x4*)part + i;
  f32x4 s = p[0];
#pragma unroll
  for (int sp = 1; sp < SPLITK; ++sp) s += p[(size_t)sp * 40 * 8192];
  u32x2 v = {cvtpk(s[0], s[1]), cvtpk(s[2], s[3])};
  *(u32x2*)(out + (size_t)m * 2 * DM + DM + n) = v;
}

constexpr float THR = 8.f;
DI bf16x8 pack8(const f32x16& x, int s) {
  u32x4 w = {cvtpk(x[8 * s], x[8 * s + 1]), cvtpk(x[8 * s + 2], x[8 * s + 3]), cvtpk(x[8 * s + 4], x[8 * s + 5]),
             cvtpk(x[8 * s + 6], x[8 * s + 7])};
  return __builtin_bit_cast(bf16x8, w);
}
DI f32x16 splat16(float v) {
  f32x16 x;
#pragma unroll
  for (int i = 0; i < 16; ++i) x[i] = v;
  return x;
}
DI float max16(const f32x16& x) {
  float a = fmaxf(fmaxf(x[0], x[1]), x[2]);
#pragma unroll
  for (int i = 3; i < 15; i += 2) a = fmaxf(fmaxf(a, x[i]), x[i + 1]);
  return fmaxf(a, x[15]);
}

#define SBAR() __builtin_amdgcn_sched_barrier(0)
constexpr float BIGSUM = 4096.f;
DI float sum16(const f32x16& x) {
  float a = (x[0] + x[1]) + (x[2] + x[3]), b = (x[4] + x[5]) + (x[6] + x[7]);
  float c = (x[8] + x[9]) + (x[10] + x[11]), d = (x[12] + x[13]) + (x[14] + x[15]);
  return (a + b) + (c + d);
}
DI void exp16(f32x16& x) {
#pragma unroll
  for (int i = 0; i < 16; ++i) x[i] = __builtin_amdgcn_exp2f(x[i]);
}
DI void attnB_item(const u16* __restrict__ Q, const u16* __restrict__ K, const u16* __restrict__ V,
                   u16* __restrict__ ctx, int head, int jitem) {
  const int tid = threadIdx.x, lane = tid & 63, wid8 = __builtin_amdgcn_readfirstlane(tid >> 6);
  const int wid = wid8 & 3, grp = wid8 >> 2;
  const int r = lane & 31, h = lane >> 5;
  constexpr unsigned TB = 16384;
  const unsigned KBUF = grp * 65536, VBUF = KBUF + 32768;
  unsigned koff, voff;
  {
    const int l4 = lane >> 4, c = lane & 15;
    koff = (unsigned)((16 * wid + l4) * DHB + ((c ^ l4) * 8)) * 2u;
    voff = (unsigned)((16 * wid + l4) * DHB + ((c ^ (l4 << 2)) * 8)) * 2u;
  }
  const u16* Kh = K + (size_t)head * S_ * DHB;
  const u16* Vh = V + (size_t)head * S_ * DHB;
  auto stageK = [&](int buf, int i) {
    const u16* g = Kh + (size_t)min(2 * i + grp, 31) * 64 * DHB;
    dma_base(KBUF + buf * TB + wid * 4096);
    dma_piece<0>(g, koff); dma_piece_x<1024, 64>(g, koff); dma_piece_x<2048, 128>(g, koff); dma_piece_x<3072, 192>(g, koff);
  };
  auto stageV = [&](int buf, int i) {
    const u16* g = Vh + (size_t)min(2 * i + grp, 31) * 64 * DHB;
    dma_base(VBUF + buf * TB + wid * 4096);
    dma_piece<0>(g, voff); dma_piece<1024>(g, voff); dma_piece<2048>(g, voff); dma_piece<3072>(g, voff);
  };
  unsigned ka[8], va[4];
#pragma unroll
  for (int ks = 0; ks < 8; ++ks) {
    ka[ks] = KBUF + r * 256 + (((2 * ks + h) ^ (r & 15)) * 16);
    asm volatile("" : "+v"(ka[ks]));
  }
  {
    const int q = (lane & 15) >> 2, p = lane & 3, cb = (lane >> 4) & 1;
#pragma unroll
    for (int db = 0; db < 4; ++db) va[db] = VBUF + (4 * h + q) * 256 + ((db ^ q) * 64) + cb * 32 + p * 8;
  }
  bf16x8 qf[8];
  f32x16 o[4], negm;
  float mcur, lsum;
  auto loadQ = [&](int qb) {
    int lz = threadIdx.x;
    asm volatile("" : "+v"(lz));
    const u16* qp = Q + ((size_t)head * S_ + qb * 128) * DHB + (unsigned)((wid * 32 + (lz & 31)) * DHB + ((lz >> 5) & 1) * 8);
#pragma unroll
    for (int ks = 0; ks < 8; ++ks) qf[ks] = *(const bf16x8*)(qp + ks * 16);
  };
  stageK(0, 0); stageK(1, 1); stageV(0, 0);
  loadQ(15 - jitem);

  auto qk_abs = [&](unsigned off) {
    bf16x8 kf[8];
#pragma unroll
    for (int ks = 0; ks < 8; ++ks) kf[ks] = lds_read128(off + ka[ks]);
    f32x16 s = MFMA32(kf[0], qf[0], f32x16{});
#pragma unroll
    for (int ks = 1; ks < 8; ++ks) s = MFMA32(kf[ks], qf[ks], s);
    return s;
  };
  auto remax = [&](const f32x16& st, bool first, f32x16& s) {
    const float tm = xhalf_max(max16(st));
    const float mnew = first ? tm : fmaxf(tm, mcur);
    const float delta = mnew - mcur;
    const float f = __builtin_amdgcn_exp2f(-fmaxf(delta, 0.f));
    mcur = mnew;
    negm = splat16(-mnew);
#pragma unroll
    for (int i = 0; i < 16; ++i) s[i] = __builtin_amdgcn_exp2f(st[i] - mnew);
#pragma unroll
    for (int db = 0; db < 4; ++db)
#pragma unroll
      for (int i = 0; i < 16; ++i) o[db][i] *= f;
    lsum *= f;
    return delta;
  };
  auto slow = [&](int blk, f32x16& s) {
    int lz = threadIdx.x;
    asm volatile("" : "+v"(lz));
    const u16* kp = Kh + (size_t)blk * 32 * DHB + (unsigned)((lz & 31) * DHB + ((lz >> 5) & 1) * 8);
    f32x16 st = f32x16{};
#pragma unroll
    for (int ks = 0; ks < 8; ++ks) {
      st = MFMA32(*(const bf16x8*)(kp + ks * 16), qf[ks], st);
      asm volatile("" ::: "memory");
      SBAR();
    }
    return remax(st, false, s);
  };
  auto pv = [&](unsigned off, const f32x16& p) {
#pragma unroll
    for (int sx = 0; sx < 2; ++sx) {
      const bf16x8 pf = pack8(p, sx);
#pragma unroll
      for (int db = 0; db < 4; ++db) {
        const bf16x8 vf = cat8(lds_read_tr(off + va[db] + (16 * sx) * 256), lds_read_tr(off + va[db] + (16 * sx + 8) * 256));
        o[db] = MFMA32(vf, pf, o[db]);
      }
    }
  };
#define EXP2(x) x = __builtin_amdgcn_exp2f(x)
#define VRD(dst, off) dst = cat8(lds_read_tr(off), lds_read_tr((off) + 2048))

#pragma unroll 1
  for (int pass = 0; pass < 2; ++pass) {
    const int qb = pass ? jitem : 15 - jitem;
    const int nfull = qb;
    auto init = [&]() {
      float z = 0.f;
      asm volatile("" : "+v"(z));
#pragma unroll
      for (int db = 0; db < 4; ++db) o[db] = splat16(z);
      mcur = z - 1e30f; lsum = z;
    };
#pragma unroll
    for (int ks = 0; ks < 8; ++ks) asm volatile("" : "+v"(qf[ks]));
    WAIT_VM0();
    BARRIER();
    if (nfull == 0) init();
    else {
      init();
      f32x16 sa, sb;
      bf16x8 kg[8];
      float pa;
      {
        const f32x16 st = qk_abs(0);
        const float m = xhalf_max(max16(st));
        mcur = m;
        negm = splat16(-m);
#pragma unroll
        for (int i = 0; i < 8; ++i) sa[i] = __builtin_amdgcn_exp2f(st[i] - m);
#pragma unroll
        for (int i = 8; i < 16; ++i) sa[i] = st[i] - m;
        pa = ((sa[0] + sa[1]) + (sa[2] + sa[3])) + ((sa[4] + sa[5]) + (sa[6] + sa[7]));
#pragma unroll
        for (int ks = 0; ks < 8; ++ks) kg[ks] = lds_read128(ka[ks] + 8192);
      }
#pragma unroll
      for (int ks = 0; ks < 8; ++ks) ka[ks] ^= TB;
      int t = 0;
      do {
        constexpr unsigned vo = 0, kn = 0;
        const unsigned kd = (t & 1) * TB, vn = TB - kd;
        const u16* kbn = Kh + (size_t)min(2 * (t + 2) + grp, 31) * 64 * DHB;
        const u16* vbn = Vh + (size_t)(2 * (t + 1) + grp) * 64 * DHB;
#define DMAK(i) do { if ((i) == 0) { dma_base(KBUF + kd + wid * 4096); dma_piece<0>(kbn, koff); } else dma_piece_x<(i) * 1024, (i) * 64>(kbn, koff); } while (0)
#define DMAV(i) do { if ((i) == 0) dma_base(VBUF + vn + wid * 4096); dma_piece<(i) * 1024>(vbn, voff); } while (0)
        bf16x8 kf[8], vf[8], vg[8];
#define KF(i) kf[i] = lds_read128(kn + ka[i])
#define KG(i) kg[i] = lds_read128(kn + ka[i] + 8192)
        sb = MFMA32(kg[0], qf[0], negm);
        VRD(vf[0], vo + va[0]); DMAV(0);
        EXP2(sa[8]); EXP2(sa[9]);
        SBAR();
        sb = MFMA32(kg[1], qf[1], sb);
        VRD(vf[1], vo + va[1]); DMAV(1);
        EXP2(sa[10]); EXP2(sa[11]);
        SBAR();
        sb = MFMA32(kg[2], qf[2], sb);
        VRD(vf[2], vo + va[2]); DMAV(2);
        EXP2(sa[12]); EXP2(sa[13]);
        float pa2 = (sa[8] + sa[9]) + (sa[10] + sa[11]);
        SBAR();
        sb = MFMA32(kg[3], qf[3], sb);
        VRD(vf[3], vo + va[3]); DMAV(3);
        EXP2(sa[14]); EXP2(sa[15]);
        pa += sa[12] + sa[13];
        SBAR();
        sb = MFMA32(kg[4], qf[4], sb);
        DMAK(0);
        pa2 += sa[14] + sa[15];
        pa += pa2;
        if (__any(!(pa <= BIGSUM))) {
          const float delta = slow(4 * t + 2 * grp, sa);
#pragma unroll
          for (int i = 0; i < 16; ++i) sb[i] -= delta;
          pa = sum16(sa);
        }
        sb = MFMA32(kg[5], qf[5], sb);
        VRD(vf[4], vo + va[0] + 4096); VRD(vf[5], vo + va[1] + 4096);
        const bf16x8 p0 = pack8(sa, 0);
        lsum += pa;
        SBAR();
        sb = MFMA32(kg[6], qf[6], sb);
        VRD(vf[6], vo + va[2] + 4096); DMAK(1);
        const bf16x8 p1 = pack8(sa, 1);
        SBAR();
        sb = MFMA32(kg[7], qf[7], sb);
        VRD(vf[7], vo + va[3] + 4096); DMAK(2);
        SBAR();
        o[0] = MFMA32(vf[0], p0, o[0]);
        KF(0); DMAK(3);
        SBAR();
        o[1] = MFMA32(vf[1], p0, o[1]);
        KF(1);
        EXP2(sb[0]); EXP2(sb[1]); EXP2(sb[2]);
        SBAR();
        o[2] = MFMA32(vf[2], p0, o[2]);
        KF(2);
        EXP2(sb[3]); EXP2(sb[4]);
        SBAR();
        o[3] = MFMA32(vf[3], p0, o[3]);
        KF(3);
        EXP2(sb[5]); EXP2(sb[6]);
        float pb = (sb[0] + sb[1]) + (sb[2] + sb[3]);
        SBAR();
        o[0] = MFMA32(vf[4], p1, o[0]);
        KF(4);
        EXP2(sb[7]); EXP2(sb[8]);
        SBAR();
        o[1] = MFMA32(vf[5], p1, o[1]);
        KF(5);
        EXP2(sb[9]); EXP2(sb[10]);
        float pb2 = (sb[4] + sb[5]) + (sb[6] + sb[7]);
        SBAR();
        o[2] = MFMA32(vf[6], p1, o[2]);
        KF(6);
        EXP2(sb[11]); EXP2(sb[12]);
        SBAR();
        o[3] = MFMA32(vf[7], p1, o[3]);
        KF(7);
        EXP2(sb[13]); EXP2(sb[14]);
        pb += (sb[8] + sb[9]) + sb[10];
        SBAR();
        sa = MFMA32(kf[0], qf[0], negm);
        VRD(vg[0], vo + va[0] + 8192);
        EXP2(sb[15]);
        pb2 += (sb[11] + sb[12]) + sb[13];
        SBAR();
        sa = MFMA32(kf[1], qf[1], sa);
        VRD(vg[1], vo + va[1] + 8192);
        pb += sb[14] + sb[15];
        pb += pb2;
        if (__any(!(pb <= BIGSUM))) {
          const float delta = slow(4 * t + 2 * grp + 1, sb);
#pragma unroll
          for (int i = 0; i < 16; ++i) sa[i] -= delta;
          pb = sum16(sb);
        }
        sa = MFMA32(kf[2], qf[2], sa);
        VRD(vg[2], vo + va[2] + 8192);
        const bf16x8 r0 = pack8(sb, 0);
        lsum += pb;
        SBAR();
        sa = MFMA32(kf[3], qf[3], sa);
        VRD(vg[3], vo + va[3] + 8192);
        const bf16x8 r1 = pack8(sb, 1);
        SBAR();
        sa = MFMA32(kf[4], qf[4], sa);
        VRD(vg[4], vo + va[0] + 12288);
        SBAR();
        sa = MFMA32(kf[5], qf[5], sa);
        VRD(vg[5], vo + va[1] + 12288);
        SBAR();
        sa = MFMA32(kf[6], qf[6], sa);
        VRD(vg[6], vo + va[2] + 12288);
        SBAR();
        sa = MFMA32(kf[7], qf[7], sa);
        VRD(vg[7], vo + va[3] + 12288);
        SBAR();
        o[0] = MFMA32(vg[0], r0, o[0]);
        KG(0);
        SBAR();
        o[1] = MFMA32(vg[1], r0, o[1]);
        KG(1); EXP2(sa[0]); EXP2(sa[1]);
        SBAR();
        o[2] = MFMA32(vg[2], r0, o[2]);
        KG(2); EXP2(sa[2]); EXP2(sa[3]);
        SBAR();
        o[3] = MFMA32(vg[3], r0, o[3]);
        KG(3); EXP2(sa[4]); EXP2(sa[5]);
        SBAR();
        o[0] = MFMA32(vg[4], r1, o[0]);
        KG(4); EXP2(sa[6]); EXP2(sa[7]);
        pa = (sa[0] + sa[1]) + (sa[2] + sa[3]);
        SBAR();
        o[1] = MFMA32(vg[5], r1, o[1]);
        KG(5);
        pa += (sa[4] + sa[5]) + (sa[6] + sa[7]);
        SBAR();
        o[2] = MFMA32(vg[6], r1, o[2]);
        KG(6); KG(7);
        SBAR();
        o[3] = MFMA32(vg[7], r1, o[3]);
#undef KF
#undef KG
#pragma unroll
        for (int ks = 0; ks < 8; ++ks) ka[ks] ^= TB;
#pragma unroll
        for (int db = 0; db < 4; ++db) va[db] ^= TB;
        WAIT_VM0();
        BARRIER();
#undef DMAK
#undef DMAV
      } while (++t < nfull);
#pragma unroll
      for (int ks = 0; ks < 8; ++ks) ka[ks] ^= TB;
    }
    {
      constexpr unsigned db_ = 0;
#pragma unroll 1
      for (int jj = 0; jj < 2; ++jj) {
        const int j = 2 * grp + jj;
        if (j <= wid) {
          f32x16 st = qk_abs(db_ + jj * 8192), s;
          if (j == wid) {
            int lz = threadIdx.x;
            asm volatile("" : "+v"(lz));
            const int dq = (lz & 31) - 4 * ((lz >> 5) & 1);
#pragma unroll
            for (int i = 0; i < 16; ++i) {
              const int kr = (i & 3) + 8 * (i >> 2);
              if (kr > dq) st[i] = -__builtin_inff();
            }
          }
          remax(st, false, s);
          lsum += sum16(s);
          pv(db_ + jj * 32 * 256, s);
        }
      }
      BARRIER();
      if (qb & 1) {
#pragma unroll
        for (int ks = 0; ks < 8; ++ks) ka[ks] ^= TB;
#pragma unroll
        for (int db = 0; db < 4; ++db) va[db] ^= TB;
      }
    }
    if (pass == 0) { stageK(0, 0); stageV(0, 0); }
    {
      int lz = threadIdx.x;
      asm volatile("" : "+v"(lz));
      const int lane = lz & 63, r = lz & 31, h = (lz >> 5) & 1;
      const unsigned slot = wid < 2 ? 49152 + wid * 65536 : 131072 + (wid - 2) * 16384;
      const unsigned mine = slot + grp * 8192, theirs = slot + (grp ^ 1) * 8192;
      const float lt = xhalf_sum(lsum);
      if (grp) {
        const f32x16 t0 = o[0], t1 = o[1];
        o[0] = o[2]; o[1] = o[3]; o[2] = t0; o[3] = t1;
      }
#pragma unroll
      for (int q = 0; q < 8; ++q) {
        const f32x16& x = o[2 + (q >> 2)];
        f32x4 v = {x[4 * (q & 3)], x[4 * (q & 3) + 1], x[4 * (q & 3) + 2], x[4 * (q & 3) + 3]};
        *(lds_f32x4*)(uintptr_t)(mine + q * 1024 + lane * 16) = v;
      }
      {
        f32x2 ml = {mcur, lt};
        *(lds_f32x2*)(uintptr_t)((grp ^ 1) * 65536 + TB + wid * 4096 + lane * 8) = ml;
      }
      BARRIER();
      const f32x2 ml2 = *(const lds_f32x2*)(uintptr_t)(KBUF + TB + wid * 4096 + lane * 8);
      const float mm = fmaxf(mcur, ml2[0]);
      const float f0 = __builtin_amdgcn_exp2f(mcur - mm), f1 = __builtin_amdgcn_exp2f(ml2[0] - mm);
      const float inv = 1.f / (lt * f0 + ml2[1] * f1);
      const float c0 = f0 * inv, c1 = f1 * inv;
      u32x2 w[8];
#pragma unroll
      for (int q = 0; q < 8; ++q) {
        const f32x4 y = *(const lds_f32x4*)(uintptr_t)(theirs + q * 1024 + lane * 16);
        const f32x16& x = o[q >> 2];
        w[q][0] = cvtpk(x[4 * (q & 3)] * c0 + y[0] * c1, x[4 * (q & 3) + 1] * c0 + y[1] * c1);
        w[q][1] = cvtpk(x[4 * (q & 3) + 2] * c0 + y[2] * c1, x[4 * (q & 3) + 3] * c0 + y[3] * c1);
      }
      if (pass == 0) stageK(1, 1);
#pragma unroll
      for (int q = 0; q < 8; ++q) *(lds_u32x2*)(uintptr_t)(theirs + r * 128 + ((q ^ (r & 7)) * 16) + h * 8) = w[q];
      u16* op = ctx + ((size_t)head * S_ + qb * 128 + wid * 32 + (lane >> 3)) * DHB + (lane & 7) * 8 + grp * 64;
#pragma unroll
      for (int it = 0; it < 4; ++it) {
        const int rr = it * 8 + (lane >> 3);
        const bf16x8 v = lds_read128(theirs + rr * 128 + (((lane & 7) ^ (rr & 7)) * 16));
        *(bf16x8*)(op + (size_t)it * 8 * DHB) = v;
      }
    }
    if (pass == 0) loadQ(jitem);
  }
#undef EXP2
#undef VRD
}

template <int QT>
DI void attnA_block(const u16* __restrict__ qkv, u16* __restrict__ oA, int head, int qb) {
  const int tid = threadIdx.x, lane = tid & 63, wid = __builtin_amdgcn_readfirstlane(tid >> 6);
  const int r = lane & 31, h = lane >> 5;
  const int q0 = qb * (256 * QT) + wid * (32 * QT);
  constexpr unsigned KBUF = 0, VBUF = 16384, TB = 8192;
  const u16* Qh = qkv + (size_t)(0 * HA + head) * S_ * 32;
  const u16* Kh = qkv + (size_t)(1 * HA + head) * S_ * 32;
  const u16* Vh = qkv + (size_t)(2 * HA + head) * S_ * 32;
  unsigned koff[1], voff[1];
  {
    const int row = 16 * wid + (lane >> 2), c = lane & 3;
    koff[0] = (unsigned)(row * 64 + ((c ^ ((row >> 2) & 3)) * 16));
    voff[0] = (unsigned)(row * 64 + c * 16);
  }
  auto stage = [&](int buf, int t) {
    const u16* kb = Kh + (size_t)t * 128 * 32;
    const u16* vb = Vh + (size_t)t * 128 * 32;
    dma16(kb, koff[0], KBUF + buf * TB + wid * 1024);
    dma16(vb, voff[0], VBUF + buf * TB + wid * 1024);
  };
  stage(0, 0);
  bf16x8 qf[QT][2];
#pragma unroll
  for (int qt = 0; qt < QT; ++qt)
#pragma unroll
    for (int ks = 0; ks < 2; ++ks) qf[qt][ks] = *(const bf16x8*)(Qh + (size_t)(q0 + 32 * qt + r) * 32 + ks * 16 + h * 8);
  unsigned ka[2], va;
#pragma unroll
  for (int ks = 0; ks < 2; ++ks) ka[ks] = KBUF + r * 64 + (((2 * ks + h) ^ ((r >> 2) & 3)) * 16);
  {
    const int q = (lane & 15) >> 2, p = lane & 3, cb = (lane >> 4) & 1;
    va = VBUF + (4 * h + q) * 64 + cb * 32 + p * 8;
  }
  f32x16 o[QT], negm[QT];
  float mcur[QT];
#pragma unroll
  for (int qt = 0; qt < QT; ++qt) { o[qt] = f32x16{}; negm[qt] = f32x16{}; mcur[qt] = 0.f; }
#pragma unroll
  for (int qt = 0; qt < QT; ++qt) { asm volatile("" : "+v"(qf[qt][0])); asm volatile("" : "+v"(qf[qt][1])); }
  WAIT_VM0();
  BARRIER();
  constexpr int NKT = S_ / 128;
  for (int t = 0; t < NKT; ++t) {
    const unsigned bo = (t & 1) * TB;
    if (t + 1 < NKT) stage((t + 1) & 1, t + 1);
    f32x16 s[QT][4];
#pragma unroll
    for (int kb = 0; kb < 4; ++kb) {
      const bf16x8 k0 = lds_read128(bo + ka[0] + kb * 2048), k1 = lds_read128(bo + ka[1] + kb * 2048);
#pragma unroll
      for (int qt = 0; qt < QT; ++qt) {
        s[qt][kb] = MFMA32(k0, qf[qt][0], negm[qt]);
        s[qt][kb] = MFMA32(k1, qf[qt][1], s[qt][kb]);
      }
    }
    float tm[QT];
    bool need = t == 0;
#pragma unroll
    for (int qt = 0; qt < QT; ++qt) {
      tm[qt] = xhalf_max(fmaxf(fmaxf(max16(s[qt][0]), max16(s[qt][1])), fmaxf(max16(s[qt][2]), max16(s[qt][3]))));
      need = need || tm[qt] > THR;
    }
    if (__any(need)) {
#pragma unroll
      for (int qt = 0; qt < QT; ++qt) {
        const float dpos = fmaxf(tm[qt], 0.f);
        const float delta = t == 0 ? tm[qt] : dpos;
        const float f = __builtin_amdgcn_exp2f(-dpos);
        mcur[qt] += delta;
        negm[qt] = splat16(-mcur[qt]);
#pragma unroll
        for (int kb = 0; kb < 4; ++kb)
#pragma unroll
          for (int i = 0; i < 16; ++i) s[qt][kb][i] -= delta;
#pragma unroll
        for (int i = 0; i < 16; ++i) o[qt][i] *= f;
      }
    }
#pragma unroll
    for (int kb = 0; kb < 4; ++kb) {
#pragma unroll
      for (int sx = 0; sx < 2; ++sx) {
        const bf16x8 vf = cat8(lds_read_tr(bo + va + (32 * kb + 16 * sx) * 64), lds_read_tr(bo + va + (32 * kb + 16 * sx + 8) * 64));
#pragma unroll
        for (int qt = 0; qt < QT; ++qt) {
#pragma unroll
          for (int i = 0; i < 8; ++i) s[qt][kb][8 * sx + i] = __builtin_amdgcn_exp2f(s[qt][kb][8 * sx + i]);
          o[qt] = MFMA32(vf, pack8(s[qt][kb], sx), o[qt]);
        }
      }
    }
    WAIT_VM0();
    BARRIER();
  }
#pragma unroll
  for (int qt = 0; qt < QT; ++qt) {
    const float l = __shfl(o[qt][8], r + 32);
    const float inv = 1.f / l;
    u16* op = oA + (size_t)(q0 + 32 * qt + r) * DM + head * DHA + 4 * h;
#pragma unroll
    for (int g = 0; g < 3; ++g) {
      if (g < 2 || h == 0) {
        u32x2 v = {cvtpk(o[qt][4 * g] * inv, o[qt][4 * g + 1] * inv), cvtpk(o[qt][4 * g + 2] * inv, o[qt][4 * g + 3] * inv)};
        *(u32x2*)(op + 8 * g) = v;
      }
    }
  }
}

constexpr int AQT = 1;
constexpr int NBLK_B = HB * 8, NBLK_A = HA * (S_ / (256 * AQT));
__global__ __launch_bounds__(512, 2) void k_attn(const u16* __restrict__ qB, const u16* __restrict__ kB,
                                                 const u16* __restrict__ vB, u16* __restrict__ ctx,
                                                 const u16* __restrict__ qkvA, u16* __restrict__ oA) {
  const int b = blockIdx.x;
  if (b < NBLK_B) {
    const int head = (b & 7) + 8 * (b >> 6), j = (b >> 3) & 7;
    attnB_item(qB, kB, vB, ctx, head, j);
  } else {
    const int a = b - NBLK_B;
    constexpr int PER = S_ / (256 * AQT);
    const int head = (a & 7) + 8 * (a / (8 * PER)), qb = (a >> 3) % PER;
    attnA_block<AQT>(qkvA, oA, head, qb);
  }
}

extern "C" void kernel_launch(void* const* d_in, const int* in_sizes, int n_in, void* d_out, int out_size, void* d_ws,
                              size_t ws_size, hipStream_t stream) {
  const u16* Ax = (const u16*)d_in[0];
  const u16* Awqkv = (const u16*)d_in[1];
  const u16* Awo = (const u16*)d_in[2];
  const u16* Bx = (const u16*)d_in[3];
  const u16* Bckv = (const u16*)d_in[4];
  const u16* Bwq = (const u16*)d_in[5];
  const u16* Bwuk = (const u16*)d_in[6];
  const u16* Bwuv = (const u16*)d_in[7];
  const u16* Bwo = (const u16*)d_in[8];
  u16* out = (u16*)d_out;
  char* ws = (char*)d_ws;
  u16* qkvA = (u16*)ws;                 ws += (size_t)3 * HA * S_ * 32 * 2;
  u16* qB = (u16*)ws;                   ws += (size_t)S_ * NB * 2;
  u16* kB = (u16*)ws;                   ws += (size_t)S_ * NB * 2;
  u16* vB = (u16*)ws;                   ws += (size_t)S_ * NB * 2;
  u16* ctx = (u16*)ws;                  ws += (size_t)S_ * NB * 2;
  u16* oA = (u16*)ws;                   ws += (size_t)S_ * DM * 2;
  float* part = (float*)ws;             ws += (size_t)SPLITK * S_ * DM * 4;

  (void)hipFuncSetAttribute((const void*)k_proj, hipFuncAttributeMaxDynamicSharedMemorySize, G1::LDS);
  (void)hipFuncSetAttribute((const void*)k_out, hipFuncAttributeMaxDynamicSharedMemorySize, G2::LDS);
  (void)hipFuncSetAttribute((const void*)k_attn, hipFuncAttributeMaxDynamicSharedMemorySize, 163840);
  k_proj<<<448, 512, G1::LDS, stream>>>(Ax, Awqkv, Bx, Bckv, Bwq, Bwuk, Bwuv, qkvA, qB, kB, vB);
  k_attn<<<NBLK_B + NBLK_A, 512, 163840, stream>>>(qB, kB, vB, ctx, qkvA, oA);
  k_out<<<40 * SPLITK + 40, 512, G2::LDS, stream>>>(oA, Awo, ctx, Bwo, out, part);
  k_reduce<<<S_ * DM / 4 / 256, 256, 0, stream>>>(part, out);
}
```

```cpp
#include <hip/hip_runtime.h>
#include <hip/hip_bf16.h>
#include <stdint.h>
#include <type_traits>

constexpr int S_ = 2048;
constexpr int DM = 640;
constexpr int HA = 32, DHA = 20;
constexpr int HB = 32, DHB = 128, DC = 512;
constexpr int NB = HB * DHB;

typedef unsigned short u16;
typedef short bf16x8 __attribute__((ext_vector_type(8)));
typedef short s16x4 __attribute__((ext_vector_type(4)));
typedef float f32x16 __attribute__((ext_vector_type(16)));
typedef float f32x4 __attribute__((ext_vector_type(4)));
typedef float f32x2 __attribute__((ext_vector_type(2)));
typedef __bf16 bf16x2 __attribute__((ext_vector_type(2)));
typedef unsigned u32x2 __attribute__((ext_vector_type(2)));
typedef unsigned u32x4 __attribute__((ext_vector_type(4)));
typedef __attribute__((address_space(3))) s16x4 lds_s16x4;
typedef __attribute__((address_space(3))) bf16x8 lds_bf16x8;
typedef __attribute__((address_space(3))) u32x2 lds_u32x2;

#define DI __device__ __forceinline__
#define MFMA32(a, b, c) __builtin_amdgcn_mfma_f32_32x32x16_bf16((a), (b), (c), 0, 0, 0)

DI unsigned cvtpk(float lo, float hi) {
  f32x2 v = {lo, hi};
  bf16x2 b = __builtin_convertvector(v, bf16x2);
  return __builtin_bit_cast(unsigned, b);
}
DI void dma16(const void* sbase, unsigned voff, unsigned lds_addr) {
  unsigned keep;
  asm volatile("s_mov_b32 %0, m0\n\ts_mov_b32 m0, %3\n\ts_nop 0\n\tglobal_load_lds_dwordx4 %1, %2\n\ts_mov_b32 m0, %0"
               : "=&s"(keep) : "v"(voff), "s"(sbase), "s"(lds_addr) : "memory");
}
DI void dma_base(unsigned lds_addr) { asm volatile("s_mov_b32 m0, %0\n\ts_nop 0" :: "s"(lds_addr) : "memory"); }
template <int OFF> DI void dma_piece(const void* sbase, unsigned voff) {
  asm volatile("global_load_lds_dwordx4 %0, %1 offset:%2" :: "v"(voff), "s"(sbase), "i"(OFF) : "memory");
}
template <int OFF, int X> DI void dma_piece_x(const void* sbase, unsigned voff) {
  unsigned t;
  asm volatile("v_xor_b32 %0, %4, %1\n\tglobal_load_lds_dwordx4 %0, %2 offset:%3" : "=&v"(t) : "v"(voff), "s"(sbase), "i"(OFF), "i"(X) : "memory");
}
#define WAIT_VM0() asm volatile("s_waitcnt vmcnt(0)" ::: "memory")
#define BARRIER() do { asm volatile("s_waitcnt lgkmcnt(0)" ::: "memory"); __builtin_amdgcn_s_barrier(); asm volatile("" ::: "memory"); } while (0)

DI bf16x8 lds_read128(unsigned off) { return *(const lds_bf16x8*)(uintptr_t)off; }
DI s16x4 lds_read_tr(unsigned off) { return __builtin_amdgcn_ds_read_tr16_b64_v4i16((lds_s16x4*)(uintptr_t)off); }
DI bf16x8 cat8(s16x4 a, s16x4 b) { return __builtin_shufflevector(a, b, 0, 1, 2, 3, 4, 5, 6, 7); }
DI float xhalf_max(float x) {
  auto r = __builtin_amdgcn_permlane32_swap(__float_as_uint(x), __float_as_uint(x), false, false);
  return fmaxf(__uint_as_float(r[0]), __uint_as_float(r[1]));
}
DI float xhalf_sum(float x) {
  auto r = __builtin_amdgcn_permlane32_swap(__float_as_uint(x), __float_as_uint(x), false, false);
  return __uint_as_float(r[0]) + __uint_as_float(r[1]);
}

template <int BM, int BN, int WM, int WN, int NST_>
struct GCfg {
  static constexpr int NST = NST_;
  static constexpr int NW = WM * WN, TM = BM / WM, TN = BN / WN, MT = TM / 32, NT = TN / 32;
  static constexpr int XB = BM * 128, WROW = BN * 2, WB = 64 * WROW, STAGE = XB + WB;
  static constexpr int XI = BM / 8 / NW;
  static constexpr int WI = WB / 1024 / NW;
  static constexpr int WCH = BN / 8;
  static constexpr int WRPI = 64 / WCH;
  static constexpr int LDS = NST * STAGE;
};

template <class C, class Epi, bool XHEAD = false>
DI void gemm_tile(const u16* __restrict__ X, int ldx, const u16* __restrict__ W, int ldw, int m0, int n0, int ncols,
                  int kbeg, int nk, const Epi& epi) {
  const int tid = threadIdx.x, lane = tid & 63, wid = __builtin_amdgcn_readfirstlane(tid >> 6);
  const int wm = wid / C::WN_, wn = wid % C::WN_;
  const int r = lane & 31, h = lane >> 5;
  unsigned xoff[C::XI], woff[C::WI];
#pragma unroll
  for (int i = 0; i < C::XI; ++i) {
    const int row = 8 * (wid * C::XI + i) + (lane >> 3);
    xoff[i] = (unsigned)(row * ldx + (((lane & 7) ^ ((row >> 1) & 7)) * 8)) * 2u;
  }
#pragma unroll
  for (int i = 0; i < C::WI; ++i) {
    const int k = C::WRPI * (wid * C::WI + i) + lane / C::WCH;
    int col = (((lane % C::WCH) ^ ((k & 3) << 2)) * 8);
    if (n0 + col > ncols - 8) col = ncols - 8 - n0;
    woff[i] = (unsigned)(k * ldw + col) * 2u;
  }
  const u16* xs = XHEAD ? X + (size_t)m0 * ldx : X + (size_t)m0 * ldx + kbeg;
  const int kt0 = kbeg >> 6;
  auto xk = [&](int kt) { return XHEAD ? xs + (size_t)((kt + kt0) >> 1) * S_ * DHB + ((kt + kt0) & 1) * 64 : xs + kt * 64; };
  const u16* ws = W + (size_t)kbeg * ldw + n0;
  auto stage = [&](int buf, int kt) {
    const u16* xb = xk(kt);
    const u16* wb = ws + (size_t)kt * 64 * ldw;
    const unsigned lb = buf * C::STAGE;
#pragma unroll
    for (int i = 0; i < C::XI; ++i) dma16(xb, xoff[i], lb + (wid * C::XI + i) * 1024);
#pragma unroll
    for (int i = 0; i < C::WI; ++i) dma16(wb, woff[i], lb + C::XB + (wid * C::WI + i) * 1024);
  };
  unsigned xa[4];
#pragma unroll
  for (int ks = 0; ks < 4; ++ks) xa[ks] = (wm * C::TM + r) * 128 + (((2 * ks + h) ^ ((r >> 1) & 7)) * 16);
  unsigned wa[C::NT];
  {
    const int q = (lane & 15) >> 2, p = lane & 3, cb = (lane >> 4) & 1;
#pragma unroll
    for (int nt = 0; nt < C::NT; ++nt) {
      const int nb = wn * C::NT + nt;
      wa[nt] = C::XB + (8 * h + q) * C::WROW + ((nb ^ q) * 64) + cb * 32 + p * 8;
    }
  }
  f32x16 acc[C::NT][C::MT];
#pragma unroll
  for (int nt = 0; nt < C::NT; ++nt)
#pragma unroll
    for (int mt = 0; mt < C::MT; ++mt) acc[nt][mt] = f32x16{};

  constexpr int AHEAD = C::NST - 1, PER = C::XI + C::WI;
  stage(0, 0);
  if constexpr (AHEAD == 2) { stage(1, 1); asm volatile("s_waitcnt vmcnt(%0)" :: "i"(PER) : "memory"); }
  else WAIT_VM0();
  BARRIER();
  int cur = 0, nxt = AHEAD;
  for (int kt = 0; kt < nk; ++kt) {
    const unsigned lb = cur * C::STAGE;
    const bool more = kt + AHEAD < nk;
    const u16* xb = xk(kt + AHEAD);
    const u16* wb = ws + (size_t)(kt + AHEAD) * 64 * ldw;
    const unsigned nb = nxt * C::STAGE;
    cur = cur + 1 == C::NST ? 0 : cur + 1;
    nxt = nxt + 1 == C::NST ? 0 : nxt + 1;
#pragma unroll
    for (int ks = 0; ks < 4; ++ks) {
      bf16x8 xf[C::MT], wf[C::NT];
#pragma unroll
      for (int mt = 0; mt < C::MT; ++mt) xf[mt] = lds_read128(lb + xa[ks] + mt * 4096);
#pragma unroll
      for (int nt = 0; nt < C::NT; ++nt)
        wf[nt] = cat8(lds_read_tr(lb + wa[nt] + (16 * ks) * C::WROW), lds_read_tr(lb + wa[nt] + (16 * ks + 4) * C::WROW));
      if (more) {
        constexpr int TOT = C::XI + C::WI;
#pragma unroll
        for (int i = ks * TOT / 4; i < (ks + 1) * TOT / 4; ++i) {
          if (i < C::XI) dma16(xb, xoff[i], nb + (wid * C::XI + i) * 1024);
          else dma16(wb, woff[i - C::XI], nb + C::XB + (wid * C::WI + i - C::XI) * 1024);
        }
      }
#pragma unroll
      for (int nt = 0; nt < C::NT; ++nt)
#pragma unroll
        for (int mt = 0; mt < C::MT; ++mt) acc[nt][mt] = MFMA32(wf[nt], xf[mt], acc[nt][mt]);
    }
    if (AHEAD == 2 && more) asm volatile("s_waitcnt vmcnt(%0)" :: "i"(PER) : "memory");
    else WAIT_VM0();
    BARRIER();
  }
  if constexpr (Epi::DIRECT) {
    f32x4* dst = (f32x4*)epi.out + (size_t)wid * (C::NT * C::MT * 4 * 64) + lane;
#pragma unroll
    for (int nt = 0; nt < C::NT; ++nt)
#pragma unroll
      for (int mt = 0; mt < C::MT; ++mt)
#pragma unroll
        for (int g = 0; g < 4; ++g) {
          f32x4 v = {acc[nt][mt][4 * g], acc[nt][mt][4 * g + 1], acc[nt][mt][4 * g + 2], acc[nt][mt][4 * g + 3]};
          dst[((nt * C::MT + mt) * 4 + g) * 64] = v;
        }
  } else {
    static_assert(C::TN == 64, "staging layout");
    const unsigned sb = wid * (C::TM * 128);
#pragma unroll
    for (int nt = 0; nt < C::NT; ++nt)
#pragma unroll
      for (int g = 0; g < 4; ++g) {
        const int nl = nt * 32 + 8 * g + 4 * h;
        const float sc = epi.scale_of(n0 + wn * C::TN + nl);
        const unsigned a = sb + r * 128 + (((nl >> 3) ^ (r & 7)) * 16) + (nl & 4) * 2;
#pragma unroll
        for (int mt = 0; mt < C::MT; ++mt) {
          u32x2 v = {cvtpk(acc[nt][mt][4 * g] * sc, acc[nt][mt][4 * g + 1] * sc),
                     cvtpk(acc[nt][mt][4 * g + 2] * sc, acc[nt][mt][4 * g + 3] * sc)};
          *(lds_u32x2*)(uintptr_t)(a + mt * 4096) = v;
        }
      }
    epi.template store<C::TM>(sb, m0 + wm * C::TM, n0 + wn * C::TN, lane);
  }
}

template <int BM, int BN, int WM, int WN, int NST>
struct GC : GCfg<BM, BN, WM, WN, NST> { static constexpr int WN_ = WN; };

struct EpiPlain {
  static constexpr bool DIRECT = false, PREFW = false;
  u16* out; int ld; float scale;
  DI float scale_of(int) const { return scale; }
  template <int TM> DI void store(unsigned sb, int mb, int nb, int lane) const {
    const int rr = lane >> 3, c = lane & 7;
    u16* dst = out + (size_t)(mb + rr) * ld + nb + c * 8;
    const unsigned a = sb + rr * 128 + ((c ^ (rr & 7)) * 16);
#pragma unroll
    for (int it = 0; it < TM / 8; ++it) {
      const bf16x8 v = lds_read128(a + it * 1024);
      *(bf16x8*)(dst + (size_t)it * 8 * ld) = v;
    }
  }
};
struct EpiHead {
  static constexpr bool DIRECT = false, PREFW = true;
  u16* out; float scale;
  DI float scale_of(int) const { return scale; }
  template <int TM> DI void store(unsigned sb, int mb, int nb, int lane) const {
    const int rr = lane >> 3, c = lane & 7;
    u16* dst = out + ((size_t)(nb >> 7) * S_ + mb + rr) * DHB + (nb & 127) + c * 8;
    const unsigned a = sb + rr * 128 + ((c ^ (rr & 7)) * 16);
#pragma unroll
    for (int it = 0; it < TM / 8; ++it) {
      const bf16x8 v = lds_read128(a + it * 1024);
      *(bf16x8*)(dst + (size_t)it * 8 * DHB) = v;
    }
  }
};
struct EpiQKVA {
  static constexpr bool DIRECT = false, PREFW = true;
  u16* out; float qscale;
  DI float scale_of(int n) const { return n < DM ? qscale : 1.f; }
  template <int TM> DI void store(unsigned sb, int mb, int nb, int lane) const {
    const int rr = lane >> 4, gq = lane & 15;
    const int n = nb + gq * 4;
    if (n >= 3 * DM) return;
    const int which = n / DM, rem = n - which * DM, head = rem / DHA, dd = rem - head * DHA;
    u16* dst = out + ((size_t)((which * HA + head) * S_ + mb + rr)) * 32;
#pragma unroll 4
    for (int it = 0; it < TM / 4; ++it) {
      const int row = it * 4 + rr;
      const u32x2 v = *(const lds_u32x2*)(uintptr_t)(sb + row * 128 + (((gq >> 1) ^ (row & 7)) * 16) + (gq & 1) * 8);
      u16* d = dst + it * 4 * 32;
      *(u32x2*)(d + dd) = v;
      if (dd == 16) {
        u32x2 z0 = {which == 2 ? 0x3F80u : 0u, 0u};
        u32x4 z1 = {0u, 0u, 0u, 0u};
        *(u32x2*)(d + 20) = z0;
        *(u32x4*)(d + 24) = z1;
      }
    }
  }
};
struct EpiF32 {
  static constexpr bool DIRECT = true, PREFW = false;
  float* out;
};

typedef GC<256, 256, 2, 4, 2> G1;
typedef GC<256, 128, 4, 2, 3> G2;

constexpr float LOG2E = 1.4426950408889634f;

__global__ __launch_bounds__(512, 2) void k_proj(const u16* __restrict__ Ax, const u16* __restrict__ Awqkv,
                                                 const u16* __restrict__ Bx, const u16* __restrict__ Bckv,
                                                 const u16* __restrict__ Bwq, const u16* __restrict__ Bwuk,
                                                 const u16* __restrict__ Bwuv, u16* __restrict__ qkvA,
                                                 u16* __restrict__ qB, u16* __restrict__ kB, u16* __restrict__ vB) {
  const int b = blockIdx.x, xcd = b & 7, idx = b >> 3;
  const int tn = xcd + 8 * (idx >> 3), tm = idx & 7;
  const int m0 = tm * 256;
  if (tn < 8) {
    EpiQKVA e{qkvA, 0.22360679774997896f * LOG2E};
    gemm_tile<G1>(Ax, DM, Awqkv, 3 * DM, m0, tn * 256, 3 * DM, 0, DM / 64, e);
  } else if (tn < 24) {
    EpiHead e{qB, 0.08838834764831845f * LOG2E};
    gemm_tile<G1>(Bx, DM, Bwq, NB, m0, (tn - 8) * 256, NB, 0, DM / 64, e);
  } else {
    const bool isv = tn >= 40;
    EpiHead e{isv ? vB : kB, 1.f};
    gemm_tile<G1>(Bckv, DC, isv ? Bwuv : Bwuk, NB, m0, (tn - (isv ? 40 : 24)) * 256, NB, 0, DC / 64, e);
  }
}

constexpr int SPLITK = 5;
__global__ __launch_bounds__(512, 2) void k_out(const u16* __restrict__ oA, const u16* __restrict__ Awo,
                                                const u16* __restrict__ ctx, const u16* __restrict__ Bwo,
                                                u16* __restrict__ out, float* __restrict__ part) {
  const int b = blockIdx.x;
  if (b < 40 * SPLITK) {
    const int tm = b & 7, rest = b >> 3, tn = rest % 5, sp = rest / 5;
    EpiF32 e{part + ((size_t)sp * 40 + tm * 5 + tn) * (256 * 128)};
    gemm_tile<G2, EpiF32, true>(ctx, DHB, Bwo, DM, tm * 256, tn * 128, DM, sp * 13 * 64, sp == 4 ? 12 : 13, e);
  } else {
    const int t = b - 40 * SPLITK, tm = t & 7, tn = t >> 3;
    EpiPlain e{out, 2 * DM, 1.f};
    gemm_tile<G2>(oA, DM, Awo, DM, tm * 256, tn * 128, DM, 0, DM / 64, e);
  }
}

__global__ __launch_bounds__(256) void k_reduce(const float* __restrict__ part, u16* __restrict__ out) {
  const int i = blockIdx.x * 256 + threadIdx.x;
  const int tile = i >> 13, w = (i >> 10) & 7, j = (i >> 6) & 15, lane = i & 63;
  const int nt = j >> 3, mt = (j >> 2) & 1, g = j & 3;
  const int tm = tile / 5, tn = tile - tm * 5;
  const int m = tm * 256 + (w >> 1) * 64 + mt * 32 + (lane & 31);
  const int n = tn * 128 + (w & 1) * 64 + nt * 32 + 8 * g + 4 * (lane >> 5);
  const f32x4* p = (const f32x4*)part + i;
  f32x4 s = p[0];
#pragma unroll
  for (int sp = 1; sp < SPLITK; ++sp) s += p[(size_t)sp * 40 * 8192];
  u32x2 v = {cvtpk(s[0], s[1]), cvtpk(s[2], s[3])};
  *(u32x2*)(out + (size_t)m * 2 * DM + DM + n) = v;
}

constexpr float THR = 8.f;
DI bf16x8 pack8(const f32x16& x, int s) {
  u32x4 w = {cvtpk(x[8 * s], x[8 * s + 1]), cvtpk(x[8 * s + 2], x[8 * s + 3]), cvtpk(x[8 * s + 4], x[8 * s + 5]),
             cvtpk(x[8 * s + 6], x[8 * s + 7])};
  return __builtin_bit_cast(bf16x8, w);
}
DI f32x16 splat16(float v) {
  f32x16 x;
#pragma unroll
  for (int i = 0; i < 16; ++i) x[i] = v;
  return x;
}
DI float max16(const f32x16& x) {
  float a = fmaxf(fmaxf(x[0], x[1]), x[2]);
#pragma unroll
  for (int i = 3; i < 15; i += 2) a = fmaxf(fmaxf(a, x[i]), x[i + 1]);
  return fmaxf(a, x[15]);
}

#define SBAR() __builtin_amdgcn_sched_barrier(0)
constexpr float BIGSUM = 4096.f;
DI float sum16(const f32x16& x) {
  float a = (x[0] + x[1]) + (x[2] + x[3]), b = (x[4] + x[5]) + (x[6] + x[7]);
  float c = (x[8] + x[9]) + (x[10] + x[11]), d = (x[12] + x[13]) + (x[14] + x[15]);
  return (a + b) + (c + d);
}
DI void exp16(f32x16& x) {
#pragma unroll
  for (int i = 0; i < 16; ++i) x[i] = __builtin_amdgcn_exp2f(x[i]);
}
DI void attnB_item(const u16* __restrict__ Q, const u16* __restrict__ K, const u16* __restrict__ V,
                   u16* __restrict__ ctx, int head, int jitem) {
  const int tid = threadIdx.x, lane = tid & 63, wid = __builtin_amdgcn_readfirstlane(tid >> 6);
  const int r = lane & 31, h = lane >> 5;
  constexpr unsigned KBUF = 0, VBUF = 32768, TB = 16384, STG = 65536;
  unsigned koff, voff;
  {
    const int l4 = lane >> 4, c = lane & 15;
    koff = (unsigned)((16 * wid + l4) * DHB + ((c ^ l4) * 8)) * 2u;
    voff = (unsigned)((16 * wid + l4) * DHB + ((c ^ (l4 << 2)) * 8)) * 2u;
  }
  const u16* Kh = K + (size_t)head * S_ * DHB;
  const u16* Vh = V + (size_t)head * S_ * DHB;
  auto stageK = [&](int buf, int t) {
    const u16* g = Kh + (size_t)t * 64 * DHB;
    dma_base(KBUF + buf * TB + wid * 4096);
    dma_piece<0>(g, koff); dma_piece_x<1024, 64>(g, koff); dma_piece_x<2048, 128>(g, koff); dma_piece_x<3072, 192>(g, koff);
  };
  auto stageV = [&](int buf, int t) {
    const u16* g = Vh + (size_t)t * 64 * DHB;
    dma_base(VBUF + buf * TB + wid * 4096);
    dma_piece<0>(g, voff); dma_piece<1024>(g, voff); dma_piece<2048>(g, voff); dma_piece<3072>(g, voff);
  };
  unsigned ka[8], va[4];
#pragma unroll
  for (int ks = 0; ks < 8; ++ks) {
    ka[ks] = KBUF + r * 256 + (((2 * ks + h) ^ (r & 15)) * 16);
    asm volatile("" : "+v"(ka[ks]));
  }
  {
    const int q = (lane & 15) >> 2, p = lane & 3, cb = (lane >> 4) & 1;
#pragma unroll
    for (int db = 0; db < 4; ++db) va[db] = VBUF + (4 * h + q) * 256 + ((db ^ q) * 64) + cb * 32 + p * 8;
  }
  bf16x8 qf[8];
  f32x16 o[4], negm;
  float mcur, lsum;
  auto loadQ = [&](int qb) {
    int lz = threadIdx.x;
    asm volatile("" : "+v"(lz));
    const u16* qp = Q + ((size_t)head * S_ + qb * 128) * DHB + (unsigned)((wid * 32 + (lz & 31)) * DHB + ((lz >> 5) & 1) * 8);
#pragma unroll
    for (int ks = 0; ks < 8; ++ks) qf[ks] = *(const bf16x8*)(qp + ks * 16);
  };
  stageK(0, 0); stageK(1, 1); stageV(0, 0);
  loadQ(15 - jitem);

  auto qk_abs = [&](unsigned off) {
    bf16x8 kf[8];
#pragma unroll
    for (int ks = 0; ks < 8; ++ks) kf[ks] = lds_read128(off + ka[ks]);
    f32x16 s = MFMA32(kf[0], qf[0], f32x16{});
#pragma unroll
    for (int ks = 1; ks < 8; ++ks) s = MFMA32(kf[ks], qf[ks], s);
    return s;
  };
  auto remax = [&](const f32x16& st, bool first, f32x16& s) {
    const float tm = xhalf_max(max16(st));
    const float mnew = first ? tm : fmaxf(tm, mcur);
    const float delta = mnew - mcur;
    const float f = __builtin_amdgcn_exp2f(-fmaxf(delta, 0.f));
    mcur = mnew;
    negm = splat16(-mnew);
#pragma unroll
    for (int i = 0; i < 16; ++i) s[i] = __builtin_amdgcn_exp2f(st[i] - mnew);
#pragma unroll
    for (int db = 0; db < 4; ++db)
#pragma unroll
      for (int i = 0; i < 16; ++i) o[db][i] *= f;
    lsum *= f;
    return delta;
  };
  auto slow = [&](int blk) {
    int lz = threadIdx.x;
    asm volatile("" : "+v"(lz));
    const u16* kp = Kh + (size_t)blk * 32 * DHB + (unsigned)((lz & 31) * DHB + ((lz >> 5) & 1) * 8);
    f32x16 st = f32x16{};
#pragma unroll
    for (int ks = 0; ks < 8; ++ks) {
      st = MFMA32(*(const bf16x8*)(kp + ks * 16), qf[ks], st);
      asm volatile("" ::: "memory");
      SBAR();
    }
    return st;
  };
  auto pv = [&](unsigned off, const f32x16& p) {
#pragma unroll
    for (int sx = 0; sx < 2; ++sx) {
      const bf16x8 pf = pack8(p, sx);
#pragma unroll
      for (int db = 0; db < 4; ++db) {
        const bf16x8 vf = cat8(lds_read_tr(off + va[db] + (16 * sx) * 256), lds_read_tr(off + va[db] + (16 * sx + 8) * 256));
        o[db] = MFMA32(vf, pf, o[db]);
      }
    }
  };
#define EXP2(x) x = __builtin_amdgcn_exp2f(x)
#define VRD(dst, off) dst = cat8(lds_read_tr(off), lds_read_tr((off) + 2048))

#pragma unroll 1
  for (int pass = 0; pass < 2; ++pass) {
    const int qb = pass ? jitem : 15 - jitem;
    const int ntile = 2 * qb + 2;
    const int kbd = 4 * qb + wid;
    auto maskblk = [&](f32x16& s, int kb) {
      int lz = threadIdx.x;
      asm volatile("" : "+v"(lz));
      const int dq = (kbd - kb) * 32 + (lz & 31) - 4 * ((lz >> 5) & 1);
#pragma unroll
      for (int i = 0; i < 16; ++i)
        if ((i & 3) + 8 * (i >> 2) > dq) s[i] = -__builtin_inff();
    };
    {
      float z = 0.f;
      asm volatile("" : "+v"(z));
#pragma unroll
      for (int db = 0; db < 4; ++db) o[db] = splat16(z);
      lsum = z;
    }
#pragma unroll
    for (int ks = 0; ks < 8; ++ks) asm volatile("" : "+v"(qf[ks]));
    WAIT_VM0();
    BARRIER();
    {
      f32x16 sa, sb;
      bf16x8 kg[8];
      float pa;
      {
        f32x16 st = qk_abs(0);
        if (kbd == 0) maskblk(st, 0);
        const float m = xhalf_max(max16(st));
        mcur = m;
        negm = splat16(-m);
#pragma unroll
        for (int i = 0; i < 8; ++i) sa[i] = __builtin_amdgcn_exp2f(st[i] - m);
#pragma unroll
        for (int i = 8; i < 16; ++i) sa[i] = st[i] - m;
        pa = ((sa[0] + sa[1]) + (sa[2] + sa[3])) + ((sa[4] + sa[5]) + (sa[6] + sa[7]));
#pragma unroll
        for (int ks = 0; ks < 8; ++ks) kg[ks] = lds_read128(ka[ks] + 8192);
      }
      auto tile = [&](auto BUFC, auto LASTC, int t) {
        constexpr unsigned B_ = decltype(BUFC)::value;
        constexpr bool LAST = decltype(LASTC)::value;
        constexpr unsigned vo = B_ * TB, vn = TB - vo;
        constexpr unsigned kn = TB - vo, kd = vo;
        const u16* kbn = Kh + (size_t)min(t + 2, 31) * 64 * DHB;
        const u16* vbn = Vh + (size_t)min(t + 1, 31) * 64 * DHB;
#define DMAK(i) do { if ((i) == 0) { dma_base(KBUF + kd + wid * 4096); dma_piece<0>(kbn, koff); } else dma_piece_x<(i) * 1024, (i) * 64>(kbn, koff); } while (0)
#define DMAV(i) do { if ((i) == 0) dma_base(VBUF + vn + wid * 4096); dma_piece<(i) * 1024>(vbn, voff); } while (0)
        bf16x8 kf[8], vf[8], vg[8];
#define KF(i) kf[i] = lds_read128(kn + ka[i])
#define KG(i) kg[i] = lds_read128(kn + ka[i] + 8192)
        sb = MFMA32(kg[0], qf[0], negm);
        VRD(vf[0], vo + va[0]); DMAV(0);
        EXP2(sa[8]); EXP2(sa[9]);
        SBAR();
        sb = MFMA32(kg[1], qf[1], sb);
        VRD(vf[1], vo + va[1]); DMAV(1);
        EXP2(sa[10]); EXP2(sa[11]);
        SBAR();
        sb = MFMA32(kg[2], qf[2], sb);
        VRD(vf[2], vo + va[2]); DMAV(2);
        EXP2(sa[12]); EXP2(sa[13]);
        float pa2 = (sa[8] + sa[9]) + (sa[10] + sa[11]);
        SBAR();
        sb = MFMA32(kg[3], qf[3], sb);
        VRD(vf[3], vo + va[3]); DMAV(3);
        EXP2(sa[14]); EXP2(sa[15]);
        pa += sa[12] + sa[13];
        SBAR();
        sb = MFMA32(kg[4], qf[4], sb);
        DMAK(0);
        pa2 += sa[14] + sa[15];
        pa += pa2;
        if (__any(!(pa <= BIGSUM))) {
          f32x16 st = slow(2 * t);
          if (2 * t >= kbd) maskblk(st, 2 * t);
          const float delta = remax(st, false, sa);
#pragma unroll
          for (int i = 0; i < 16; ++i) sb[i] -= delta;
          pa = sum16(sa);
        }
        sb = MFMA32(kg[5], qf[5], sb);
        VRD(vf[4], vo + va[0] + 4096); VRD(vf[5], vo + va[1] + 4096);
        const bf16x8 p0 = pack8(sa, 0);
        lsum += pa;
        SBAR();
        sb = MFMA32(kg[6], qf[6], sb);
        VRD(vf[6], vo + va[2] + 4096); DMAK(1);
        const bf16x8 p1 = pack8(sa, 1);
        SBAR();
        sb = MFMA32(kg[7], qf[7], sb);
        VRD(vf[7], vo + va[3] + 4096); DMAK(2);
        SBAR();
        o[0] = MFMA32(vf[0], p0, o[0]);
        KF(0); DMAK(3);
        if (2 * t + 1 >= kbd) maskblk(sb, 2 * t + 1);
        SBAR();
        o[1] = MFMA32(vf[1], p0, o[1]);
        KF(1);
        EXP2(sb[0]); EXP2(sb[1]); EXP2(sb[2]);
        SBAR();
        o[2] = MFMA32(vf[2], p0, o[2]);
        KF(2);
        EXP2(sb[3]); EXP2(sb[4]);
        SBAR();
        o[3] = MFMA32(vf[3], p0, o[3]);
        KF(3);
        EXP2(sb[5]); EXP2(sb[6]);
        float pb = (sb[0] + sb[1]) + (sb[2] + sb[3]);
        SBAR();
        o[0] = MFMA32(vf[4], p1, o[0]);
        KF(4);
        EXP2(sb[7]); EXP2(sb[8]);
        SBAR();
        o[1] = MFMA32(vf[5], p1, o[1]);
        KF(5);
        EXP2(sb[9]); EXP2(sb[10]);
        float pb2 = (sb[4] + sb[5]) + (sb[6] + sb[7]);
        SBAR();
        o[2] = MFMA32(vf[6], p1, o[2]);
        KF(6);
        EXP2(sb[11]); EXP2(sb[12]);
        SBAR();
        o[3] = MFMA32(vf[7], p1, o[3]);
        KF(7);
        EXP2(sb[13]); EXP2(sb[14]);
        pb += (sb[8] + sb[9]) + sb[10];
        SBAR();
        sa = MFMA32(kf[0], qf[0], negm);
        VRD(vg[0], vo + va[0] + 8192);
        EXP2(sb[15]);
        pb2 += (sb[11] + sb[12]) + sb[13];
        SBAR();
        sa = MFMA32(kf[1], qf[1], sa);
        VRD(vg[1], vo + va[1] + 8192);
        pb += sb[14] + sb[15];
        pb += pb2;
        if (__any(!(pb <= BIGSUM))) {
          f32x16 st = slow(2 * t + 1);
          if (2 * t + 1 >= kbd) maskblk(st, 2 * t + 1);
          const float delta = remax(st, false, sb);
#pragma unroll
          for (int i = 0; i < 16; ++i) sa[i] -= delta;
          pb = sum16(sb);
        }
        sa = MFMA32(kf[2], qf[2], sa);
        VRD(vg[2], vo + va[2] + 8192);
        const bf16x8 r0 = pack8(sb, 0);
        lsum += pb;
        SBAR();
        sa = MFMA32(kf[3], qf[3], sa);
        VRD(vg[3], vo + va[3] + 8192);
        const bf16x8 r1 = pack8(sb, 1);
        SBAR();
        sa = MFMA32(kf[4], qf[4], sa);
        VRD(vg[4], vo + va[0] + 12288);
        SBAR();
        sa = MFMA32(kf[5], qf[5], sa);
        VRD(vg[5], vo + va[1] + 12288);
        SBAR();
        sa = MFMA32(kf[6], qf[6], sa);
        VRD(vg[6], vo + va[2] + 12288);
        SBAR();
        sa = MFMA32(kf[7], qf[7], sa);
        VRD(vg[7], vo + va[3] + 12288);
        SBAR();
        o[0] = MFMA32(vg[0], r0, o[0]);
        KG(0);
        if (2 * t + 2 >= kbd) maskblk(sa, 2 * t + 2);
        SBAR();
        o[1] = MFMA32(vg[1], r0, o[1]);
        KG(1); EXP2(sa[0]); EXP2(sa[1]);
        SBAR();
        o[2] = MFMA32(vg[2], r0, o[2]);
        KG(2); EXP2(sa[2]); EXP2(sa[3]);
        SBAR();
        o[3] = MFMA32(vg[3], r0, o[3]);
        KG(3); EXP2(sa[4]); EXP2(sa[5]);
        SBAR();
        o[0] = MFMA32(vg[4], r1, o[0]);
        KG(4); EXP2(sa[6]); EXP2(sa[7]);
        pa = (sa[0] + sa[1]) + (sa[2] + sa[3]);
        SBAR();
        o[1] = MFMA32(vg[5], r1, o[1]);
        KG(5);
        pa += (sa[4] + sa[5]) + (sa[6] + sa[7]);
        SBAR();
        o[2] = MFMA32(vg[6], r1, o[2]);
        KG(6); KG(7);
        SBAR();
        o[3] = MFMA32(vg[7], r1, o[3]);
#undef KF
#undef KG
        WAIT_VM0();
        BARRIER();
#undef DMAK
#undef DMAV
      };
      typedef std::integral_constant<unsigned, 0> C0;
      typedef std::integral_constant<unsigned, 1> C1;
      int t = 0;
      do {
        tile(C0{}, std::false_type{}, t);
        tile(C1{}, std::false_type{}, t + 1);
        t += 2;
      } while (t < ntile);
    }
    if (pass == 0) { stageK(0, 0); stageK(1, 1); stageV(0, 0); }
    const float inv = 1.f / xhalf_sum(lsum);
    {
      int lz = threadIdx.x;
      asm volatile("" : "+v"(lz));
      const int lane = lz & 63, r = lz & 31, h = (lz >> 5) & 1;
      const unsigned sb_ = STG + wid * 4096;
      u16* op = ctx + ((size_t)head * S_ + qb * 128 + wid * 32 + (lane >> 3)) * DHB + (lane & 7) * 8;
#pragma unroll
      for (int hf = 0; hf < 2; ++hf) {
#pragma unroll
        for (int d2 = 0; d2 < 2; ++d2)
#pragma unroll
          for (int g = 0; g < 4; ++g) {
            const f32x16& x = o[2 * hf + d2];
            u32x2 v = {cvtpk(x[4 * g] * inv, x[4 * g + 1] * inv), cvtpk(x[4 * g + 2] * inv, x[4 * g + 3] * inv)};
            *(lds_u32x2*)(uintptr_t)(sb_ + r * 128 + (((4 * d2 + g) ^ (r & 7)) * 16) + h * 8) = v;
          }
#pragma unroll
        for (int it = 0; it < 4; ++it) {
          const int rr = it * 8 + (lane >> 3);
          const bf16x8 v = lds_read128(sb_ + rr * 128 + (((lane & 7) ^ (rr & 7)) * 16));
          *(bf16x8*)(op + (size_t)it * 8 * DHB + hf * 64) = v;
        }
      }
    }
    if (pass == 0) loadQ(jitem);
  }
#undef EXP2
#undef VRD
}

template <int QT>
DI void attnA_block(const u16* __restrict__ qkv, u16* __restrict__ oA, int head, int qb) {
  const int tid = threadIdx.x, lane = tid & 63, wid = __builtin_amdgcn_readfirstlane(tid >> 6);
  const int r = lane & 31, h = lane >> 5;
  const int q0 = qb * (128 * QT) + wid * (32 * QT);
  constexpr unsigned KBUF = 0, VBUF = 16384, TB = 8192;
  const u16* Qh = qkv + (size_t)(0 * HA + head) * S_ * 32;
  const u16* Kh = qkv + (size_t)(1 * HA + head) * S_ * 32;
  const u16* Vh = qkv + (size_t)(2 * HA + head) * S_ * 32;
  unsigned koff[2], voff[2];
#pragma unroll
  for (int i = 0; i < 2; ++i) {
    const int row = 16 * (wid * 2 + i) + (lane >> 2), c = lane & 3;
    koff[i] = (unsigned)(row * 64 + ((c ^ ((row >> 2) & 3)) * 16));
    voff[i] = (unsigned)(row * 64 + c * 16);
  }
  auto stage = [&](int buf, int t) {
    const u16* kb = Kh + (size_t)t * 128 * 32;
    const u16* vb = Vh + (size_t)t * 128 * 32;
#pragma unroll
    for (int i = 0; i < 2; ++i) dma16(kb, koff[i], KBUF + buf * TB + (wid * 2 + i) * 1024);
#pragma unroll
    for (int i = 0; i < 2; ++i) dma16(vb, voff[i], VBUF + buf * TB + (wid * 2 + i) * 1024);
  };
  stage(0, 0);
  bf16x8 qf[QT][2];
#pragma unroll
  for (int qt = 0; qt < QT; ++qt)
#pragma unroll
    for (int ks = 0; ks < 2; ++ks) qf[qt][ks] = *(const bf16x8*)(Qh + (size_t)(q0 + 32 * qt + r) * 32 + ks * 16 + h * 8);
  unsigned ka[2], va;
#pragma unroll
  for (int ks = 0; ks < 2; ++ks) ka[ks] = KBUF + r * 64 + (((2 * ks + h) ^ ((r >> 2) & 3)) * 16);
  {
    const int q = (lane & 15) >> 2, p = lane & 3, cb = (lane >> 4) & 1;
    va = VBUF + (4 * h + q) * 64 + cb * 32 + p * 8;
  }
  f32x16 o[QT], negm[QT];
  float mcur[QT];
#pragma unroll
  for (int qt = 0; qt < QT; ++qt) { o[qt] = f32x16{}; negm[qt] = f32x16{}; mcur[qt] = 0.f; }
#pragma unroll
  for (int qt = 0; qt < QT; ++qt) { asm volatile("" : "+v"(qf[qt][0])); asm volatile("" : "+v"(qf[qt][1])); }
  WAIT_VM0();
  BARRIER();
  constexpr int NKT = S_ / 128;
  for (int t = 0; t < NKT; ++t) {
    const unsigned bo = (t & 1) * TB;
    if (t + 1 < NKT) stage((t + 1) & 1, t + 1);
    f32x16 s[QT][4];
#pragma unroll
    for (int kb = 0; kb < 4; ++kb) {
      const bf16x8 k0 = lds_read128(bo + ka[0] + kb * 2048), k1 = lds_read128(bo + ka[1] + kb * 2048);
#pragma unroll
      for (int qt = 0; qt < QT; ++qt) {
        s[qt][kb] = MFMA32(k0, qf[qt][0], negm[qt]);
        s[qt][kb] = MFMA32(k1, qf[qt][1], s[qt][kb]);
      }
    }
    float tm[QT];
    bool need = t == 0;
#pragma unroll
    for (int qt = 0; qt < QT; ++qt) {
      tm[qt] = xhalf_max(fmaxf(fmaxf(max16(s[qt][0]), max16(s[qt][1])), fmaxf(max16(s[qt][2]), max16(s[qt][3]))));
      need = need || tm[qt] > THR;
    }
    if (__any(need)) {
#pragma unroll
      for (int qt = 0; qt < QT; ++qt) {
        const float dpos = fmaxf(tm[qt], 0.f);
        const float delta = t == 0 ? tm[qt] : dpos;
        const float f = __builtin_amdgcn_exp2f(-dpos);
        mcur[qt] += delta;
        negm[qt] = splat16(-mcur[qt]);
#pragma unroll
        for (int kb = 0; kb < 4; ++kb)
#pragma unroll
          for (int i = 0; i < 16; ++i) s[qt][kb][i] -= delta;
#pragma unroll
        for (int i = 0; i < 16; ++i) o[qt][i] *= f;
      }
    }
#pragma unroll
    for (int kb = 0; kb < 4; ++kb) {
#pragma unroll
      for (int sx = 0; sx < 2; ++sx) {
        const bf16x8 vf = cat8(lds_read_tr(bo + va + (32 * kb + 16 * sx) * 64), lds_read_tr(bo + va + (32 * kb + 16 * sx + 8) * 64));
#pragma unroll
        for (int qt = 0; qt < QT; ++qt) {
#pragma unroll
          for (int i = 0; i < 8; ++i) s[qt][kb][8 * sx + i] = __builtin_amdgcn_exp2f(s[qt][kb][8 * sx + i]);
          o[qt] = MFMA32(vf, pack8(s[qt][kb], sx), o[qt]);
        }
      }
    }
    WAIT_VM0();
    BARRIER();
  }
#pragma unroll
  for (int qt = 0; qt < QT; ++qt) {
    const float l = __shfl(o[qt][8], r + 32);
    const float inv = 1.f / l;
    u16* op = oA + (size_t)(q0 + 32 * qt + r) * DM + head * DHA + 4 * h;
#pragma unroll
    for (int g = 0; g < 3; ++g) {
      if (g < 2 || h == 0) {
        u32x2 v = {cvtpk(o[qt][4 * g] * inv, o[qt][4 * g + 1] * inv), cvtpk(o[qt][4 * g + 2] * inv, o[qt][4 * g + 3] * inv)};
        *(u32x2*)(op + 8 * g) = v;
      }
    }
  }
}

constexpr int AQT = 2;
constexpr int NBLK_B = HB * 8, NBLK_A = HA * (S_ / (128 * AQT));
__global__ __launch_bounds__(256, 2) void k_attn(const u16* __restrict__ qB, const u16* __restrict__ kB,
                                                 const u16* __restrict__ vB, u16* __restrict__ ctx,
                                                 const u16* __restrict__ qkvA, u16* __restrict__ oA) {
  const int b = blockIdx.x;
  if (b < NBLK_B) {
    const int head = (b & 7) + 8 * (b >> 6), j = (b >> 3) & 7;
    attnB_item(qB, kB, vB, ctx, head, j);
  } else {
    const int a = b - NBLK_B;
    constexpr int PER = S_ / (128 * AQT);
    const int head = (a & 7) + 8 * (a / (8 * PER)), qb = (a >> 3) % PER;
    attnA_block<AQT>(qkvA, oA, head, qb);
  }
}

extern "C" void kernel_launch(void* const* d_in, const int* in_sizes, int n_in, void* d_out, int out_size, void* d_ws,
                              size_t ws_size, hipStream_t stream) {
  const u16* Ax = (const u16*)d_in[0];
  const u16* Awqkv = (const u16*)d_in[1];
  const u16* Awo = (const u16*)d_in[2];
  const u16* Bx = (const u16*)d_in[3];
  const u16* Bckv = (const u16*)d_in[4];
  const u16* Bwq = (const u16*)d_in[5];
  const u16* Bwuk = (const u16*)d_in[6];
  const u16* Bwuv = (const u16*)d_in[7];
  const u16* Bwo = (const u16*)d_in[8];
  u16* out = (u16*)d_out;
  char* ws = (char*)d_ws;
  u16* qkvA = (u16*)ws;                 ws += (size_t)3 * HA * S_ * 32 * 2;
  u16* qB = (u16*)ws;                   ws += (size_t)S_ * NB * 2;
  u16* kB = (u16*)ws;                   ws += (size_t)S_ * NB * 2;
  u16* vB = (u16*)ws;                   ws += (size_t)S_ * NB * 2;
  u16* ctx = (u16*)ws;                  ws += (size_t)S_ * NB * 2;
  u16* oA = (u16*)ws;                   ws += (size_t)S_ * DM * 2;
  float* part = (float*)ws;             ws += (size_t)SPLITK * S_ * DM * 4;

  (void)hipFuncSetAttribute((const void*)k_proj, hipFuncAttributeMaxDynamicSharedMemorySize, G1::LDS);
  (void)hipFuncSetAttribute((const void*)k_out, hipFuncAttributeMaxDynamicSharedMemorySize, G2::LDS);
  (void)hipFuncSetAttribute((const void*)k_attn, hipFuncAttributeMaxDynamicSharedMemorySize, 81920);
  k_proj<<<448, 512, G1::LDS, stream>>>(Ax, Awqkv, Bx, Bckv, Bwq, Bwuk, Bwuv, qkvA, qB, kB, vB);
  k_attn<<<NBLK_B + NBLK_A, 256, 81920, stream>>>(qB, kB, vB, ctx, qkvA, oA);
  k_out<<<40 * SPLITK + 40, 512, G2::LDS, stream>>>(oA, Awo, ctx, Bwo, out, part);
  k_reduce<<<S_ * DM / 4 / 256, 256, 0, stream>>>(part, out);
}
```

```cpp
#include <hip/hip_runtime.h>
#include <hip/hip_bf16.h>
#include <stdint.h>
#include <type_traits>

constexpr int S_ = 2048;
constexpr int DM = 640;
constexpr int HA = 32, DHA = 20;
constexpr int HB = 32, DHB = 128, DC = 512;
constexpr int NB = HB * DHB;

typedef unsigned short u16;
typedef short bf16x8 __attribute__((ext_vector_type(8)));
typedef short s16x4 __attribute__((ext_vector_type(4)));
typedef float f32x16 __attribute__((ext_vector_type(16)));
typedef float f32x4 __attribute__((ext_vector_type(4)));
typedef float f32x2 __attribute__((ext_vector_type(2)));
typedef __bf16 bf16x2 __attribute__((ext_vector_type(2)));
typedef unsigned u32x2 __attribute__((ext_vector_type(2)));
typedef unsigned u32x4 __attribute__((ext_vector_type(4)));
typedef __attribute__((address_space(3))) s16x4 lds_s16x4;
typedef __attribute__((address_space(3))) bf16x8 lds_bf16x8;
typedef __attribute__((address_space(3))) u32x2 lds_u32x2;

#define DI __device__ __forceinline__
#define MFMA32(a, b, c) __builtin_amdgcn_mfma_f32_32x32x16_bf16((a), (b), (c), 0, 0, 0)

DI unsigned cvtpk(float lo, float hi) {
  f32x2 v = {lo, hi};
  bf16x2 b = __builtin_convertvector(v, bf16x2);
  return __builtin_bit_cast(unsigned, b);
}
DI void dma16(const void* sbase, unsigned voff, unsigned lds_addr) {
  unsigned keep;
  asm volatile("s_mov_b32 %0, m0\n\ts_mov_b32 m0, %3\n\ts_nop 0\n\tglobal_load_lds_dwordx4 %1, %2\n\ts_mov_b32 m0, %0"
               : "=&s"(keep) : "v"(voff), "s"(sbase), "s"(lds_addr) : "memory");
}
DI void dma_base(unsigned lds_addr) { asm volatile("s_mov_b32 m0, %0\n\ts_nop 0" :: "s"(lds_addr) : "memory"); }
template <int OFF> DI void dma_piece(const void* sbase, unsigned voff) {
  asm volatile("global_load_lds_dwordx4 %0, %1 offset:%2" :: "v"(voff), "s"(sbase), "i"(OFF) : "memory");
}
template <int OFF, int X> DI void dma_piece_x(const void* sbase, unsigned voff) {
  unsigned t;
  asm volatile("v_xor_b32 %0, %4, %1\n\tglobal_load_lds_dwordx4 %0, %2 offset:%3" : "=&v"(t) : "v"(voff), "s"(sbase), "i"(OFF), "i"(X) : "memory");
}
#define WAIT_VM0() asm volatile("s_waitcnt vmcnt(0)" ::: "memory")
#define BARRIER() do { asm volatile("s_waitcnt lgkmcnt(0)" ::: "memory"); __builtin_amdgcn_s_barrier(); asm volatile("" ::: "memory"); } while (0)

DI bf16x8 lds_read128(unsigned off) { return *(const lds_bf16x8*)(uintptr_t)off; }
DI s16x4 lds_read_tr(unsigned off) { return __builtin_amdgcn_ds_read_tr16_b64_v4i16((lds_s16x4*)(uintptr_t)off); }
DI bf16x8 cat8(s16x4 a, s16x4 b) { return __builtin_shufflevector(a, b, 0, 1, 2, 3, 4, 5, 6, 7); }
DI float xhalf_max(float x) {
  auto r = __builtin_amdgcn_permlane32_swap(__float_as_uint(x), __float_as_uint(x), false, false);
  return fmaxf(__uint_as_float(r[0]), __uint_as_float(r[1]));
}
DI float xhalf_sum(float x) {
  auto r = __builtin_amdgcn_permlane32_swap(__float_as_uint(x), __float_as_uint(x), false, false);
  return __uint_as_float(r[0]) + __uint_as_float(r[1]);
}

template <int BM, int BN, int WM, int WN, int NST_>
struct GCfg {
  static constexpr int NST = NST_;
  static constexpr int NW = WM * WN, TM = BM / WM, TN = BN / WN, MT = TM / 32, NT = TN / 32;
  static constexpr int XB = BM * 128, WROW = BN * 2, WB = 64 * WROW, STAGE = XB + WB;
  static constexpr int XI = BM / 8 / NW;
  static constexpr int WI = WB / 1024 / NW;
  static constexpr int WCH = BN / 8;
  static constexpr int WRPI = 64 / WCH;
  static constexpr int LDS = NST * STAGE;
};

template <class C, class Epi, bool XHEAD = false>
DI void gemm_tile(const u16* __restrict__ X, int ldx, const u16* __restrict__ W, int ldw, int m0, int n0, int ncols,
                  int kbeg, int nk, const Epi& epi) {
  const int tid = threadIdx.x, lane = tid & 63, wid = __builtin_amdgcn_readfirstlane(tid >> 6);
  const int wm = wid / C::WN_, wn = wid % C::WN_;
  const int r = lane & 31, h = lane >> 5;
  unsigned xoff[C::XI], woff[C::WI];
#pragma unroll
  for (int i = 0; i < C::XI; ++i) {
    const int row = 8 * (wid * C::XI + i) + (lane >> 3);
    xoff[i] = (unsigned)(row * ldx + (((lane & 7) ^ ((row >> 1) & 7)) * 8)) * 2u;
  }
#pragma unroll
  for (int i = 0; i < C::WI; ++i) {
    const int k = C::WRPI * (wid * C::WI + i) + lane / C::WCH;
    int col = (((lane % C::WCH) ^ ((k & 3) << 2)) * 8);
    if (n0 + col > ncols - 8) col = ncols - 8 - n0;
    woff[i] = (unsigned)(k * ldw + col) * 2u;
  }
  const u16* xs = XHEAD ? X + (size_t)m0 * ldx : X + (size_t)m0 * ldx + kbeg;
  const int kt0 = kbeg >> 6;
  auto xk = [&](int kt) { return XHEAD ? xs + (size_t)((kt + kt0) >> 1) * S_ * DHB + ((kt + kt0) & 1) * 64 : xs + kt * 64; };
  const u16* ws = W + (size_t)kbeg * ldw + n0;
  auto stage = [&](int buf, int kt) {
    const u16* xb = xk(kt);
    const u16* wb = ws + (size_t)kt * 64 * ldw;
    const unsigned lb = buf * C::STAGE;
#pragma unroll
    for (int i = 0; i < C::XI; ++i) dma16(xb, xoff[i], lb + (wid * C::XI + i) * 1024);
#pragma unroll
    for (int i = 0; i < C::WI; ++i) dma16(wb, woff[i], lb + C::XB + (wid * C::WI + i) * 1024);
  };
  unsigned xa[4];
#pragma unroll
  for (int ks = 0; ks < 4; ++ks) xa[ks] = (wm * C::TM + r) * 128 + (((2 * ks + h) ^ ((r >> 1) & 7)) * 16);
  unsigned wa[C::NT];
  {
    const int q = (lane & 15) >> 2, p = lane & 3, cb = (lane >> 4) & 1;
#pragma unroll
    for (int nt = 0; nt < C::NT; ++nt) {
      const int nb = wn * C::NT + nt;
      wa[nt] = C::XB + (8 * h + q) * C::WROW + ((nb ^ q) * 64) + cb * 32 + p * 8;
    }
  }
  f32x16 acc[C::NT][C::MT];
#pragma unroll
  for (int nt = 0; nt < C::NT; ++nt)
#pragma unroll
    for (int mt = 0; mt < C::MT; ++mt) acc[nt][mt] = f32x16{};

  constexpr int AHEAD = C::NST - 1, PER = C::XI + C::WI;
  stage(0, 0);
  if constexpr (AHEAD == 2) { stage(1, 1); asm volatile("s_waitcnt vmcnt(%0)" :: "i"(PER) : "memory"); }
  else WAIT_VM0();
  BARRIER();
  int cur = 0, nxt = AHEAD;
  for (int kt = 0; kt < nk; ++kt) {
    const unsigned lb = cur * C::STAGE;
    const bool more = kt + AHEAD < nk;
    const u16* xb = xk(kt + AHEAD);
    const u16* wb = ws + (size_t)(kt + AHEAD) * 64 * ldw;
    const unsigned nb = nxt * C::STAGE;
    cur = cur + 1 == C::NST ? 0 : cur + 1;
    nxt = nxt + 1 == C::NST ? 0 : nxt + 1;
#pragma unroll
    for (int ks = 0; ks < 4; ++ks) {
      bf16x8 xf[C::MT], wf[C::NT];
#pragma unroll
      for (int mt = 0; mt < C::MT; ++mt) xf[mt] = lds_read128(lb + xa[ks] + mt * 4096);
#pragma unroll
      for (int nt = 0; nt < C::NT; ++nt)
        wf[nt] = cat8(lds_read_tr(lb + wa[nt] + (16 * ks) * C::WROW), lds_read_tr(lb + wa[nt] + (16 * ks + 4) * C::WROW));
      if (more) {
        constexpr int TOT = C::XI + C::WI;
#pragma unroll
        for (int i = ks * TOT / 4; i < (ks + 1) * TOT / 4; ++i) {
          if (i < C::XI) dma16(xb, xoff[i], nb + (wid * C::XI + i) * 1024);
          else dma16(wb, woff[i - C::XI], nb + C::XB + (wid * C::WI + i - C::XI) * 1024);
        }
      }
#pragma unroll
      for (int nt = 0; nt < C::NT; ++nt)
#pragma unroll
        for (int mt = 0; mt < C::MT; ++mt) acc[nt][mt] = MFMA32(wf[nt], xf[mt], acc[nt][mt]);
    }
    if (AHEAD == 2 && more) asm volatile("s_waitcnt vmcnt(%0)" :: "i"(PER) : "memory");
    else WAIT_VM0();
    BARRIER();
  }
  if constexpr (Epi::DIRECT) {
    f32x4* dst = (f32x4*)epi.out + (size_t)wid * (C::NT * C::MT * 4 * 64) + lane;
#pragma unroll
    for (int nt = 0; nt < C::NT; ++nt)
#pragma unroll
      for (int mt = 0; mt < C::MT; ++mt)
#pragma unroll
        for (int g = 0; g < 4; ++g) {
          f32x4 v = {acc[nt][mt][4 * g], acc[nt][mt][4 * g + 1], acc[nt][mt][4 * g + 2], acc[nt][mt][4 * g + 3]};
          dst[((nt * C::MT + mt) * 4 + g) * 64] = v;
        }
  } else {
    static_assert(C::TN == 64, "staging layout");
    const unsigned sb = wid * (C::TM * 128);
#pragma unroll
    for (int nt = 0; nt < C::NT; ++nt)
#pragma unroll
      for (int g = 0; g < 4; ++g) {
        const int nl = nt * 32 + 8 * g + 4 * h;
        const float sc = epi.scale_of(n0 + wn * C::TN + nl);
        const unsigned a = sb + r * 128 + (((nl >> 3) ^ (r & 7)) * 16) + (nl & 4) * 2;
#pragma unroll
        for (int mt = 0; mt < C::MT; ++mt) {
          u32x2 v = {cvtpk(acc[nt][mt][4 * g] * sc, acc[nt][mt][4 * g + 1] * sc),
                     cvtpk(acc[nt][mt][4 * g + 2] * sc, acc[nt][mt][4 * g + 3] * sc)};
          *(lds_u32x2*)(uintptr_t)(a + mt * 4096) = v;
        }
      }
    epi.template store<C::TM>(sb, m0 + wm * C::TM, n0 + wn * C::TN, lane);
  }
}

template <int BM, int BN, int WM, int WN, int NST>
struct GC : GCfg<BM, BN, WM, WN, NST> { static constexpr int WN_ = WN; };

struct EpiPlain {
  static constexpr bool DIRECT = false, PREFW = false;
  u16* out; int ld; float scale;
  DI float scale_of(int) const { return scale; }
  template <int TM> DI void store(unsigned sb, int mb, int nb, int lane) const {
    const int rr = lane >> 3, c = lane & 7;
    u16* dst = out + (size_t)(mb + rr) * ld + nb + c * 8;
    const unsigned a = sb + rr * 128 + ((c ^ (rr & 7)) * 16);
#pragma unroll
    for (int it = 0; it < TM / 8; ++it) {
      const bf16x8 v = lds_read128(a + it * 1024);
      *(bf16x8*)(dst + (size_t)it * 8 * ld) = v;
    }
  }
};
struct EpiHead {
  static constexpr bool DIRECT = false, PREFW = true;
  u16* out; float scale;
  DI float scale_of(int) const { return scale; }
  template <int TM> DI void store(unsigned sb, int mb, int nb, int lane) const {
    const int rr = lane >> 3, c = lane & 7;
    u16* dst = out + ((size_t)(nb >> 7) * S_ + mb + rr) * DHB + (nb & 127) + c * 8;
    const unsigned a = sb + rr * 128 + ((c ^ (rr & 7)) * 16);
#pragma unroll
    for (int it = 0; it < TM / 8; ++it) {
      const bf16x8 v = lds_read128(a + it * 1024);
      *(bf16x8*)(dst + (size_t)it * 8 * DHB) = v;
    }
  }
};
struct EpiQKVA {
  static constexpr bool DIRECT = false, PREFW = true;
  u16* out; float qscale;
  DI float scale_of(int n) const { return n < DM ? qscale : 1.f; }
  template <int TM> DI void store(unsigned sb, int mb, int nb, int lane) const {
    const int rr = lane >> 4, gq = lane & 15;
    const int n = nb + gq * 4;
    if (n >= 3 * DM) return;
    const int which = n / DM, rem = n - which * DM, head = rem / DHA, dd = rem - head * DHA;
    u16* dst = out + ((size_t)((which * HA + head) * S_ + mb + rr)) * 32;
#pragma unroll 4
    for (int it = 0; it < TM / 4; ++it) {
      const int row = it * 4 + rr;
      const u32x2 v = *(const lds_u32x2*)(uintptr_t)(sb + row * 128 + (((gq >> 1) ^ (row & 7)) * 16) + (gq & 1) * 8);
      u16* d = dst + it * 4 * 32;
      *(u32x2*)(d + dd) = v;
      if (dd == 16) {
        u32x2 z0 = {which == 2 ? 0x3F80u : 0u, 0u};
        u32x4 z1 = {0u, 0u, 0u, 0u};
        *(u32x2*)(d + 20) = z0;
        *(u32x4*)(d + 24) = z1;
      }
    }
  }
};
struct EpiF32 {
  static constexpr bool DIRECT = true, PREFW = false;
  float* out;
};

typedef GC<256, 256, 2, 4, 2> G1;
typedef GC<256, 128, 4, 2, 3> G2;

constexpr float LOG2E = 1.4426950408889634f;

__global__ __launch_bounds__(512, 2) void k_proj(const u16* __restrict__ Ax, const u16* __restrict__ Awqkv,
                                                 const u16* __restrict__ Bx, const u16* __restrict__ Bckv,
                                                 const u16* __restrict__ Bwq, const u16* __restrict__ Bwuk,
                                                 const u16* __restrict__ Bwuv, u16* __restrict__ qkvA,
                                                 u16* __restrict__ qB, u16* __restrict__ kB, u16* __restrict__ vB) {
  const int b = blockIdx.x, xcd = b & 7, idx = b >> 3;
  const int tn = xcd + 8 * (idx >> 3), tm = idx & 7;
  const int m0 = tm * 256;
  if (tn < 8) {
    EpiQKVA e{qkvA, 0.22360679774997896f * LOG2E};
    gemm_tile<G1>(Ax, DM, Awqkv, 3 * DM, m0, tn * 256, 3 * DM, 0, DM / 64, e);
  } else if (tn < 24) {
    EpiHead e{qB, 0.08838834764831845f * LOG2E};
    gemm_tile<G1>(Bx, DM, Bwq, NB, m0, (tn - 8) * 256, NB, 0, DM / 64, e);
  } else {
    const bool isv = tn >= 40;
    EpiHead e{isv ? vB : kB, 1.f};
    gemm_tile<G1>(Bckv, DC, isv ? Bwuv : Bwuk, NB, m0, (tn - (isv ? 40 : 24)) * 256, NB, 0, DC / 64, e);
  }
}

constexpr int SPLITK = 5;
__global__ __launch_bounds__(512, 2) void k_out(const u16* __restrict__ oA, const u16* __restrict__ Awo,
                                                const u16* __restrict__ ctx, const u16* __restrict__ Bwo,
                                                u16* __restrict__ out, float* __restrict__ part) {
  const int b = blockIdx.x;
  if (b < 40 * SPLITK) {
    const int tm = b & 7, rest = b >> 3, tn = rest % 5, sp = rest / 5;
    EpiF32 e{part + ((size_t)sp * 40 + tm * 5 + tn) * (256 * 128)};
    gemm_tile<G2, EpiF32, true>(ctx, DHB, Bwo, DM, tm * 256, tn * 128, DM, sp * 13 * 64, sp == 4 ? 12 : 13, e);
  } else {
    const int t = b - 40 * SPLITK, tm = t & 7, tn = t >> 3;
    EpiPlain e{out, 2 * DM, 1.f};
    gemm_tile<G2>(oA, DM, Awo, DM, tm * 256, tn * 128, DM, 0, DM / 64, e);
  }
}

__global__ __launch_bounds__(256) void k_reduce(const float* __restrict__ part, u16* __restrict__ out) {
  const int i = blockIdx.x * 256 + threadIdx.x;
  const int tile = i >> 13, w = (i >> 10) & 7, j = (i >> 6) & 15, lane = i & 63;
  const int nt = j >> 3, mt = (j >> 2) & 1, g = j & 3;
  const int tm = tile / 5, tn = tile - tm * 5;
  const int m = tm * 256 + (w >> 1) * 64 + mt * 32 + (lane & 31);
  const int n = tn * 128 + (w & 1) * 64 + nt * 32 + 8 * g + 4 * (lane >> 5);
  const f32x4* p = (const f32x4*)part + i;
  f32x4 s = p[0];
#pragma unroll
  for (int sp = 1; sp < SPLITK; ++sp) s += p[(size_t)sp * 40 * 8192];
  u32x2 v = {cvtpk(s[0], s[1]), cvtpk(s[2], s[3])};
  *(u32x2*)(out + (size_t)m * 2 * DM + DM + n) = v;
}

constexpr float THR = 8.f;
DI bf16x8 pack8(const f32x16& x, int s) {
  u32x4 w = {cvtpk(x[8 * s], x[8 * s + 1]), cvtpk(x[8 * s + 2], x[8 * s + 3]), cvtpk(x[8 * s + 4], x[8 * s + 5]),
             cvtpk(x[8 * s + 6], x[8 * s + 7])};
  return __builtin_bit_cast(bf16x8, w);
}
DI f32x16 splat16(float v) {
  f32x16 x;
#pragma unroll
  for (int i = 0; i < 16; ++i) x[i] = v;
  return x;
}
DI float max16(const f32x16& x) {
  float a = fmaxf(fmaxf(x[0], x[1]), x[2]);
#pragma unroll
  for (int i = 3; i < 15; i += 2) a = fmaxf(fmaxf(a, x[i]), x[i + 1]);
  return fmaxf(a, x[15]);
}

#define SBAR() __builtin_amdgcn_sched_barrier(0)
constexpr float BIGSUM = 4096.f;
DI float sum16(const f32x16& x) {
  float a = (x[0] + x[1]) + (x[2] + x[3]), b = (x[4] + x[5]) + (x[6] + x[7]);
  float c = (x[8] + x[9]) + (x[10] + x[11]), d = (x[12] + x[13]) + (x[14] + x[15]);
  return (a + b) + (c + d);
}
DI void exp16(f32x16& x) {
#pragma unroll
  for (int i = 0; i < 16; ++i) x[i] = __builtin_amdgcn_exp2f(x[i]);
}
DI void attnB_item(const u16* __restrict__ Q, const u16* __restrict__ K, const u16* __restrict__ V,
                   u16* __restrict__ ctx, int head, int jitem) {
  const int tid = threadIdx.x, lane = tid & 63, wid = __builtin_amdgcn_readfirstlane(tid >> 6);
  const int r = lane & 31, h = lane >> 5;
  constexpr unsigned KBUF = 0, VBUF = 32768, TB = 16384, STG = 65536;
  unsigned koff, voff;
  {
    const int l4 = lane >> 4, c = lane & 15;
    koff = (unsigned)((16 * wid + l4) * DHB + ((c ^ l4) * 8)) * 2u;
    voff = (unsigned)((16 * wid + l4) * DHB + ((c ^ (l4 << 2)) * 8)) * 2u;
  }
  const u16* Kh = K + (size_t)head * S_ * DHB;
  const u16* Vh = V + (size_t)head * S_ * DHB;
  auto stageK = [&](int buf, int t) {
    const u16* g = Kh + (size_t)t * 64 * DHB;
    dma_base(KBUF + buf * TB + wid * 4096);
    dma_piece<0>(g, koff); dma_piece_x<1024, 64>(g, koff); dma_piece_x<2048, 128>(g, koff); dma_piece_x<3072, 192>(g, koff);
  };
  auto stageV = [&](int buf, int t) {
    const u16* g = Vh + (size_t)t * 64 * DHB;
    dma_base(VBUF + buf * TB + wid * 4096);
    dma_piece<0>(g, voff); dma_piece<1024>(g, voff); dma_piece<2048>(g, voff); dma_piece<3072>(g, voff);
  };
  unsigned ka[8], va[4];
#pragma unroll
  for (int ks = 0; ks < 8; ++ks) {
    ka[ks] = KBUF + r * 256 + (((2 * ks + h) ^ (r & 15)) * 16);
    asm volatile("" : "+v"(ka[ks]));
  }
  {
    const int q = (lane & 15) >> 2, p = lane & 3, cb = (lane >> 4) & 1;
#pragma unroll
    for (int db = 0; db < 4; ++db) va[db] = VBUF + (4 * h + q) * 256 + ((db ^ q) * 64) + cb * 32 + p * 8;
  }
  bf16x8 qf[8];
  f32x16 o[4], negm;
  float mcur, lsum;
  auto loadQ = [&](int qb) {
    int lz = threadIdx.x;
    asm volatile("" : "+v"(lz));
    const u16* qp = Q + ((size_t)head * S_ + qb * 128) * DHB + (unsigned)((wid * 32 + (lz & 31)) * DHB + ((lz >> 5) & 1) * 8);
#pragma unroll
    for (int ks = 0; ks < 8; ++ks) qf[ks] = *(const bf16x8*)(qp + ks * 16);
  };
  stageK(0, 0); stageK(1, 1); stageV(0, 0);
  loadQ(15 - jitem);

  auto qk_abs = [&](unsigned off) {
    bf16x8 kf[8];
#pragma unroll
    for (int ks = 0; ks < 8; ++ks) kf[ks] = lds_read128(off + ka[ks]);
    f32x16 s = MFMA32(kf[0], qf[0], f32x16{});
#pragma unroll
    for (int ks = 1; ks < 8; ++ks) s = MFMA32(kf[ks], qf[ks], s);
    return s;
  };
  auto remax = [&](const f32x16& st, bool first, f32x16& s) {
    const float tm = xhalf_max(max16(st));
    const float mnew = first ? tm : fmaxf(tm, mcur);
    const float delta = mnew - mcur;
    const float f = __builtin_amdgcn_exp2f(-fmaxf(delta, 0.f));
    mcur = mnew;
    negm = splat16(-mnew);
#pragma unroll
    for (int i = 0; i < 16; ++i) s[i] = __builtin_amdgcn_exp2f(st[i] - mnew);
#pragma unroll
    for (int db = 0; db < 4; ++db)
#pragma unroll
      for (int i = 0; i < 16; ++i) o[db][i] *= f;
    lsum *= f;
    return delta;
  };
  auto slow = [&](int blk) {
    int lz = threadIdx.x;
    asm volatile("" : "+v"(lz));
    const u16* kp = Kh + (size_t)blk * 32 * DHB + (unsigned)((lz & 31) * DHB + ((lz >> 5) & 1) * 8);
    f32x16 st = f32x16{};
#pragma unroll
    for (int ks = 0; ks < 8; ++ks) {
      st = MFMA32(*(const bf16x8*)(kp + ks * 16), qf[ks], st);
      asm volatile("" ::: "memory");
      SBAR();
    }
    return st;
  };
  auto pv = [&](unsigned off, const f32x16& p) {
#pragma unroll
    for (int sx = 0; sx < 2; ++sx) {
      const bf16x8 pf = pack8(p, sx);
#pragma unroll
      for (int db = 0; db < 4; ++db) {
        const bf16x8 vf = cat8(lds_read_tr(off + va[db] + (16 * sx) * 256), lds_read_tr(off + va[db] + (16 * sx + 8) * 256));
        o[db] = MFMA32(vf, pf, o[db]);
      }
    }
  };
#define EXP2(x) x = __builtin_amdgcn_exp2f(x)
#define VRD(dst, off) dst = cat8(lds_read_tr(off), lds_read_tr((off) + 2048))

#pragma unroll 1
  for (int pass = 0; pass < 2; ++pass) {
    const int qb = pass ? jitem : 15 - jitem;
    const int ntile = 2 * qb + 2;
    const int kbd = 4 * qb + wid;
    auto maskblk = [&](f32x16& s, int kb) {
      int lz = threadIdx.x;
      asm volatile("" : "+v"(lz));
      const int dq = (kbd - kb) * 32 + (lz & 31) - 4 * ((lz >> 5) & 1);
#pragma unroll
      for (int i = 0; i < 16; ++i)
        if ((i & 3) + 8 * (i >> 2) > dq) s[i] = -__builtin_inff();
    };
    {
      float z = 0.f;
      asm volatile("" : "+v"(z));
#pragma unroll
      for (int db = 0; db < 4; ++db) o[db] = splat16(z);
      lsum = z;
    }
#pragma unroll
    for (int ks = 0; ks < 8; ++ks) asm volatile("" : "+v"(qf[ks]));
    WAIT_VM0();
    BARRIER();
    {
      f32x16 sa, sb;
      bf16x8 kg[8];
      float pa;
      {
        f32x16 st = qk_abs(0);
        if (kbd == 0) maskblk(st, 0);
        const float m = xhalf_max(max16(st));
        mcur = m;
        negm = splat16(-m);
#pragma unroll
        for (int i = 0; i < 8; ++i) sa[i] = __builtin_amdgcn_exp2f(st[i] - m);
#pragma unroll
        for (int i = 8; i < 16; ++i) sa[i] = st[i] - m;
        pa = ((sa[0] + sa[1]) + (sa[2] + sa[3])) + ((sa[4] + sa[5]) + (sa[6] + sa[7]));
#pragma unroll
        for (int ks = 0; ks < 8; ++ks) kg[ks] = lds_read128(ka[ks] + 8192);
      }
      auto tile = [&](auto BUFC, auto LASTC, int t) {
        constexpr unsigned B_ = decltype(BUFC)::value;
        constexpr bool LAST = decltype(LASTC)::value;
        constexpr unsigned vo = B_ * TB, vn = TB - vo;
        constexpr unsigned kn = TB - vo, kd = vo;
        const u16* kbn = Kh + (size_t)min(t + 2, 31) * 64 * DHB;
        const u16* vbn = Vh + (size_t)min(t + 1, 31) * 64 * DHB;
#define DMAK(i) do { if ((i) == 0) { dma_base(KBUF + kd + wid * 4096); dma_piece<0>(kbn, koff); } else dma_piece_x<(i) * 1024, (i) * 64>(kbn, koff); } while (0)
#define DMAV(i) do { if ((i) == 0) dma_base(VBUF + vn + wid * 4096); dma_piece<(i) * 1024>(vbn, voff); } while (0)
        bf16x8 kf[8], vf[8], vg[8];
#define KF(i) kf[i] = lds_read128(kn + ka[i])
#define KG(i) kg[i] = lds_read128(kn + ka[i] + 8192)
        sb = MFMA32(kg[0], qf[0], negm);
        VRD(vf[0], vo + va[0]); DMAV(0);
        EXP2(sa[8]); EXP2(sa[9]);
        SBAR();
        sb = MFMA32(kg[1], qf[1], sb);
        VRD(vf[1], vo + va[1]); DMAV(1);
        EXP2(sa[10]); EXP2(sa[11]);
        SBAR();
        sb = MFMA32(kg[2], qf[2], sb);
        VRD(vf[2], vo + va[2]); DMAV(2);
        EXP2(sa[12]); EXP2(sa[13]);
        float pa2 = (sa[8] + sa[9]) + (sa[10] + sa[11]);
        SBAR();
        sb = MFMA32(kg[3], qf[3], sb);
        VRD(vf[3], vo + va[3]); DMAV(3);
        EXP2(sa[14]); EXP2(sa[15]);
        pa += sa[12] + sa[13];
        SBAR();
        sb = MFMA32(kg[4], qf[4], sb);
        DMAK(0);
        pa2 += sa[14] + sa[15];
        pa += pa2;
        if (__any(!(pa <= BIGSUM))) {
          f32x16 st = slow(2 * t);
          if (2 * t >= kbd) maskblk(st, 2 * t);
          const float delta = remax(st, false, sa);
#pragma unroll
          for (int i = 0; i < 16; ++i) sb[i] -= delta;
          pa = sum16(sa);
        }
        sb = MFMA32(kg[5], qf[5], sb);
        VRD(vf[4], vo + va[0] + 4096); VRD(vf[5], vo + va[1] + 4096);
        const bf16x8 p0 = pack8(sa, 0);
        lsum += pa;
        SBAR();
        sb = MFMA32(kg[6], qf[6], sb);
        VRD(vf[6], vo + va[2] + 4096); DMAK(1);
        const bf16x8 p1 = pack8(sa, 1);
        SBAR();
        sb = MFMA32(kg[7], qf[7], sb);
        VRD(vf[7], vo + va[3] + 4096); DMAK(2);
        SBAR();
        o[0] = MFMA32(vf[0], p0, o[0]);
        KF(0); DMAK(3);
        if (2 * t + 1 >= kbd) maskblk(sb, 2 * t + 1);
        SBAR();
        o[1] = MFMA32(vf[1], p0, o[1]);
        KF(1);
        EXP2(sb[0]); EXP2(sb[1]); EXP2(sb[2]);
        SBAR();
        o[2] = MFMA32(vf[2], p0, o[2]);
        KF(2);
        EXP2(sb[3]); EXP2(sb[4]);
        SBAR();
        o[3] = MFMA32(vf[3], p0, o[3]);
        KF(3);
        EXP2(sb[5]); EXP2(sb[6]);
        float pb = (sb[0] + sb[1]) + (sb[2] + sb[3]);
        SBAR();
        o[0] = MFMA32(vf[4], p1, o[0]);
        KF(4);
        EXP2(sb[7]); EXP2(sb[8]);
        SBAR();
        o[1] = MFMA32(vf[5], p1, o[1]);
        KF(5);
        EXP2(sb[9]); EXP2(sb[10]);
        float pb2 = (sb[4] + sb[5]) + (sb[6] + sb[7]);
        SBAR();
        o[2] = MFMA32(vf[6], p1, o[2]);
        KF(6);
        EXP2(sb[11]); EXP2(sb[12]);
        SBAR();
        o[3] = MFMA32(vf[7], p1, o[3]);
        KF(7);
        EXP2(sb[13]); EXP2(sb[14]);
        pb += (sb[8] + sb[9]) + sb[10];
        SBAR();
        sa = MFMA32(kf[0], qf[0], negm);
        VRD(vg[0], vo + va[0] + 8192);
        EXP2(sb[15]);
        pb2 += (sb[11] + sb[12]) + sb[13];
        SBAR();
        sa = MFMA32(kf[1], qf[1], sa);
        VRD(vg[1], vo + va[1] + 8192);
        pb += sb[14] + sb[15];
        pb += pb2;
        if (__any(!(pb <= BIGSUM))) {
          f32x16 st = slow(2 * t + 1);
          if (2 * t + 1 >= kbd) maskblk(st, 2 * t + 1);
          const float delta = remax(st, false, sb);
#pragma unroll
          for (int i = 0; i < 16; ++i) sa[i] -= delta;
          pb = sum16(sb);
        }
        sa = MFMA32(kf[2], qf[2], sa);
        VRD(vg[2], vo + va[2] + 8192);
        const bf16x8 r0 = pack8(sb, 0);
        lsum += pb;
        SBAR();
        sa = MFMA32(kf[3], qf[3], sa);
        VRD(vg[3], vo + va[3] + 8192);
        const bf16x8 r1 = pack8(sb, 1);
        SBAR();
        sa = MFMA32(kf[4], qf[4], sa);
        VRD(vg[4], vo + va[0] + 12288);
        SBAR();
        sa = MFMA32(kf[5], qf[5], sa);
        VRD(vg[5], vo + va[1] + 12288);
        SBAR();
        sa = MFMA32(kf[6], qf[6], sa);
        VRD(vg[6], vo + va[2] + 12288);
        SBAR();
        sa = MFMA32(kf[7], qf[7], sa);
        VRD(vg[7], vo + va[3] + 12288);
        SBAR();
        o[0] = MFMA32(vg[0], r0, o[0]);
        KG(0);
        if (2 * t + 2 >= kbd) maskblk(sa, 2 * t + 2);
        SBAR();
        o[1] = MFMA32(vg[1], r0, o[1]);
        KG(1); EXP2(sa[0]); EXP2(sa[1]);
        SBAR();
        o[2] = MFMA32(vg[2], r0, o[2]);
        KG(2); EXP2(sa[2]); EXP2(sa[3]);
        SBAR();
        o[3] = MFMA32(vg[3], r0, o[3]);
        KG(3); EXP2(sa[4]); EXP2(sa[5]);
        SBAR();
        o[0] = MFMA32(vg[4], r1, o[0]);
        KG(4); EXP2(sa[6]); EXP2(sa[7]);
        pa = (sa[0] + sa[1]) + (sa[2] + sa[3]);
        SBAR();
        o[1] = MFMA32(vg[5], r1, o[1]);
        KG(5);
        pa += (sa[4] + sa[5]) + (sa[6] + sa[7]);
        SBAR();
        o[2] = MFMA32(vg[6], r1, o[2]);
        KG(6); KG(7);
        SBAR();
        o[3] = MFMA32(vg[7], r1, o[3]);
#undef KF
#undef KG
        WAIT_VM0();
        BARRIER();
#undef DMAK
#undef DMAV
      };
      typedef std::integral_constant<unsigned, 0> C0;
      typedef std::integral_constant<unsigned, 1> C1;
      int t = 0;
      do {
        tile(C0{}, std::false_type{}, t);
        tile(C1{}, std::false_type{}, t + 1);
        t += 2;
      } while (t < ntile);
    }
    if (pass == 0) { stageK(0, 0); stageK(1, 1); stageV(0, 0); }
    const float inv = 1.f / xhalf_sum(lsum);
    {
      int lz = threadIdx.x;
      asm volatile("" : "+v"(lz));
      const int lane = lz & 63, r = lz & 31, h = (lz >> 5) & 1;
      const unsigned sb_ = STG + wid * 4096;
      u16* op = ctx + ((size_t)head * S_ + qb * 128 + wid * 32 + (lane >> 3)) * DHB + (lane & 7) * 8;
#pragma unroll
      for (int hf = 0; hf < 2; ++hf) {
#pragma unroll
        for (int d2 = 0; d2 < 2; ++d2)
#pragma unroll
          for (int g = 0; g < 4; ++g) {
            const f32x16& x = o[2 * hf + d2];
            u32x2 v = {cvtpk(x[4 * g] * inv, x[4 * g + 1] * inv), cvtpk(x[4 * g + 2] * inv, x[4 * g + 3] * inv)};
            *(lds_u32x2*)(uintptr_t)(sb_ + r * 128 + (((4 * d2 + g) ^ (r & 7)) * 16) + h * 8) = v;
          }
#pragma unroll
        for (int it = 0; it < 4; ++it) {
          const int rr = it * 8 + (lane >> 3);
          const bf16x8 v = lds_read128(sb_ + rr * 128 + (((lane & 7) ^ (rr & 7)) * 16));
          *(bf16x8*)(op + (size_t)it * 8 * DHB + hf * 64) = v;
        }
      }
    }
    if (pass == 0) loadQ(jitem);
  }
#undef EXP2
#undef VRD
}

DI void attnA_block(const u16* __restrict__ qkv, u16* __restrict__ oA, int head, int qb) {
  const int tid = threadIdx.x, lane = tid & 63, wid = __builtin_amdgcn_readfirstlane(tid >> 6);
  const int r = lane & 31, h = lane >> 5;
  const int q0 = qb * 256 + wid * 64;
  constexpr unsigned KBUF = 0, VBUF = 16384, TB = 8192;
  const u16* Qh = qkv + (size_t)(0 * HA + head) * S_ * 32;
  const u16* Kh = qkv + (size_t)(1 * HA + head) * S_ * 32;
  const u16* Vh = qkv + (size_t)(2 * HA + head) * S_ * 32;
  unsigned koff, voff;
  {
    const int row = 32 * wid + (lane >> 2), c = lane & 3;
    koff = (unsigned)(row * 64 + ((c ^ ((row >> 2) & 3)) * 16));
    voff = (unsigned)(row * 64 + c * 16);
  }
  auto stage = [&](int buf, int t) {
    dma_base(KBUF + buf * TB + wid * 2048);
    dma_piece<0>(Kh + (size_t)t * 128 * 32, koff); dma_piece<1024>(Kh + (size_t)t * 128 * 32, koff);
    dma_base(VBUF + buf * TB + wid * 2048);
    dma_piece<0>(Vh + (size_t)t * 128 * 32, voff); dma_piece<1024>(Vh + (size_t)t * 128 * 32, voff);
  };
  stage(0, 0);
  bf16x8 qf[2][2];
#pragma unroll
  for (int qt = 0; qt < 2; ++qt)
#pragma unroll
    for (int ks = 0; ks < 2; ++ks) qf[qt][ks] = *(const bf16x8*)(Qh + (size_t)(q0 + 32 * qt + r) * 32 + ks * 16 + h * 8);
  unsigned ka[2], va;
#pragma unroll
  for (int ks = 0; ks < 2; ++ks) {
    ka[ks] = KBUF + r * 64 + (((2 * ks + h) ^ ((r >> 2) & 3)) * 16);
    asm volatile("" : "+v"(ka[ks]));
  }
  {
    const int q = (lane & 15) >> 2, p = lane & 3, cb = (lane >> 4) & 1;
    va = VBUF + (4 * h + q) * 64 + cb * 32 + p * 8;
  }
  f32x16 o[2], negm[2];
  float mcur[2];
#pragma unroll
  for (int qt = 0; qt < 2; ++qt) { o[qt] = f32x16{}; negm[qt] = f32x16{}; mcur[qt] = 0.f; }
#pragma unroll
  for (int qt = 0; qt < 2; ++qt) { asm volatile("" : "+v"(qf[qt][0])); asm volatile("" : "+v"(qf[qt][1])); }
  WAIT_VM0();
  BARRIER();
  constexpr int NKT = S_ / 128;
#define EXP2(x) x = __builtin_amdgcn_exp2f(x)
#define EXP4(s, i) EXP2(s[i]); EXP2(s[i + 1]); EXP2(s[i + 2]); EXP2(s[i + 3])
#define VRDA(dst, off) dst = cat8(lds_read_tr(off), lds_read_tr((off) + 512))
  auto slow_tile = [&](bool first) {
#pragma unroll 1
    for (int kb = 0; kb < 4; ++kb) {
      const bf16x8 k0 = lds_read128(ka[0] + kb * 2048), k1 = lds_read128(ka[1] + kb * 2048);
      bf16x8 v0, v1;
      VRDA(v0, va + kb * 2048); VRDA(v1, va + kb * 2048 + 1024);
#pragma unroll
      for (int qt = 0; qt < 2; ++qt) {
        f32x16 st = MFMA32(k0, qf[qt][0], f32x16{});
        st = MFMA32(k1, qf[qt][1], st);
        const float tm = xhalf_max(max16(st));
        const float mnew = (first && kb == 0) ? tm : fmaxf(tm, mcur[qt]);
        const float f = __builtin_amdgcn_exp2f(-fmaxf(mnew - mcur[qt], 0.f));
        mcur[qt] = mnew;
#pragma unroll
        for (int i = 0; i < 16; ++i) { st[i] = __builtin_amdgcn_exp2f(st[i] - mnew); o[qt][i] *= f; }
        o[qt] = MFMA32(v0, pack8(st, 0), o[qt]);
        o[qt] = MFMA32(v1, pack8(st, 1), o[qt]);
      }
    }
    negm[0] = splat16(-mcur[0]);
    negm[1] = splat16(-mcur[1]);
  };
  for (int t = 0; t < NKT; ++t) {
    if (t + 1 < NKT) stage((t + 1) & 1, t + 1);
    const f32x16 os0 = o[0], os1 = o[1];
    f32x16 sa0, sa1, sb0, sb1;
    bf16x8 k0, k1, v0, v1;
    k0 = lds_read128(ka[0]); k1 = lds_read128(ka[1]);
    sa0 = MFMA32(k0, qf[0][0], negm[0]); sa1 = MFMA32(k0, qf[1][0], negm[1]);
    sa0 = MFMA32(k1, qf[0][1], sa0); sa1 = MFMA32(k1, qf[1][1], sa1);
    k0 = lds_read128(ka[0] + 2048); k1 = lds_read128(ka[1] + 2048);
#define UNIT(C0, C1, N0, N1, kb)                                                                   \
    do {                                                                                             \
      VRDA(v0, va + (kb) * 2048); VRDA(v1, va + (kb) * 2048 + 1024);                                 \
      SBAR();                                                                                        \
      if ((kb) < 3) N0 = MFMA32(k0, qf[0][0], negm[0]);                                              \
      EXP4(C0, 0);                                                                                   \
      SBAR();                                                                                        \
      if ((kb) < 3) N1 = MFMA32(k0, qf[1][0], negm[1]);                                              \
      EXP4(C0, 4);                                                                                   \
      SBAR();                                                                                        \
      if ((kb) < 3) N0 = MFMA32(k1, qf[0][1], N0);                                                   \
      EXP4(C0, 8);                                                                                   \
      const bf16x8 p00 = pack8(C0, 0);                                                               \
      SBAR();                                                                                        \
      if ((kb) < 3) N1 = MFMA32(k1, qf[1][1], N1);                                                   \
      EXP4(C0, 12);                                                                                  \
      if ((kb) < 2) { k0 = lds_read128(ka[0] + ((kb) + 2) * 2048); k1 = lds_read128(ka[1] + ((kb) + 2) * 2048); } \
      SBAR();                                                                                        \
      o[0] = MFMA32(v0, p00, o[0]);                                                                  \
      EXP4(C1, 0);                                                                                   \
      const bf16x8 p01 = pack8(C0, 1);                                                               \
      SBAR();                                                                                        \
      o[0] = MFMA32(v1, p01, o[0]);                                                                  \
      EXP4(C1, 4); EXP4(C1, 8);                                                                      \
      const bf16x8 p10 = pack8(C1, 0);                                                               \
      SBAR();                                                                                        \
      o[1] = MFMA32(v0, p10, o[1]);                                                                  \
      EXP4(C1, 12);                                                                                  \
      const bf16x8 p11 = pack8(C1, 1);                                                               \
      SBAR();                                                                                        \
      o[1] = MFMA32(v1, p11, o[1]);                                                                  \
    } while (0)
    UNIT(sa0, sa1, sb0, sb1, 0);
    UNIT(sb0, sb1, sa0, sa1, 1);
    UNIT(sa0, sa1, sb0, sb1, 2);
    UNIT(sb0, sb1, sa0, sa1, 3);
#undef UNIT
    const float lim = t == 0 ? -1.f : 1048576.f;
    const bool bad = h && !((o[0][8] - os0[8] <= lim) && (o[1][8] - os1[8] <= lim));
    if (__any(bad)) {
      o[0] = os0; o[1] = os1;
      slow_tile(t == 0);
    }
    ka[0] ^= TB; ka[1] ^= TB; va ^= TB;
    WAIT_VM0();
    BARRIER();
  }
#undef EXP2
#undef EXP4
#undef VRDA
#pragma unroll
  for (int qt = 0; qt < 2; ++qt) {
    const float l = __shfl(o[qt][8], r + 32);
    const float inv = 1.f / l;
    u16* op = oA + (size_t)(q0 + 32 * qt + r) * DM + head * DHA + 4 * h;
#pragma unroll
    for (int g = 0; g < 3; ++g) {
      if (g < 2 || h == 0) {
        u32x2 v = {cvtpk(o[qt][4 * g] * inv, o[qt][4 * g + 1] * inv), cvtpk(o[qt][4 * g + 2] * inv, o[qt][4 * g + 3] * inv)};
        *(u32x2*)(op + 8 * g) = v;
      }
    }
  }
}

constexpr int AQT = 2;
constexpr int NBLK_B = HB * 8, NBLK_A = HA * (S_ / (128 * AQT));
__global__ __launch_bounds__(256, 2) void k_attn(const u16* __restrict__ qB, const u16* __restrict__ kB,
                                                 const u16* __restrict__ vB, u16* __restrict__ ctx,
                                                 const u16* __restrict__ qkvA, u16* __restrict__ oA) {
  const int b = blockIdx.x;
  if (b < NBLK_B) {
    const int head = (b & 7) + 8 * (b >> 6), j = (b >> 3) & 7;
    attnB_item(qB, kB, vB, ctx, head, j);
  } else {
    const int a = b - NBLK_B;
    constexpr int PER = S_ / (128 * AQT);
    const int head = (a & 7) + 8 * (a / (8 * PER)), qb = (a >> 3) % PER;
    attnA_block(qkvA, oA, head, qb);
  }
}

extern "C" void kernel_launch(void* const* d_in, const int* in_sizes, int n_in, void* d_out, int out_size, void* d_ws,
                              size_t ws_size, hipStream_t stream) {
  const u16* Ax = (const u16*)d_in[0];
  const u16* Awqkv = (const u16*)d_in[1];
  const u16* Awo = (const u16*)d_in[2];
  const u16* Bx = (const u16*)d_in[3];
  const u16* Bckv = (const u16*)d_in[4];
  const u16* Bwq = (const u16*)d_in[5];
  const u16* Bwuk = (const u16*)d_in[6];
  const u16* Bwuv = (const u16*)d_in[7];
  const u16* Bwo = (const u16*)d_in[8];
  u16* out = (u16*)d_out;
  char* ws = (char*)d_ws;
  u16* qkvA = (u16*)ws;                 ws += (size_t)3 * HA * S_ * 32 * 2;
  u16* qB = (u16*)ws;                   ws += (size_t)S_ * NB * 2;
  u16* kB = (u16*)ws;                   ws += (size_t)S_ * NB * 2;
  u16* vB = (u16*)ws;                   ws += (size_t)S_ * NB * 2;
  u16* ctx = (u16*)ws;                  ws += (size_t)S_ * NB * 2;
  u16* oA = (u16*)ws;                   ws += (size_t)S_ * DM * 2;
  float* part = (float*)ws;             ws += (size_t)SPLITK * S_ * DM * 4;

  (void)hipFuncSetAttribute((const void*)k_proj, hipFuncAttributeMaxDynamicSharedMemorySize, G1::LDS);
  (void)hipFuncSetAttribute((const void*)k_out, hipFuncAttributeMaxDynamicSharedMemorySize, G2::LDS);
  (void)hipFuncSetAttribute((const void*)k_attn, hipFuncAttributeMaxDynamicSharedMemorySize, 81920);
  k_proj<<<448, 512, G1::LDS, stream>>>(Ax, Awqkv, Bx, Bckv, Bwq, Bwuk, Bwuv, qkvA, qB, kB, vB);
  k_attn<<<NBLK_B + NBLK_A, 256, 81920, stream>>>(qB, kB, vB, ctx, qkvA, oA);
  k_out<<<40 * SPLITK + 40, 512, G2::LDS, stream>>>(oA, Awo, ctx, Bwo, out, part);
  k_reduce<<<S_ * DM / 4 / 256, 256, 0, stream>>>(part, out);
}
```

```cpp
#include <hip/hip_runtime.h>
#include <hip/hip_bf16.h>
#include <stdint.h>
#include <type_traits>

constexpr int S_ = 2048;
constexpr int DM = 640;
constexpr int HA = 32, DHA = 20;
constexpr int HB = 32, DHB = 128, DC = 512;
constexpr int NB = HB * DHB;

typedef unsigned short u16;
typedef short bf16x8 __attribute__((ext_vector_type(8)));
typedef short s16x4 __attribute__((ext_vector_type(4)));
typedef float f32x16 __attribute__((ext_vector_type(16)));
typedef float f32x4 __attribute__((ext_vector_type(4)));
typedef float f32x2 __attribute__((ext_vector_type(2)));
typedef __bf16 bf16x2 __attribute__((ext_vector_type(2)));
typedef unsigned u32x2 __attribute__((ext_vector_type(2)));
typedef unsigned u32x4 __attribute__((ext_vector_type(4)));
typedef __attribute__((address_space(3))) s16x4 lds_s16x4;
typedef __attribute__((address_space(3))) bf16x8 lds_bf16x8;
typedef __attribute__((address_space(3))) u32x2 lds_u32x2;

#define DI __device__ __forceinline__
#define MFMA32(a, b, c) __builtin_amdgcn_mfma_f32_32x32x16_bf16((a), (b), (c), 0, 0, 0)

DI unsigned cvtpk(float lo, float hi) {
  f32x2 v = {lo, hi};
  bf16x2 b = __builtin_convertvector(v, bf16x2);
  return __builtin_bit_cast(unsigned, b);
}
DI void dma16(const void* sbase, unsigned voff, unsigned lds_addr) {
  unsigned keep;
  asm volatile("s_mov_b32 %0, m0\n\ts_mov_b32 m0, %3\n\ts_nop 0\n\tglobal_load_lds_dwordx4 %1, %2\n\ts_mov_b32 m0, %0"
               : "=&s"(keep) : "v"(voff), "s"(sbase), "s"(lds_addr) : "memory");
}
DI void dma_base(unsigned lds_addr) { asm volatile("s_mov_b32 m0, %0\n\ts_nop 0" :: "s"(lds_addr) : "memory"); }
template <int OFF> DI void dma_piece(const void* sbase, unsigned voff) {
  asm volatile("global_load_lds_dwordx4 %0, %1 offset:%2" :: "v"(voff), "s"(sbase), "i"(OFF) : "memory");
}
template <int OFF, int X> DI void dma_piece_x(const void* sbase, unsigned voff) {
  unsigned t;
  asm volatile("v_xor_b32 %0, %4, %1\n\tglobal_load_lds_dwordx4 %0, %2 offset:%3" : "=&v"(t) : "v"(voff), "s"(sbase), "i"(OFF), "i"(X) : "memory");
}
#define WAIT_VM0() asm volatile("s_waitcnt vmcnt(0)" ::: "memory")
#define BARRIER() do { asm volatile("s_waitcnt lgkmcnt(0)" ::: "memory"); __builtin_amdgcn_s_barrier(); asm volatile("" ::: "memory"); } while (0)

DI bf16x8 lds_read128(unsigned off) { return *(const lds_bf16x8*)(uintptr_t)off; }
DI s16x4 lds_read_tr(unsigned off) { return __builtin_amdgcn_ds_read_tr16_b64_v4i16((lds_s16x4*)(uintptr_t)off); }
DI bf16x8 cat8(s16x4 a, s16x4 b) { return __builtin_shufflevector(a, b, 0, 1, 2, 3, 4, 5, 6, 7); }
DI float xhalf_max(float x) {
  auto r = __builtin_amdgcn_permlane32_swap(__float_as_uint(x), __float_as_uint(x), false, false);
  return fmaxf(__uint_as_float(r[0]), __uint_as_float(r[1]));
}
DI float xhalf_sum(float x) {
  auto r = __builtin_amdgcn_permlane32_swap(__float_as_uint(x), __float_as_uint(x), false, false);
  return __uint_as_float(r[0]) + __uint_as_float(r[1]);
}

template <int BM, int BN, int WM, int WN, int NST_>
struct GCfg {
  static constexpr int NST = NST_;
  static constexpr int NW = WM * WN, TM = BM / WM, TN = BN / WN, MT = TM / 32, NT = TN / 32;
  static constexpr int XB = BM * 128, WROW = BN * 2, WB = 64 * WROW, STAGE = XB + WB;
  static constexpr int XI = BM / 8 / NW;
  static constexpr int WI = WB / 1024 / NW;
  static constexpr int WCH = BN / 8;
  static constexpr int WRPI = 64 / WCH;
  static constexpr int LDS = NST * STAGE;
};

template <class C, class Epi, bool XHEAD = false>
DI void gemm_tile(const u16* __restrict__ X, int ldx, const u16* __restrict__ W, int ldw, int m0, int n0, int ncols,
                  int kbeg, int nk, const Epi& epi) {
  const int tid = threadIdx.x, lane = tid & 63, wid = __builtin_amdgcn_readfirstlane(tid >> 6);
  const int wm = wid / C::WN_, wn = wid % C::WN_;
  const int r = lane & 31, h = lane >> 5;
  unsigned xoff[C::XI], woff[C::WI];
#pragma unroll
  for (int i = 0; i < C::XI; ++i) {
    const int row = 8 * (wid * C::XI + i) + (lane >> 3);
    xoff[i] = (unsigned)(row * ldx + (((lane & 7) ^ ((row >> 1) & 7)) * 8)) * 2u;
  }
#pragma unroll
  for (int i = 0; i < C::WI; ++i) {
    const int k = C::WRPI * (wid * C::WI + i) + lane / C::WCH;
    int col = (((lane % C::WCH) ^ ((k & 3) << 2)) * 8);
    if (n0 + col > ncols - 8) col = ncols - 8 - n0;
    woff[i] = (unsigned)(k * ldw + col) * 2u;
  }
  const u16* xs = XHEAD ? X + (size_t)m0 * ldx : X + (size_t)m0 * ldx + kbeg;
  const int kt0 = kbeg >> 6;
  auto xk = [&](int kt) { return XHEAD ? xs + (size_t)((kt + kt0) >> 1) * S_ * DHB + ((kt + kt0) & 1) * 64 : xs + kt * 64; };
  const u16* ws = W + (size_t)kbeg * ldw + n0;
  auto stage = [&](int buf, int kt) {
    const u16* xb = xk(kt);
    const u16* wb = ws + (size_t)kt * 64 * ldw;
    const unsigned lb = buf * C::STAGE;
#pragma unroll
    for (int i = 0; i < C::XI; ++i) dma16(xb, xoff[i], lb + (wid * C::XI + i) * 1024);
#pragma unroll
    for (int i = 0; i < C::WI; ++i) dma16(wb, woff[i], lb + C::XB + (wid * C::WI + i) * 1024);
  };
  unsigned xa[4];
#pragma unroll
  for (int ks = 0; ks < 4; ++ks) xa[ks] = (wm * C::TM + r) * 128 + (((2 * ks + h) ^ ((r >> 1) & 7)) * 16);
  unsigned wa[C::NT];
  {
    const int q = (lane & 15) >> 2, p = lane & 3, cb = (lane >> 4) & 1;
#pragma unroll
    for (int nt = 0; nt < C::NT; ++nt) {
      const int nb = wn * C::NT + nt;
      wa[nt] = C::XB + (8 * h + q) * C::WROW + ((nb ^ q) * 64) + cb * 32 + p * 8;
    }
  }
  f32x16 acc[C::NT][C::MT];
#pragma unroll
  for (int nt = 0; nt < C::NT; ++nt)
#pragma unroll
    for (int mt = 0; mt < C::MT; ++mt) acc[nt][mt] = f32x16{};

  constexpr int AHEAD = C::NST - 1, PER = C::XI + C::WI;
  stage(0, 0);
  if constexpr (AHEAD == 2) { stage(1, 1); asm volatile("s_waitcnt vmcnt(%0)" :: "i"(PER) : "memory"); }
  else WAIT_VM0();
  BARRIER();
  int cur = 0, nxt = AHEAD;
  for (int kt = 0; kt < nk; ++kt) {
    const unsigned lb = cur * C::STAGE;
    const bool more = kt + AHEAD < nk;
    const u16* xb = xk(kt + AHEAD);
    const u16* wb = ws + (size_t)(kt + AHEAD) * 64 * ldw;
    const unsigned nb = nxt * C::STAGE;
    cur = cur + 1 == C::NST ? 0 : cur + 1;
    nxt = nxt + 1 == C::NST ? 0 : nxt + 1;
#pragma unroll
    for (int ks = 0; ks < 4; ++ks) {
      bf16x8 xf[C::MT], wf[C::NT];
#pragma unroll
      for (int mt = 0; mt < C::MT; ++mt) xf[mt] = lds_read128(lb + xa[ks] + mt * 4096);
#pragma unroll
      for (int nt = 0; nt < C::NT; ++nt)
        wf[nt] = cat8(lds_read_tr(lb + wa[nt] + (16 * ks) * C::WROW), lds_read_tr(lb + wa[nt] + (16 * ks + 4) * C::WROW));
      if (more) {
        constexpr int TOT = C::XI + C::WI;
#pragma unroll
        for (int i = ks * TOT / 4; i < (ks + 1) * TOT / 4; ++i) {
          if (i < C::XI) dma16(xb, xoff[i], nb + (wid * C::XI + i) * 1024);
          else dma16(wb, woff[i - C::XI], nb + C::XB + (wid * C::WI + i - C::XI) * 1024);
        }
      }
#pragma unroll
      for (int nt = 0; nt < C::NT; ++nt)
#pragma unroll
        for (int mt = 0; mt < C::MT; ++mt) acc[nt][mt] = MFMA32(wf[nt], xf[mt], acc[nt][mt]);
    }
    if (AHEAD == 2 && more) asm volatile("s_waitcnt vmcnt(%0)" :: "i"(PER) : "memory");
    else WAIT_VM0();
    BARRIER();
  }
  if constexpr (Epi::DIRECT) {
    f32x4* dst = (f32x4*)epi.out + (size_t)wid * (C::NT * C::MT * 4 * 64) + lane;
#pragma unroll
    for (int nt = 0; nt < C::NT; ++nt)
#pragma unroll
      for (int mt = 0; mt < C::MT; ++mt)
#pragma unroll
        for (int g = 0; g < 4; ++g) {
          f32x4 v = {acc[nt][mt][4 * g], acc[nt][mt][4 * g + 1], acc[nt][mt][4 * g + 2], acc[nt][mt][4 * g + 3]};
          dst[((nt * C::MT + mt) * 4 + g) * 64] = v;
        }
  } else {
    static_assert(C::TN == 64, "staging layout");
    const unsigned sb = wid * (C::TM * 128);
#pragma unroll
    for (int nt = 0; nt < C::NT; ++nt)
#pragma unroll
      for (int g = 0; g < 4; ++g) {
        const int nl = nt * 32 + 8 * g + 4 * h;
        const float sc = epi.scale_of(n0 + wn * C::TN + nl);
        const unsigned a = sb + r * 128 + (((nl >> 3) ^ (r & 7)) * 16) + (nl & 4) * 2;
#pragma unroll
        for (int mt = 0; mt < C::MT; ++mt) {
          u32x2 v = {cvtpk(acc[nt][mt][4 * g] * sc, acc[nt][mt][4 * g + 1] * sc),
                     cvtpk(acc[nt][mt][4 * g + 2] * sc, acc[nt][mt][4 * g + 3] * sc)};
          *(lds_u32x2*)(uintptr_t)(a + mt * 4096) = v;
        }
      }
    epi.template store<C::TM>(sb, m0 + wm * C::TM, n0 + wn * C::TN, lane);
  }
}

template <int BM, int BN, int WM, int WN, int NST>
struct GC : GCfg<BM, BN, WM, WN, NST> { static constexpr int WN_ = WN; };

struct EpiPlain {
  static constexpr bool DIRECT = false, PREFW = false;
  u16* out; int ld; float scale;
  DI float scale_of(int) const { return scale; }
  template <int TM> DI void store(unsigned sb, int mb, int nb, int lane) const {
    const int rr = lane >> 3, c = lane & 7;
    u16* dst = out + (size_t)(mb + rr) * ld + nb + c * 8;
    const unsigned a = sb + rr * 128 + ((c ^ (rr & 7)) * 16);
#pragma unroll
    for (int it = 0; it < TM / 8; ++it) {
      const bf16x8 v = lds_read128(a + it * 1024);
      *(bf16x8*)(dst + (size_t)it * 8 * ld) = v;
    }
  }
};
struct EpiHead {
  static constexpr bool DIRECT = false, PREFW = true;
  u16* out; float scale;
  DI float scale_of(int) const { return scale; }
  template <int TM> DI void store(unsigned sb, int mb, int nb, int lane) const {
    const int rr = lane >> 3, c = lane & 7;
    u16* dst = out + ((size_t)(nb >> 7) * S_ + mb + rr) * DHB + (nb & 127) + c * 8;
    const unsigned a = sb + rr * 128 + ((c ^ (rr & 7)) * 16);
#pragma unroll
    for (int it = 0; it < TM / 8; ++it) {
      const bf16x8 v = lds_read128(a + it * 1024);
      *(bf16x8*)(dst + (size_t)it * 8 * DHB) = v;
    }
  }
};
struct EpiQKVA {
  static constexpr bool DIRECT = false, PREFW = true;
  u16* out; float qscale;
  DI float scale_of(int n) const { return n < DM ? qscale : 1.f; }
  template <int TM> DI void store(unsigned sb, int mb, int nb, int lane) const {
    const int rr = lane >> 4, gq = lane & 15;
    const int n = nb + gq * 4;
    if (n >= 3 * DM) return;
    const int which = n / DM, rem = n - which * DM, head = rem / DHA, dd = rem - head * DHA;
    u16* dst = out + ((size_t)((which * HA + head) * S_ + mb + rr)) * 32;
#pragma unroll 4
    for (int it = 0; it < TM / 4; ++it) {
      const int row = it * 4 + rr;
      const u32x2 v = *(const lds_u32x2*)(uintptr_t)(sb + row * 128 + (((gq >> 1) ^ (row & 7)) * 16) + (gq & 1) * 8);
      u16* d = dst + it * 4 * 32;
      *(u32x2*)(d + dd) = v;
      if (dd == 16) {
        u32x2 z0 = {which == 2 ? 0x3F80u : 0u, 0u};
        u32x4 z1 = {0u, 0u, 0u, 0u};
        *(u32x2*)(d + 20) = z0;
        *(u32x4*)(d + 24) = z1;
      }
    }
  }
};
struct EpiF32 {
  static constexpr bool DIRECT = true, PREFW = false;
  float* out;
};

typedef GC<256, 256, 2, 4, 2> G1;
typedef GC<256, 128, 4, 2, 3> G2;

constexpr float LOG2E = 1.4426950408889634f;

struct PUnit { const u16* x; const u16* w; u16* out; int ldx, ldw, n0, ncols, nk, kind; float scale; };
__global__ __launch_bounds__(512, 2) void k_proj(const u16* __restrict__ Ax, const u16* __restrict__ Awqkv,
                                                 const u16* __restrict__ Bx, const u16* __restrict__ Bckv,
                                                 const u16* __restrict__ Bwq, const u16* __restrict__ Bwuk,
                                                 const u16* __restrict__ Bwuv, u16* __restrict__ qkvA,
                                                 u16* __restrict__ qB, u16* __restrict__ kB, u16* __restrict__ vB) {
  typedef G1 C;
  const int tid = threadIdx.x, lane = tid & 63, wid = __builtin_amdgcn_readfirstlane(tid >> 6);
  const int wm = wid >> 2, wn = wid & 3;
  const int r = lane & 31, h = lane >> 5;
  const int tm = (blockIdx.x >> 3) & 7, m0 = tm * 256;
  auto decode = [&](int u) {
    const int tn = (u & 7) + 8 * (u >> 6);
    PUnit d;
    if (tn < 8) d = PUnit{Ax, Awqkv, qkvA, DM, 3 * DM, tn * 256, 3 * DM, DM / 64, 0, 0.22360679774997896f * LOG2E};
    else if (tn < 24) d = PUnit{Bx, Bwq, qB, DM, NB, (tn - 8) * 256, NB, DM / 64, 1, 0.08838834764831845f * LOG2E};
    else if (tn < 40) d = PUnit{Bckv, Bwuk, kB, DC, NB, (tn - 24) * 256, NB, DC / 64, 1, 1.f};
    else d = PUnit{Bckv, Bwuv, vB, DC, NB, (tn - 40) * 256, NB, DC / 64, 1, 1.f};
    d.x += (size_t)m0 * d.ldx;
    d.w += d.n0;
    return d;
  };
  unsigned xoff[4], woff[4];
  auto offsets = [&](const PUnit& d) {
#pragma unroll
    for (int i = 0; i < 4; ++i) {
      const int row = 8 * (wid * 4 + i) + (lane >> 3);
      xoff[i] = (unsigned)(row * d.ldx + (((lane & 7) ^ ((row >> 1) & 7)) * 8)) * 2u;
      const int k = 2 * (wid * 4 + i) + (lane >> 5);
      int col = ((lane & 31) ^ ((k & 3) << 2)) * 8;
      if (d.n0 + col > d.ncols - 8) col = d.ncols - 8 - d.n0;
      woff[i] = (unsigned)(k * d.ldw + col) * 2u;
    }
  };
  u32x4 pfd = {0u, 0u, 0u, 0u};
  auto warm = [&](const PUnit& d) {
    const int rows = d.nk * 8;
    int col = (tid & 31) * 8;
    if (d.n0 + col > d.ncols - 8) col = d.ncols - 8 - d.n0;
    const u16* pw = d.w + (size_t)(tm * rows + (tid >> 5)) * d.ldw + col;
    for (int i = 0; i < rows; i += 16)
      asm volatile("global_load_dwordx4 %0, %1, off" : "+v"(pfd) : "v"(pw + (size_t)i * d.ldw) : "memory");
  };
  unsigned xa[4], wa[2];
#pragma unroll
  for (int ks = 0; ks < 4; ++ks) xa[ks] = (wm * 128 + r) * 128 + (((2 * ks + h) ^ ((r >> 1) & 7)) * 16);
  {
    const int q = (lane & 15) >> 2, p = lane & 3, cb = (lane >> 4) & 1;
#pragma unroll
    for (int nt = 0; nt < 2; ++nt) wa[nt] = C::XB + (8 * h + q) * C::WROW + (((wn * 2 + nt) ^ q) * 64) + cb * 32 + p * 8;
  }
  const int nunit = blockIdx.x + 256 < 448 ? 2 : 1;
  PUnit cu = decode(blockIdx.x);
  offsets(cu);
#pragma unroll
  for (int i = 0; i < 4; ++i) dma16(cu.x, xoff[i], (wid * 4 + i) * 1024);
#pragma unroll
  for (int i = 0; i < 4; ++i) dma16(cu.w, woff[i], C::XB + (wid * 4 + i) * 1024);
  warm(cu);
  if (nunit == 2) warm(decode(blockIdx.x + 256));
  WAIT_VM0();
  asm volatile("" :: "v"(pfd));
  BARRIER();
  unsigned lb = 0;
#pragma unroll 1
  for (int un = 0; un < nunit; ++un) {
    f32x16 acc[2][4];
#pragma unroll
    for (int nt = 0; nt < 2; ++nt)
#pragma unroll
      for (int mt = 0; mt < 4; ++mt) acc[nt][mt] = f32x16{};
    const bool hasnext = un + 1 < nunit;
    PUnit nu = cu;
    if (hasnext) nu = decode(blockIdx.x + 256);
#pragma unroll 1
    for (int kt = 0; kt < cu.nk; ++kt) {
      const bool last = kt + 1 == cu.nk;
      const bool more = !last || hasnext;
      if (last && hasnext) offsets(nu);
      const u16* xb = last ? nu.x : cu.x + (kt + 1) * 64;
      const u16* wb = last ? nu.w : cu.w + (size_t)(kt + 1) * 64 * cu.ldw;
      const unsigned nb = C::STAGE - lb;
#pragma unroll
      for (int ks = 0; ks < 4; ++ks) {
        bf16x8 xf[4], wf[2];
#pragma unroll
        for (int mt = 0; mt < 4; ++mt) xf[mt] = lds_read128(lb + xa[ks] + mt * 4096);
#pragma unroll
        for (int nt = 0; nt < 2; ++nt)
          wf[nt] = cat8(lds_read_tr(lb + wa[nt] + (16 * ks) * C::WROW), lds_read_tr(lb + wa[nt] + (16 * ks + 4) * C::WROW));
        if (more) {
          dma16(xb, xoff[ks], nb + (wid * 4 + ks) * 1024);
          dma16(wb, woff[ks], nb + C::XB + (wid * 4 + ks) * 1024);
        }
#pragma unroll
        for (int nt = 0; nt < 2; ++nt)
#pragma unroll
          for (int mt = 0; mt < 4; ++mt) acc[nt][mt] = MFMA32(wf[nt], xf[mt], acc[nt][mt]);
      }
      WAIT_VM0();
      BARRIER();
      lb = nb;
    }
    {
      int lz = threadIdx.x;
      asm volatile("" : "+v"(lz));
      const int lane = lz & 63, r = lz & 31, h = (lz >> 5) & 1;
      const unsigned sb = (C::STAGE - lb) + wid * 8192;
      const int nb0 = cu.n0 + wn * 64;
#pragma unroll
      for (int hf = 0; hf < 2; ++hf) {
#pragma unroll
        for (int nt = 0; nt < 2; ++nt)
#pragma unroll
          for (int g = 0; g < 4; ++g) {
            const int nl = nt * 32 + 8 * g + 4 * h;
            const float sc = (cu.kind == 0 && nb0 + nl >= DM) ? 1.f : cu.scale;
            const unsigned a = sb + r * 128 + (((nl >> 3) ^ (r & 7)) * 16) + (nl & 4) * 2;
#pragma unroll
            for (int m2 = 0; m2 < 2; ++m2) {
              const f32x16& x = acc[nt][2 * hf + m2];
              u32x2 v = {cvtpk(x[4 * g] * sc, x[4 * g + 1] * sc), cvtpk(x[4 * g + 2] * sc, x[4 * g + 3] * sc)};
              *(lds_u32x2*)(uintptr_t)(a + m2 * 4096) = v;
            }
          }
        const int mb = m0 + wm * 128 + hf * 64;
        if (cu.kind == 0) { EpiQKVA e{cu.out, 1.f}; e.template store<64>(sb, mb, nb0, lane); }
        else { EpiHead e{cu.out, 1.f}; e.template store<64>(sb, mb, nb0, lane); }
      }
    }
    if (hasnext) BARRIER();
    cu = nu;
  }
}

constexpr int SPLITK = 5;
__global__ __launch_bounds__(512, 2) void k_out(const u16* __restrict__ oA, const u16* __restrict__ Awo,
                                                const u16* __restrict__ ctx, const u16* __restrict__ Bwo,
                                                u16* __restrict__ out, float* __restrict__ part) {
  const int b = blockIdx.x;
  if (b < 40 * SPLITK) {
    const int tm = b & 7, rest = b >> 3, tn = rest % 5, sp = rest / 5;
    EpiF32 e{part + ((size_t)sp * 40 + tm * 5 + tn) * (256 * 128)};
    gemm_tile<G2, EpiF32, true>(ctx, DHB, Bwo, DM, tm * 256, tn * 128, DM, sp * 13 * 64, sp == 4 ? 12 : 13, e);
  } else {
    const int t = b - 40 * SPLITK, tm = t & 7, tn = t >> 3;
    EpiPlain e{out, 2 * DM, 1.f};
    gemm_tile<G2>(oA, DM, Awo, DM, tm * 256, tn * 128, DM, 0, DM / 64, e);
  }
}

__global__ __launch_bounds__(256) void k_reduce(const float* __restrict__ part, u16* __restrict__ out) {
  const int i = blockIdx.x * 256 + threadIdx.x;
  const int tile = i >> 13, w = (i >> 10) & 7, j = (i >> 6) & 15, lane = i & 63;
  const int nt = j >> 3, mt = (j >> 2) & 1, g = j & 3;
  const int tm = tile / 5, tn = tile - tm * 5;
  const int m = tm * 256 + (w >> 1) * 64 + mt * 32 + (lane & 31);
  const int n = tn * 128 + (w & 1) * 64 + nt * 32 + 8 * g + 4 * (lane >> 5);
  const f32x4* p = (const f32x4*)part + i;
  f32x4 s = p[0];
#pragma unroll
  for (int sp = 1; sp < SPLITK; ++sp) s += p[(size_t)sp * 40 * 8192];
  u32x2 v = {cvtpk(s[0], s[1]), cvtpk(s[2], s[3])};
  *(u32x2*)(out + (size_t)m * 2 * DM + DM + n) = v;
}

constexpr float THR = 8.f;
DI bf16x8 pack8(const f32x16& x, int s) {
  u32x4 w = {cvtpk(x[8 * s], x[8 * s + 1]), cvtpk(x[8 * s + 2], x[8 * s + 3]), cvtpk(x[8 * s + 4], x[8 * s + 5]),
             cvtpk(x[8 * s + 6], x[8 * s + 7])};
  return __builtin_bit_cast(bf16x8, w);
}
DI f32x16 splat16(float v) {
  f32x16 x;
#pragma unroll
  for (int i = 0; i < 16; ++i) x[i] = v;
  return x;
}
DI float max16(const f32x16& x) {
  float a = fmaxf(fmaxf(x[0], x[1]), x[2]);
#pragma unroll
  for (int i = 3; i < 15; i += 2) a = fmaxf(fmaxf(a, x[i]), x[i + 1]);
  return fmaxf(a, x[15]);
}

#define SBAR() __builtin_amdgcn_sched_barrier(0)
constexpr float BIGSUM = 4096.f;
DI float sum16(const f32x16& x) {
  float a = (x[0] + x[1]) + (x[2] + x[3]), b = (x[4] + x[5]) + (x[6] + x[7]);
  float c = (x[8] + x[9]) + (x[10] + x[11]), d = (x[12] + x[13]) + (x[14] + x[15]);
  return (a + b) + (c + d);
}
DI void exp16(f32x16& x) {
#pragma unroll
  for (int i = 0; i < 16; ++i) x[i] = __builtin_amdgcn_exp2f(x[i]);
}
DI void attnB_item(const u16* __restrict__ Q, const u16* __restrict__ K, const u16* __restrict__ V,
                   u16* __restrict__ ctx, int head, int jitem) {
  const int tid = threadIdx.x, lane = tid & 63, wid = __builtin_amdgcn_readfirstlane(tid >> 6);
  const int r = lane & 31, h = lane >> 5;
  constexpr unsigned KBUF = 0, VBUF = 32768, TB = 16384, STG = 65536;
  unsigned koff, voff;
  {
    const int l4 = lane >> 4, c = lane & 15;
    koff = (unsigned)((16 * wid + l4) * DHB + ((c ^ l4) * 8)) * 2u;
    voff = (unsigned)((16 * wid + l4) * DHB + ((c ^ (l4 << 2)) * 8)) * 2u;
  }
  const u16* Kh = K + (size_t)head * S_ * DHB;
  const u16* Vh = V + (size_t)head * S_ * DHB;
  auto stageK = [&](int buf, int t) {
    const u16* g = Kh + (size_t)t * 64 * DHB;
    dma_base(KBUF + buf * TB + wid * 4096);
    dma_piece<0>(g, koff); dma_piece_x<1024, 64>(g, koff); dma_piece_x<2048, 128>(g, koff); dma_piece_x<3072, 192>(g, koff);
  };
  auto stageV = [&](int buf, int t) {
    const u16* g = Vh + (size_t)t * 64 * DHB;
    dma_base(VBUF + buf * TB + wid * 4096);
    dma_piece<0>(g, voff); dma_piece<1024>(g, voff); dma_piece<2048>(g, voff); dma_piece<3072>(g, voff);
  };
  unsigned ka[8], va[4];
#pragma unroll
  for (int ks = 0; ks < 8; ++ks) {
    ka[ks] = KBUF + r * 256 + (((2 * ks + h) ^ (r & 15)) * 16);
    asm volatile("" : "+v"(ka[ks]));
  }
  {
    const int q = (lane & 15) >> 2, p = lane & 3, cb = (lane >> 4) & 1;
#pragma unroll
    for (int db = 0; db < 4; ++db) va[db] = VBUF + (4 * h + q) * 256 + ((db ^ q) * 64) + cb * 32 + p * 8;
  }
  bf16x8 qf[8];
  f32x16 o[4], negm;
  float mcur, lsum;
  auto loadQ = [&](int qb) {
    int lz = threadIdx.x;
    asm volatile("" : "+v"(lz));
    const u16* qp = Q + ((size_t)head * S_ + qb * 128) * DHB + (unsigned)((wid * 32 + (lz & 31)) * DHB + ((lz >> 5) & 1) * 8);
#pragma unroll
    for (int ks = 0; ks < 8; ++ks) qf[ks] = *(const bf16x8*)(qp + ks * 16);
  };
  stageK(0, 0); stageK(1, 1); stageV(0, 0);
  loadQ(15 - jitem);

  auto qk_abs = [&](unsigned off) {
    bf16x8 kf[8];
#pragma unroll
    for (int ks = 0; ks < 8; ++ks) kf[ks] = lds_read128(off + ka[ks]);
    f32x16 s = MFMA32(kf[0], qf[0], f32x16{});
#pragma unroll
    for (int ks = 1; ks < 8; ++ks) s = MFMA32(kf[ks], qf[ks], s);
    return s;
  };
  auto remax = [&](const f32x16& st, bool first, f32x16& s) {
    const float tm = xhalf_max(max16(st));
    const float mnew = first ? tm : fmaxf(tm, mcur);
    const float delta = mnew - mcur;
    const float f = __builtin_amdgcn_exp2f(-fmaxf(delta, 0.f));
    mcur = mnew;
    negm = splat16(-mnew);
#pragma unroll
    for (int i = 0; i < 16; ++i) s[i] = __builtin_amdgcn_exp2f(st[i] - mnew);
#pragma unroll
    for (int db = 0; db < 4; ++db)
#pragma unroll
      for (int i = 0; i < 16; ++i) o[db][i] *= f;
    lsum *= f;
    return delta;
  };
  auto slow = [&](int blk) {
    int lz = threadIdx.x;
    asm volatile("" : "+v"(lz));
    const u16* kp = Kh + (size_t)blk * 32 * DHB + (unsigned)((lz & 31) * DHB + ((lz >> 5) & 1) * 8);
    f32x16 st = f32x16{};
#pragma unroll
    for (int ks = 0; ks < 8; ++ks) {
      st = MFMA32(*(const bf16x8*)(kp + ks * 16), qf[ks], st);
      asm volatile("" ::: "memory");
      SBAR();
    }
    return st;
  };
  auto pv = [&](unsigned off, const f32x16& p) {
#pragma unroll
    for (int sx = 0; sx < 2; ++sx) {
      const bf16x8 pf = pack8(p, sx);
#pragma unroll
      for (int db = 0; db < 4; ++db) {
        const bf16x8 vf = cat8(lds_read_tr(off + va[db] + (16 * sx) * 256), lds_read_tr(off + va[db] + (16 * sx + 8) * 256));
        o[db] = MFMA32(vf, pf, o[db]);
      }
    }
  };
#define EXP2(x) x = __builtin_amdgcn_exp2f(x)
#define VRD(dst, off) dst = cat8(lds_read_tr(off), lds_read_tr((off) + 2048))

#pragma unroll 1
  for (int pass = 0; pass < 2; ++pass) {
    const int qb = pass ? jitem : 15 - jitem;
    const int ntile = 2 * qb + 2;
    const int kbd = 4 * qb + wid;
    auto maskblk = [&](f32x16& s, int kb) {
      int lz = threadIdx.x;
      asm volatile("" : "+v"(lz));
      const int dq = (kbd - kb) * 32 + (lz & 31) - 4 * ((lz >> 5) & 1);
#pragma unroll
      for (int i = 0; i < 16; ++i)
        if ((i & 3) + 8 * (i >> 2) > dq) s[i] = -__builtin_inff();
    };
    {
      float z = 0.f;
      asm volatile("" : "+v"(z));
#pragma unroll
      for (int db = 0; db < 4; ++db) o[db] = splat16(z);
      lsum = z;
    }
#pragma unroll
    for (int ks = 0; ks < 8; ++ks) asm volatile("" : "+v"(qf[ks]));
    WAIT_VM0();
    BARRIER();
    {
      f32x16 sa, sb;
      bf16x8 kg[8];
      float pa;
      {
        f32x16 st = qk_abs(0);
        if (kbd == 0) maskblk(st, 0);
        const float m = xhalf_max(max16(st));
        mcur = m;
        negm = splat16(-m);
#pragma unroll
        for (int i = 0; i < 8; ++i) sa[i] = __builtin_amdgcn_exp2f(st[i] - m);
#pragma unroll
        for (int i = 8; i < 16; ++i) sa[i] = st[i] - m;
        pa = ((sa[0] + sa[1]) + (sa[2] + sa[3])) + ((sa[4] + sa[5]) + (sa[6] + sa[7]));
#pragma unroll
        for (int ks = 0; ks < 8; ++ks) kg[ks] = lds_read128(ka[ks] + 8192);
      }
      auto tile = [&](auto BUFC, auto LASTC, int t) {
        constexpr unsigned B_ = decltype(BUFC)::value;
        constexpr bool LAST = decltype(LASTC)::value;
        constexpr unsigned vo = B_ * TB, vn = TB - vo;
        constexpr unsigned kn = TB - vo, kd = vo;
        const u16* kbn = Kh + (size_t)min(t + 2, 31) * 64 * DHB;
        const u16* vbn = Vh + (size_t)min(t + 1, 31) * 64 * DHB;
#define DMAK(i) do { if ((i) == 0) { dma_base(KBUF + kd + wid * 4096); dma_piece<0>(kbn, koff); } else dma_piece_x<(i) * 1024, (i) * 64>(kbn, koff); } while (0)
#define DMAV(i) do { if ((i) == 0) dma_base(VBUF + vn + wid * 4096); dma_piece<(i) * 1024>(vbn, voff); } while (0)
        bf16x8 kf[8], vf[8], vg[8];
#define KF(i) kf[i] = lds_read128(kn + ka[i])
#define KG(i) kg[i] = lds_read128(kn + ka[i] + 8192)
        sb = MFMA32(kg[0], qf[0], negm);
        VRD(vf[0], vo + va[0]); DMAV(0);
        EXP2(sa[8]); EXP2(sa[9]);
        SBAR();
        sb = MFMA32(kg[1], qf[1], sb);
        VRD(vf[1], vo + va[1]); DMAV(1);
        EXP2(sa[10]); EXP2(sa[11]);
        SBAR();
        sb = MFMA32(kg[2], qf[2], sb);
        VRD(vf[2], vo + va[2]); DMAV(2);
        EXP2(sa[12]); EXP2(sa[13]);
        float pa2 = (sa[8] + sa[9]) + (sa[10] + sa[11]);
        SBAR();
        sb = MFMA32(kg[3], qf[3], sb);
        VRD(vf[3], vo + va[3]); DMAV(3);
        EXP2(sa[14]); EXP2(sa[15]);
        pa += sa[12] + sa[13];
        SBAR();
        sb = MFMA32(kg[4], qf[4], sb);
        DMAK(0);
        pa2 += sa[14] + sa[15];
        pa += pa2;
        if (__any(!(pa <= BIGSUM))) {
          f32x16 st = slow(2 * t);
          if (2 * t >= kbd) maskblk(st, 2 * t);
          const float delta = remax(st, false, sa);
#pragma unroll
          for (int i = 0; i < 16; ++i) sb[i] -= delta;
          pa = sum16(sa);
        }
        sb = MFMA32(kg[5], qf[5], sb);
        VRD(vf[4], vo + va[0] + 4096); VRD(vf[5], vo + va[1] + 4096);
        const bf16x8 p0 = pack8(sa, 0);
        lsum += pa;
        SBAR();
        sb = MFMA32(kg[6], qf[6], sb);
        VRD(vf[6], vo + va[2] + 4096); DMAK(1);
        const bf16x8 p1 = pack8(sa, 1);
        SBAR();
        sb = MFMA32(kg[7], qf[7], sb);
        VRD(vf[7], vo + va[3] + 4096); DMAK(2);
        SBAR();
        o[0] = MFMA32(vf[0], p0, o[0]);
        KF(0); DMAK(3);
        if (2 * t + 1 >= kbd) maskblk(sb, 2 * t + 1);
        SBAR();
        o[1] = MFMA32(vf[1], p0, o[1]);
        KF(1);
        EXP2(sb[0]); EXP2(sb[1]); EXP2(sb[2]);
        SBAR();
        o[2] = MFMA32(vf[2], p0, o[2]);
        KF(2);
        EXP2(sb[3]); EXP2(sb[4]);
        SBAR();
        o[3] = MFMA32(vf[3], p0, o[3]);
        KF(3);
        EXP2(sb[5]); EXP2(sb[6]);
        float pb = (sb[0] + sb[1]) + (sb[2] + sb[3]);
        SBAR();
        o[0] = MFMA32(vf[4], p1, o[0]);
        KF(4);
        EXP2(sb[7]); EXP2(sb[8]);
        SBAR();
        o[1] = MFMA32(vf[5], p1, o[1]);
        KF(5);
        EXP2(sb[9]); EXP2(sb[10]);
        float pb2 = (sb[4] + sb[5]) + (sb[6] + sb[7]);
        SBAR();
        o[2] = MFMA32(vf[6], p1, o[2]);
        KF(6);
        EXP2(sb[11]); EXP2(sb[12]);
        SBAR();
        o[3] = MFMA32(vf[7], p1, o[3]);
        KF(7);
        EXP2(sb[13]); EXP2(sb[14]);
        pb += (sb[8] + sb[9]) + sb[10];
        SBAR();
        sa = MFMA32(kf[0], qf[0], negm);
        VRD(vg[0], vo + va[0] + 8192);
        EXP2(sb[15]);
        pb2 += (sb[11] + sb[12]) + sb[13];
        SBAR();
        sa = MFMA32(kf[1], qf[1], sa);
        VRD(vg[1], vo + va[1] + 8192);
        pb += sb[14] + sb[15];
        pb += pb2;
        if (__any(!(pb <= BIGSUM))) {
          f32x16 st = slow(2 * t + 1);
          if (2 * t + 1 >= kbd) maskblk(st, 2 * t + 1);
          const float delta = remax(st, false, sb);
#pragma unroll
          for (int i = 0; i < 16; ++i) sa[i] -= delta;
          pb = sum16(sb);
        }
        sa = MFMA32(kf[2], qf[2], sa);
        VRD(vg[2], vo + va[2] + 8192);
        const bf16x8 r0 = pack8(sb, 0);
        lsum += pb;
        SBAR();
        sa = MFMA32(kf[3], qf[3], sa);
        VRD(vg[3], vo + va[3] + 8192);
        const bf16x8 r1 = pack8(sb, 1);
        SBAR();
        sa = MFMA32(kf[4], qf[4], sa);
        VRD(vg[4], vo + va[0] + 12288);
        SBAR();
        sa = MFMA32(kf[5], qf[5], sa);
        VRD(vg[5], vo + va[1] + 12288);
        SBAR();
        sa = MFMA32(kf[6], qf[6], sa);
        VRD(vg[6], vo + va[2] + 12288);
        SBAR();
        sa = MFMA32(kf[7], qf[7], sa);
        VRD(vg[7], vo + va[3] + 12288);
        SBAR();
        o[0] = MFMA32(vg[0], r0, o[0]);
        KG(0);
        if (2 * t + 2 >= kbd) maskblk(sa, 2 * t + 2);
        SBAR();
        o[1] = MFMA32(vg[1], r0, o[1]);
        KG(1); EXP2(sa[0]); EXP2(sa[1]);
        SBAR();
        o[2] = MFMA32(vg[2], r0, o[2]);
        KG(2); EXP2(sa[2]); EXP2(sa[3]);
        SBAR();
        o[3] = MFMA32(vg[3], r0, o[3]);
        KG(3); EXP2(sa[4]); EXP2(sa[5]);
        SBAR();
        o[0] = MFMA32(vg[4], r1, o[0]);
        KG(4); EXP2(sa[6]); EXP2(sa[7]);
        pa = (sa[0] + sa[1]) + (sa[2] + sa[3]);
        SBAR();
        o[1] = MFMA32(vg[5], r1, o[1]);
        KG(5);
        pa += (sa[4] + sa[5]) + (sa[6] + sa[7]);
        SBAR();
        o[2] = MFMA32(vg[6], r1, o[2]);
        KG(6); KG(7);
        SBAR();
        o[3] = MFMA32(vg[7], r1, o[3]);
#undef KF
#undef KG
        WAIT_VM0();
        BARRIER();
#undef DMAK
#undef DMAV
      };
      typedef std::integral_constant<unsigned, 0> C0;
      typedef std::integral_constant<unsigned, 1> C1;
      int t = 0;
      do {
        tile(C0{}, std::false_type{}, t);
        tile(C1{}, std::false_type{}, t + 1);
        t += 2;
      } while (t < ntile);
    }
    if (pass == 0) { stageK(0, 0); stageK(1, 1); stageV(0, 0); }
    const float inv = 1.f / xhalf_sum(lsum);
    {
      int lz = threadIdx.x;
      asm volatile("" : "+v"(lz));
      const int lane = lz & 63, r = lz & 31, h = (lz >> 5) & 1;
      const unsigned sb_ = STG + wid * 4096;
      u16* op = ctx + ((size_t)head * S_ + qb * 128 + wid * 32 + (lane >> 3)) * DHB + (lane & 7) * 8;
#pragma unroll
      for (int hf = 0; hf < 2; ++hf) {
#pragma unroll
        for (int d2 = 0; d2 < 2; ++d2)
#pragma unroll
          for (int g = 0; g < 4; ++g) {
            const f32x16& x = o[2 * hf + d2];
            u32x2 v = {cvtpk(x[4 * g] * inv, x[4 * g + 1] * inv), cvtpk(x[4 * g + 2] * inv, x[4 * g + 3] * inv)};
            *(lds_u32x2*)(uintptr_t)(sb_ + r * 128 + (((4 * d2 + g) ^ (r & 7)) * 16) + h * 8) = v;
          }
#pragma unroll
        for (int it = 0; it < 4; ++it) {
          const int rr = it * 8 + (lane >> 3);
          const bf16x8 v = lds_read128(sb_ + rr * 128 + (((lane & 7) ^ (rr & 7)) * 16));
          *(bf16x8*)(op + (size_t)it * 8 * DHB + hf * 64) = v;
        }
      }
    }
    if (pass == 0) loadQ(jitem);
  }
#undef EXP2
#undef VRD
}

DI void attnA_block(const u16* __restrict__ qkv, u16* __restrict__ oA, int head, int qb) {
  const int tid = threadIdx.x, lane = tid & 63, wid = __builtin_amdgcn_readfirstlane(tid >> 6);
  const int r = lane & 31, h = lane >> 5;
  const int q0 = qb * 256 + wid * 64;
  constexpr unsigned KBUF = 0, VBUF = 16384, TB = 8192;
  const u16* Qh = qkv + (size_t)(0 * HA + head) * S_ * 32;
  const u16* Kh = qkv + (size_t)(1 * HA + head) * S_ * 32;
  const u16* Vh = qkv + (size_t)(2 * HA + head) * S_ * 32;
  unsigned koff, voff;
  {
    const int row = 32 * wid + (lane >> 2), c = lane & 3;
    koff = (unsigned)(row * 64 + ((c ^ ((row >> 2) & 3)) * 16));
    voff = (unsigned)(row * 64 + c * 16);
  }
  auto stage = [&](int buf, int t) {
    dma_base(KBUF + buf * TB + wid * 2048);
    dma_piece<0>(Kh + (size_t)t * 128 * 32, koff); dma_piece<1024>(Kh + (size_t)t * 128 * 32, koff);
    dma_base(VBUF + buf * TB + wid * 2048);
    dma_piece<0>(Vh + (size_t)t * 128 * 32, voff); dma_piece<1024>(Vh + (size_t)t * 128 * 32, voff);
  };
  stage(0, 0);
  bf16x8 qf[2][2];
#pragma unroll
  for (int qt = 0; qt < 2; ++qt)
#pragma unroll
    for (int ks = 0; ks < 2; ++ks) qf[qt][ks] = *(const bf16x8*)(Qh + (size_t)(q0 + 32 * qt + r) * 32 + ks * 16 + h * 8);
  unsigned ka[2], va;
#pragma unroll
  for (int ks = 0; ks < 2; ++ks) {
    ka[ks] = KBUF + r * 64 + (((2 * ks + h) ^ ((r >> 2) & 3)) * 16);
    asm volatile("" : "+v"(ka[ks]));
  }
  {
    const int q = (lane & 15) >> 2, p = lane & 3, cb = (lane >> 4) & 1;
    va = VBUF + (4 * h + q) * 64 + cb * 32 + p * 8;
  }
  f32x16 o[2], negm[2];
  float mcur[2];
#pragma unroll
  for (int qt = 0; qt < 2; ++qt) { o[qt] = f32x16{}; negm[qt] = f32x16{}; mcur[qt] = 0.f; }
#pragma unroll
  for (int qt = 0; qt < 2; ++qt) { asm volatile("" : "+v"(qf[qt][0])); asm volatile("" : "+v"(qf[qt][1])); }
  WAIT_VM0();
  BARRIER();
  constexpr int NKT = S_ / 128;
#define EXP2(x) x = __builtin_amdgcn_exp2f(x)
#define EXP4(s, i) EXP2(s[i]); EXP2(s[i + 1]); EXP2(s[i + 2]); EXP2(s[i + 3])
#define VRDA(dst, off) dst = cat8(lds_read_tr(off), lds_read_tr((off) + 512))
  auto slow_tile = [&](bool first) {
#pragma unroll 1
    for (int kb = 0; kb < 4; ++kb) {
      const bf16x8 k0 = lds_read128(ka[0] + kb * 2048), k1 = lds_read128(ka[1] + kb * 2048);
      bf16x8 v0, v1;
      VRDA(v0, va + kb * 2048); VRDA(v1, va + kb * 2048 + 1024);
#pragma unroll
      for (int qt = 0; qt < 2; ++qt) {
        f32x16 st = MFMA32(k0, qf[qt][0], f32x16{});
        st = MFMA32(k1, qf[qt][1], st);
        const float tm = xhalf_max(max16(st));
        const float mnew = (first && kb == 0) ? tm : fmaxf(tm, mcur[qt]);
        const float f = __builtin_amdgcn_exp2f(-fmaxf(mnew - mcur[qt], 0.f));
        mcur[qt] = mnew;
#pragma unroll
        for (int i = 0; i < 16; ++i) { st[i] = __builtin_amdgcn_exp2f(st[i] - mnew); o[qt][i] *= f; }
        o[qt] = MFMA32(v0, pack8(st, 0), o[qt]);
        o[qt] = MFMA32(v1, pack8(st, 1), o[qt]);
      }
    }
    negm[0] = splat16(-mcur[0]);
    negm[1] = splat16(-mcur[1]);
  };
  for (int t = 0; t < NKT; ++t) {
    if (t + 1 < NKT) stage((t + 1) & 1, t + 1);
    const f32x16 os0 = o[0], os1 = o[1];
    f32x16 sa0, sa1, sb0, sb1;
    bf16x8 k0, k1, v0, v1;
    k0 = lds_read128(ka[0]); k1 = lds_read128(ka[1]);
    sa0 = MFMA32(k0, qf[0][0], negm[0]); sa1 = MFMA32(k0, qf[1][0], negm[1]);
    sa0 = MFMA32(k1, qf[0][1], sa0); sa1 = MFMA32(k1, qf[1][1], sa1);
    k0 = lds_read128(ka[0] + 2048); k1 = lds_read128(ka[1] + 2048);
#define UNIT(C0, C1, N0, N1, kb)                                                                   \
    do {                                                                                             \
      VRDA(v0, va + (kb) * 2048); VRDA(v1, va + (kb) * 2048 + 1024);                                 \
      SBAR();                                                                                        \
      if ((kb) < 3) N0 = MFMA32(k0, qf[0][0], negm[0]);                                              \
      EXP4(C0, 0);                                                                                   \
      SBAR();                                                                                        \
      if ((kb) < 3) N1 = MFMA32(k0, qf[1][0], negm[1]);                                              \
      EXP4(C0, 4);                                                                                   \
      SBAR();                                                                                        \
      if ((kb) < 3) N0 = MFMA32(k1, qf[0][1], N0);                                                   \
      EXP4(C0, 8);                                                                                   \
      const bf16x8 p00 = pack8(C0, 0);                                                               \
      SBAR();                                                                                        \
      if ((kb) < 3) N1 = MFMA32(k1, qf[1][1], N1);                                                   \
      EXP4(C0, 12);                                                                                  \
      if ((kb) < 2) { k0 = lds_read128(ka[0] + ((kb) + 2) * 2048); k1 = lds_read128(ka[1] + ((kb) + 2) * 2048); } \
      SBAR();                                                                                        \
      o[0] = MFMA32(v0, p00, o[0]);                                                                  \
      EXP4(C1, 0);                                                                                   \
      const bf16x8 p01 = pack8(C0, 1);                                                               \
      SBAR();                                                                                        \
      o[0] = MFMA32(v1, p01, o[0]);                                                                  \
      EXP4(C1, 4); EXP4(C1, 8);                                                                      \
      const bf16x8 p10 = pack8(C1, 0);                                                               \
      SBAR();                                                                                        \
      o[1] = MFMA32(v0, p10, o[1]);                                                                  \
      EXP4(C1, 12);                                                                                  \
      const bf16x8 p11 = pack8(C1, 1);                                                               \
      SBAR();                                                                                        \
      o[1] = MFMA32(v1, p11, o[1]);                                                                  \
    } while (0)
    UNIT(sa0, sa1, sb0, sb1, 0);
    UNIT(sb0, sb1, sa0, sa1, 1);
    UNIT(sa0, sa1, sb0, sb1, 2);
    UNIT(sb0, sb1, sa0, sa1, 3);
#undef UNIT
    const float lim = t == 0 ? -1.f : 1048576.f;
    const bool bad = h && !((o[0][8] - os0[8] <= lim) && (o[1][8] - os1[8] <= lim));
    if (__any(bad)) {
      o[0] = os0; o[1] = os1;
      slow_tile(t == 0);
    }
    ka[0] ^= TB; ka[1] ^= TB; va ^= TB;
    WAIT_VM0();
    BARRIER();
  }
#undef EXP2
#undef EXP4
#undef VRDA
#pragma unroll
  for (int qt = 0; qt < 2; ++qt) {
    const float l = __shfl(o[qt][8], r + 32);
    const float inv = 1.f / l;
    u16* op = oA + (size_t)(q0 + 32 * qt + r) * DM + head * DHA + 4 * h;
#pragma unroll
    for (int g = 0; g < 3; ++g) {
      if (g < 2 || h == 0) {
        u32x2 v = {cvtpk(o[qt][4 * g] * inv, o[qt][4 * g + 1] * inv), cvtpk(o[qt][4 * g + 2] * inv, o[qt][4 * g + 3] * inv)};
        *(u32x2*)(op + 8 * g) = v;
      }
    }
  }
}

constexpr int AQT = 2;
constexpr int NBLK_B = HB * 8, NBLK_A = HA * (S_ / (128 * AQT));
__global__ __launch_bounds__(256, 2) void k_attn(const u16* __restrict__ qB, const u16* __restrict__ kB,
                                                 const u16* __restrict__ vB, u16* __restrict__ ctx,
                                                 const u16* __restrict__ qkvA, u16* __restrict__ oA) {
  const int b = blockIdx.x;
  if (b < NBLK_B) {
    const int head = (b & 7) + 8 * (b >> 6), j = (b >> 3) & 7;
    attnB_item(qB, kB, vB, ctx, head, j);
  } else {
    const int a = b - NBLK_B;
    constexpr int PER = S_ / (128 * AQT);
    const int head = (a & 7) + 8 * (a / (8 * PER)), qb = (a >> 3) % PER;
    attnA_block(qkvA, oA, head, qb);
  }
}

extern "C" void kernel_launch(void* const* d_in, const int* in_sizes, int n_in, void* d_out, int out_size, void* d_ws,
                              size_t ws_size, hipStream_t stream) {
  const u16* Ax = (const u16*)d_in[0];
  const u16* Awqkv = (const u16*)d_in[1];
  const u16* Awo = (const u16*)d_in[2];
  const u16* Bx = (const u16*)d_in[3];
  const u16* Bckv = (const u16*)d_in[4];
  const u16* Bwq = (const u16*)d_in[5];
  const u16* Bwuk = (const u16*)d_in[6];
  const u16* Bwuv = (const u16*)d_in[7];
  const u16* Bwo = (const u16*)d_in[8];
  u16* out = (u16*)d_out;
  char* ws = (char*)d_ws;
  u16* qkvA = (u16*)ws;                 ws += (size_t)3 * HA * S_ * 32 * 2;
  u16* qB = (u16*)ws;                   ws += (size_t)S_ * NB * 2;
  u16* kB = (u16*)ws;                   ws += (size_t)S_ * NB * 2;
  u16* vB = (u16*)ws;                   ws += (size_t)S_ * NB * 2;
  u16* ctx = (u16*)ws;                  ws += (size_t)S_ * NB * 2;
  u16* oA = (u16*)ws;                   ws += (size_t)S_ * DM * 2;
  float* part = (float*)ws;             ws += (size_t)SPLITK * S_ * DM * 4;

  (void)hipFuncSetAttribute((const void*)k_proj, hipFuncAttributeMaxDynamicSharedMemorySize, G1::LDS);
  (void)hipFuncSetAttribute((const void*)k_out, hipFuncAttributeMaxDynamicSharedMemorySize, G2::LDS);
  (void)hipFuncSetAttribute((const void*)k_attn, hipFuncAttributeMaxDynamicSharedMemorySize, 81920);
  k_proj<<<256, 512, G1::LDS, stream>>>(Ax, Awqkv, Bx, Bckv, Bwq, Bwuk, Bwuv, qkvA, qB, kB, vB);
  k_attn<<<NBLK_B + NBLK_A, 256, 81920, stream>>>(qB, kB, vB, ctx, qkvA, oA);
  k_out<<<40 * SPLITK + 40, 512, G2::LDS, stream>>>(oA, Awo, ctx, Bwo, out, part);
  k_reduce<<<S_ * DM / 4 / 256, 256, 0, stream>>>(part, out);
}
```

```cpp
#include <hip/hip_runtime.h>
#include <hip/hip_bf16.h>
#include <stdint.h>
#include <type_traits>

constexpr int S_ = 2048;
constexpr int DM = 640;
constexpr int HA = 32, DHA = 20;
constexpr int HB = 32, DHB = 128, DC = 512;
constexpr int NB = HB * DHB;

typedef unsigned short u16;
typedef short bf16x8 __attribute__((ext_vector_type(8)));
typedef short s16x4 __attribute__((ext_vector_type(4)));
typedef float f32x16 __attribute__((ext_vector_type(16)));
typedef float f32x4 __attribute__((ext_vector_type(4)));
typedef float f32x2 __attribute__((ext_vector_type(2)));
typedef __bf16 bf16x2 __attribute__((ext_vector_type(2)));
typedef unsigned u32x2 __attribute__((ext_vector_type(2)));
typedef unsigned u32x4 __attribute__((ext_vector_type(4)));
typedef __attribute__((address_space(3))) s16x4 lds_s16x4;
typedef __attribute__((address_space(3))) bf16x8 lds_bf16x8;
typedef __attribute__((address_space(3))) u32x2 lds_u32x2;

#define DI __device__ __forceinline__
#define MFMA32(a, b, c) __builtin_amdgcn_mfma_f32_32x32x16_bf16((a), (b), (c), 0, 0, 0)

DI unsigned cvtpk(float lo, float hi) {
  f32x2 v = {lo, hi};
  bf16x2 b = __builtin_convertvector(v, bf16x2);
  return __builtin_bit_cast(unsigned, b);
}
DI void dma16(const void* sbase, unsigned voff, unsigned lds_addr) {
  unsigned keep;
  asm volatile("s_mov_b32 %0, m0\n\ts_mov_b32 m0, %3\n\ts_nop 0\n\tglobal_load_lds_dwordx4 %1, %2\n\ts_mov_b32 m0, %0"
               : "=&s"(keep) : "v"(voff), "s"(sbase), "s"(lds_addr) : "memory");
}
DI void dma_base(unsigned lds_addr) { asm volatile("s_mov_b32 m0, %0\n\ts_nop 0" :: "s"(lds_addr) : "memory"); }
template <int OFF> DI void dma_piece(const void* sbase, unsigned voff) {
  asm volatile("global_load_lds_dwordx4 %0, %1 offset:%2" :: "v"(voff), "s"(sbase), "i"(OFF) : "memory");
}
template <int OFF, int X> DI void dma_piece_x(const void* sbase, unsigned voff) {
  unsigned t;
  asm volatile("v_xor_b32 %0, %4, %1\n\tglobal_load_lds_dwordx4 %0, %2 offset:%3" : "=&v"(t) : "v"(voff), "s"(sbase), "i"(OFF), "i"(X) : "memory");
}
#define WAIT_VM0() asm volatile("s_waitcnt vmcnt(0)" ::: "memory")
#define BARRIER() do { asm volatile("s_waitcnt lgkmcnt(0)" ::: "memory"); __builtin_amdgcn_s_barrier(); asm volatile("" ::: "memory"); } while (0)

DI bf16x8 lds_read128(unsigned off) { return *(const lds_bf16x8*)(uintptr_t)off; }
DI s16x4 lds_read_tr(unsigned off) { return __builtin_amdgcn_ds_read_tr16_b64_v4i16((lds_s16x4*)(uintptr_t)off); }
DI bf16x8 cat8(s16x4 a, s16x4 b) { return __builtin_shufflevector(a, b, 0, 1, 2, 3, 4, 5, 6, 7); }
DI float xhalf_max(float x) {
  auto r = __builtin_amdgcn_permlane32_swap(__float_as_uint(x), __float_as_uint(x), false, false);
  return fmaxf(__uint_as_float(r[0]), __uint_as_float(r[1]));
}
DI float xhalf_sum(float x) {
  auto r = __builtin_amdgcn_permlane32_swap(__float_as_uint(x), __float_as_uint(x), false, false);
  return __uint_as_float(r[0]) + __uint_as_float(r[1]);
}

template <int BM, int BN, int WM, int WN, int NST_>
struct GCfg {
  static constexpr int NST = NST_;
  static constexpr int NW = WM * WN, TM = BM / WM, TN = BN / WN, MT = TM / 32, NT = TN / 32;
  static constexpr int XB = BM * 128, WROW = BN * 2, WB = 64 * WROW, STAGE = XB + WB;
  static constexpr int XI = BM / 8 / NW;
  static constexpr int WI = WB / 1024 / NW;
  static constexpr int WCH = BN / 8;
  static constexpr int WRPI = 64 / WCH;
  static constexpr int LDS = NST * STAGE;
};

template <class C, class Epi, bool XHEAD = false>
DI void gemm_tile(const u16* __restrict__ X, int ldx, const u16* __restrict__ W, int ldw, int m0, int n0, int ncols,
                  int kbeg, int nk, const Epi& epi) {
  const int tid = threadIdx.x, lane = tid & 63, wid = __builtin_amdgcn_readfirstlane(tid >> 6);
  const int wm = wid / C::WN_, wn = wid % C::WN_;
  const int r = lane & 31, h = lane >> 5;
  unsigned xoff[C::XI], woff[C::WI];
#pragma unroll
  for (int i = 0; i < C::XI; ++i) {
    const int row = 8 * (wid * C::XI + i) + (lane >> 3);
    xoff[i] = (unsigned)(row * ldx + (((lane & 7) ^ ((row >> 1) & 7)) * 8)) * 2u;
  }
#pragma unroll
  for (int i = 0; i < C::WI; ++i) {
    const int k = C::WRPI * (wid * C::WI + i) + lane / C::WCH;
    int col = (((lane % C::WCH) ^ ((k & 3) << 2)) * 8);
    if (n0 + col > ncols - 8) col = ncols - 8 - n0;
    woff[i] = (unsigned)(k * ldw + col) * 2u;
  }
  const u16* xs = XHEAD ? X + (size_t)m0 * ldx : X + (size_t)m0 * ldx + kbeg;
  const int kt0 = kbeg >> 6;
  auto xk = [&](int kt) { return XHEAD ? xs + (size_t)((kt + kt0) >> 1) * S_ * DHB + ((kt + kt0) & 1) * 64 : xs + kt * 64; };
  const u16* ws = W + (size_t)kbeg * ldw + n0;
  auto stage = [&](int buf, int kt) {
    const u16* xb = xk(kt);
    const u16* wb = ws + (size_t)kt * 64 * ldw;
    const unsigned lb = buf * C::STAGE;
#pragma unroll
    for (int i = 0; i < C::XI; ++i) dma16(xb, xoff[i], lb + (wid * C::XI + i) * 1024);
#pragma unroll
    for (int i = 0; i < C::WI; ++i) dma16(wb, woff[i], lb + C::XB + (wid * C::WI + i) * 1024);
  };
  unsigned xa[4];
#pragma unroll
  for (int ks = 0; ks < 4; ++ks) xa[ks] = (wm * C::TM + r) * 128 + (((2 * ks + h) ^ ((r >> 1) & 7)) * 16);
  unsigned wa[C::NT];
  {
    const int q = (lane & 15) >> 2, p = lane & 3, cb = (lane >> 4) & 1;
#pragma unroll
    for (int nt = 0; nt < C::NT; ++nt) {
      const int nb = wn * C::NT + nt;
      wa[nt] = C::XB + (8 * h + q) * C::WROW + ((nb ^ q) * 64) + cb * 32 + p * 8;
    }
  }
  f32x16 acc[C::NT][C::MT];
#pragma unroll
  for (int nt = 0; nt < C::NT; ++nt)
#pragma unroll
    for (int mt = 0; mt < C::MT; ++mt) acc[nt][mt] = f32x16{};

  constexpr int AHEAD = C::NST - 1, PER = C::XI + C::WI;
  stage(0, 0);
  u32x4 pfd = {0u, 0u, 0u, 0u};
  if constexpr (Epi::PREFW) {
    const int rows = nk * 8, r0 = ((m0 >> 8) & 7) * rows;
    int col = (tid & 31) * 8;
    if (n0 + col > ncols - 8) col = ncols - 8 - n0;
    const u16* pw = ws + (size_t)(r0 + (tid >> 5)) * ldw + col;
    for (int i = 0; i < rows; i += 16)
      asm volatile("global_load_dwordx4 %0, %1, off" : "+v"(pfd) : "v"(pw + (size_t)i * ldw) : "memory");
  }
  if constexpr (AHEAD == 2) { stage(1, 1); asm volatile("s_waitcnt vmcnt(%0)" :: "i"(PER) : "memory"); }
  else WAIT_VM0();
  asm volatile("" :: "v"(pfd));
  BARRIER();
  int cur = 0, nxt = AHEAD;
  for (int kt = 0; kt < nk; ++kt) {
    const unsigned lb = cur * C::STAGE;
    const bool more = kt + AHEAD < nk;
    const u16* xb = xk(kt + AHEAD);
    const u16* wb = ws + (size_t)(kt + AHEAD) * 64 * ldw;
    const unsigned nb = nxt * C::STAGE;
    cur = cur + 1 == C::NST ? 0 : cur + 1;
    nxt = nxt + 1 == C::NST ? 0 : nxt + 1;
#pragma unroll
    for (int ks = 0; ks < 4; ++ks) {
      bf16x8 xf[C::MT], wf[C::NT];
#pragma unroll
      for (int mt = 0; mt < C::MT; ++mt) xf[mt] = lds_read128(lb + xa[ks] + mt * 4096);
#pragma unroll
      for (int nt = 0; nt < C::NT; ++nt)
        wf[nt] = cat8(lds_read_tr(lb + wa[nt] + (16 * ks) * C::WROW), lds_read_tr(lb + wa[nt] + (16 * ks + 4) * C::WROW));
      if (more) {
        constexpr int TOT = C::XI + C::WI;
#pragma unroll
        for (int i = ks * TOT / 4; i < (ks + 1) * TOT / 4; ++i) {
          if (i < C::XI) dma16(xb, xoff[i], nb + (wid * C::XI + i) * 1024);
          else dma16(wb, woff[i - C::XI], nb + C::XB + (wid * C::WI + i - C::XI) * 1024);
        }
      }
#pragma unroll
      for (int nt = 0; nt < C::NT; ++nt)
#pragma unroll
        for (int mt = 0; mt < C::MT; ++mt) acc[nt][mt] = MFMA32(wf[nt], xf[mt], acc[nt][mt]);
    }
    if (AHEAD == 2 && more) asm volatile("s_waitcnt vmcnt(%0)" :: "i"(PER) : "memory");
    else WAIT_VM0();
    BARRIER();
  }
  if constexpr (Epi::DIRECT) {
    f32x4* dst = (f32x4*)epi.out + (size_t)wid * (C::NT * C::MT * 4 * 64) + lane;
#pragma unroll
    for (int nt = 0; nt < C::NT; ++nt)
#pragma unroll
      for (int mt = 0; mt < C::MT; ++mt)
#pragma unroll
        for (int g = 0; g < 4; ++g) {
          f32x4 v = {acc[nt][mt][4 * g], acc[nt][mt][4 * g + 1], acc[nt][mt][4 * g + 2], acc[nt][mt][4 * g + 3]};
          dst[((nt * C::MT + mt) * 4 + g) * 64] = v;
        }
  } else {
    static_assert(C::TN == 64, "staging layout");
    const unsigned sb = wid * (C::TM * 128);
#pragma unroll
    for (int nt = 0; nt < C::NT; ++nt)
#pragma unroll
      for (int g = 0; g < 4; ++g) {
        const int nl = nt * 32 + 8 * g + 4 * h;
        const float sc = epi.scale_of(n0 + wn * C::TN + nl);
        const unsigned a = sb + r * 128 + (((nl >> 3) ^ (r & 7)) * 16) + (nl & 4) * 2;
#pragma unroll
        for (int mt = 0; mt < C::MT; ++mt) {
          u32x2 v = {cvtpk(acc[nt][mt][4 * g] * sc, acc[nt][mt][4 * g + 1] * sc),
                     cvtpk(acc[nt][mt][4 * g + 2] * sc, acc[nt][mt][4 * g + 3] * sc)};
          *(lds_u32x2*)(uintptr_t)(a + mt * 4096) = v;
        }
      }
    epi.template store<C::TM>(sb, m0 + wm * C::TM, n0 + wn * C::TN, lane);
  }
}

template <int BM, int BN, int WM, int WN, int NST>
struct GC : GCfg<BM, BN, WM, WN, NST> { static constexpr int WN_ = WN; };

struct EpiPlain {
  static constexpr bool DIRECT = false, PREFW = false;
  u16* out; int ld; float scale;
  DI float scale_of(int) const { return scale; }
  template <int TM> DI void store(unsigned sb, int mb, int nb, int lane) const {
    const int rr = lane >> 3, c = lane & 7;
    u16* dst = out + (size_t)(mb + rr) * ld + nb + c * 8;
    const unsigned a = sb + rr * 128 + ((c ^ (rr & 7)) * 16);
#pragma unroll
    for (int it = 0; it < TM / 8; ++it) {
      const bf16x8 v = lds_read128(a + it * 1024);
      *(bf16x8*)(dst + (size_t)it * 8 * ld) = v;
    }
  }
};
struct EpiHead {
  static constexpr bool DIRECT = false, PREFW = true;
  u16* out; float scale;
  DI float scale_of(int) const { return scale; }
  template <int TM> DI void store(unsigned sb, int mb, int nb, int lane) const {
    const int rr = lane >> 3, c = lane & 7;
    u16* dst = out + ((size_t)(nb >> 7) * S_ + mb + rr) * DHB + (nb & 127) + c * 8;
    const unsigned a = sb + rr * 128 + ((c ^ (rr & 7)) * 16);
#pragma unroll
    for (int it = 0; it < TM / 8; ++it) {
      const bf16x8 v = lds_read128(a + it * 1024);
      *(bf16x8*)(dst + (size_t)it * 8 * DHB) = v;
    }
  }
};
struct EpiQKVA {
  static constexpr bool DIRECT = false, PREFW = true;
  u16* out; float qscale;
  DI float scale_of(int n) const { return n < DM ? qscale : 1.f; }
  template <int TM> DI void store(unsigned sb, int mb, int nb, int lane) const {
    const int rr = lane >> 4, gq = lane & 15;
    const int n = nb + gq * 4;
    if (n >= 3 * DM) return;
    const int which = n / DM, rem = n - which * DM, head = rem / DHA, dd = rem - head * DHA;
    u16* dst = out + ((size_t)((which * HA + head) * S_ + mb + rr)) * 32;
#pragma unroll 4
    for (int it = 0; it < TM / 4; ++it) {
      const int row = it * 4 + rr;
      const u32x2 v = *(const lds_u32x2*)(uintptr_t)(sb + row * 128 + (((gq >> 1) ^ (row & 7)) * 16) + (gq & 1) * 8);
      u16* d = dst + it * 4 * 32;
      *(u32x2*)(d + dd) = v;
      if (dd == 16) {
        u32x2 z0 = {which == 2 ? 0x3F80u : 0u, 0u};
        u32x4 z1 = {0u, 0u, 0u, 0u};
        *(u32x2*)(d + 20) = z0;
        *(u32x4*)(d + 24) = z1;
      }
    }
  }
};
struct EpiF32 {
  static constexpr bool DIRECT = true, PREFW = false;
  float* out;
};

typedef GC<256, 256, 2, 4, 2> G1;
typedef GC<256, 128, 4, 2, 3> G2;

constexpr float LOG2E = 1.4426950408889634f;

__global__ __launch_bounds__(512, 2) void k_proj(const u16* __restrict__ Ax, const u16* __restrict__ Awqkv,
                                                 const u16* __restrict__ Bx, const u16* __restrict__ Bckv,
                                                 const u16* __restrict__ Bwq, const u16* __restrict__ Bwuk,
                                                 const u16* __restrict__ Bwuv, u16* __restrict__ qkvA,
                                                 u16* __restrict__ qB, u16* __restrict__ kB, u16* __restrict__ vB) {
  const int b = blockIdx.x, xcd = b & 7, idx = b >> 3;
  const int tn = xcd + 8 * (idx >> 3), tm = idx & 7;
  const int m0 = tm * 256;
  if (tn < 8) {
    EpiQKVA e{qkvA, 0.22360679774997896f * LOG2E};
    gemm_tile<G1>(Ax, DM, Awqkv, 3 * DM, m0, tn * 256, 3 * DM, 0, DM / 64, e);
  } else if (tn < 24) {
    EpiHead e{qB, 0.08838834764831845f * LOG2E};
    gemm_tile<G1>(Bx, DM, Bwq, NB, m0, (tn - 8) * 256, NB, 0, DM / 64, e);
  } else {
    const bool isv = tn >= 40;
    EpiHead e{isv ? vB : kB, 1.f};
    gemm_tile<G1>(Bckv, DC, isv ? Bwuv : Bwuk, NB, m0, (tn - (isv ? 40 : 24)) * 256, NB, 0, DC / 64, e);
  }
}

constexpr int SPLITK = 5;
__global__ __launch_bounds__(512, 2) void k_out(const u16* __restrict__ oA, const u16* __restrict__ Awo,
                                                const u16* __restrict__ ctx, const u16* __restrict__ Bwo,
                                                u16* __restrict__ out, float* __restrict__ part) {
  const int b = blockIdx.x;
  if (b < 40 * SPLITK) {
    const int tm = b & 7, rest = b >> 3, tn = rest % 5, sp = rest / 5;
    EpiF32 e{part + ((size_t)sp * 40 + tm * 5 + tn) * (256 * 128)};
    gemm_tile<G2, EpiF32, true>(ctx, DHB, Bwo, DM, tm * 256, tn * 128, DM, sp * 13 * 64, sp == 4 ? 12 : 13, e);
  } else {
    const int t = b - 40 * SPLITK, tm = t & 7, tn = t >> 3;
    EpiPlain e{out, 2 * DM, 1.f};
    gemm_tile<G2>(oA, DM, Awo, DM, tm * 256, tn * 128, DM, 0, DM / 64, e);
  }
}

__global__ __launch_bounds__(256) void k_reduce(const float* __restrict__ part, u16* __restrict__ out) {
  const int i = blockIdx.x * 256 + threadIdx.x;
  const int tile = i >> 13, w = (i >> 10) & 7, j = (i >> 6) & 15, lane = i & 63;
  const int nt = j >> 3, mt = (j >> 2) & 1, g = j & 3;
  const int tm = tile / 5, tn = tile - tm * 5;
  const int m = tm * 256 + (w >> 1) * 64 + mt * 32 + (lane & 31);
  const int n = tn * 128 + (w & 1) * 64 + nt * 32 + 8 * g + 4 * (lane >> 5);
  const f32x4* p = (const f32x4*)part + i;
  f32x4 s = p[0];
#pragma unroll
  for (int sp = 1; sp < SPLITK; ++sp) s += p[(size_t)sp * 40 * 8192];
  u32x2 v = {cvtpk(s[0], s[1]), cvtpk(s[2], s[3])};
  *(u32x2*)(out + (size_t)m * 2 * DM + DM + n) = v;
}

constexpr float THR = 8.f;
DI bf16x8 pack8(const f32x16& x, int s) {
  u32x4 w = {cvtpk(x[8 * s], x[8 * s + 1]), cvtpk(x[8 * s + 2], x[8 * s + 3]), cvtpk(x[8 * s + 4], x[8 * s + 5]),
             cvtpk(x[8 * s + 6], x[8 * s + 7])};
  return __builtin_bit_cast(bf16x8, w);
}
DI f32x16 splat16(float v) {
  f32x16 x;
#pragma unroll
  for (int i = 0; i < 16; ++i) x[i] = v;
  return x;
}
DI float max16(const f32x16& x) {
  float a = fmaxf(fmaxf(x[0], x[1]), x[2]);
#pragma unroll
  for (int i = 3; i < 15; i += 2) a = fmaxf(fmaxf(a, x[i]), x[i + 1]);
  return fmaxf(a, x[15]);
}

#define SBAR() __builtin_amdgcn_sched_barrier(0)
constexpr float BIGSUM = 4096.f;
DI float sum16(const f32x16& x) {
  float a = (x[0] + x[1]) + (x[2] + x[3]), b = (x[4] + x[5]) + (x[6] + x[7]);
  float c = (x[8] + x[9]) + (x[10] + x[11]), d = (x[12] + x[13]) + (x[14] + x[15]);
  return (a + b) + (c + d);
}
DI void exp16(f32x16& x) {
#pragma unroll
  for (int i = 0; i < 16; ++i) x[i] = __builtin_amdgcn_exp2f(x[i]);
}
DI void attnB_item(const u16* __restrict__ Q, const u16* __restrict__ K, const u16* __restrict__ V,
                   u16* __restrict__ ctx, int head, int jitem) {
  const int tid = threadIdx.x, lane = tid & 63, wid = __builtin_amdgcn_readfirstlane(tid >> 6);
  const int r = lane & 31, h = lane >> 5;
  constexpr unsigned KBUF = 0, VBUF = 32768, TB = 16384, STG = 65536;
  unsigned koff, voff;
  {
    const int l4 = lane >> 4, c = lane & 15;
    koff = (unsigned)((16 * wid + l4) * DHB + ((c ^ l4) * 8)) * 2u;
    voff = (unsigned)((16 * wid + l4) * DHB + ((c ^ (l4 << 2)) * 8)) * 2u;
  }
  const u16* Kh = K + (size_t)head * S_ * DHB;
  const u16* Vh = V + (size_t)head * S_ * DHB;
  auto stageK = [&](int buf, int t) {
    const u16* g = Kh + (size_t)t * 64 * DHB;
    dma_base(KBUF + buf * TB + wid * 4096);
    dma_piece<0>(g, koff); dma_piece_x<1024, 64>(g, koff); dma_piece_x<2048, 128>(g, koff); dma_piece_x<3072, 192>(g, koff);
  };
  auto stageV = [&](int buf, int t) {
    const u16* g = Vh + (size_t)t * 64 * DHB;
    dma_base(VBUF + buf * TB + wid * 4096);
    dma_piece<0>(g, voff); dma_piece<1024>(g, voff); dma_piece<2048>(g, voff); dma_piece<3072>(g, voff);
  };
  unsigned ka[8], va[4];
#pragma unroll
  for (int ks = 0; ks < 8; ++ks) {
    ka[ks] = KBUF + r * 256 + (((2 * ks + h) ^ (r & 15)) * 16);
    asm volatile("" : "+v"(ka[ks]));
  }
  {
    const int q = (lane & 15) >> 2, p = lane & 3, cb = (lane >> 4) & 1;
#pragma unroll
    for (int db = 0; db < 4; ++db) va[db] = VBUF + (4 * h + q) * 256 + ((db ^ q) * 64) + cb * 32 + p * 8;
  }
  bf16x8 qf[8];
  f32x16 o[4], negm;
  float mcur, lsum;
  auto loadQ = [&](int qb) {
    int lz = threadIdx.x;
    asm volatile("" : "+v"(lz));
    const u16* qp = Q + ((size_t)head * S_ + qb * 128) * DHB + (unsigned)((wid * 32 + (lz & 31)) * DHB + ((lz >> 5) & 1) * 8);
#pragma unroll
    for (int ks = 0; ks < 8; ++ks) qf[ks] = *(const bf16x8*)(qp + ks * 16);
  };
  stageK(0, 0); stageK(1, 1); stageV(0, 0);
  loadQ(15 - jitem);

  auto qk_abs = [&](unsigned off) {
    bf16x8 kf[8];
#pragma unroll
    for (int ks = 0; ks < 8; ++ks) kf[ks] = lds_read128(off + ka[ks]);
    f32x16 s = MFMA32(kf[0], qf[0], f32x16{});
#pragma unroll
    for (int ks = 1; ks < 8; ++ks) s = MFMA32(kf[ks], qf[ks], s);
    return s;
  };
  auto remax = [&](const f32x16& st, bool first, f32x16& s) {
    const float tm = xhalf_max(max16(st));
    const float mnew = first ? tm : fmaxf(tm, mcur);
    const float delta = mnew - mcur;
    const float f = __builtin_amdgcn_exp2f(-fmaxf(delta, 0.f));
    mcur = mnew;
    negm = splat16(-mnew);
#pragma unroll
    for (int i = 0; i < 16; ++i) s[i] = __builtin_amdgcn_exp2f(st[i] - mnew);
#pragma unroll
    for (int db = 0; db < 4; ++db)
#pragma unroll
      for (int i = 0; i < 16; ++i) o[db][i] *= f;
    lsum *= f;
    return delta;
  };
  auto slow = [&](int blk) {
    int lz = threadIdx.x;
    asm volatile("" : "+v"(lz));
    const u16* kp = Kh + (size_t)blk * 32 * DHB + (unsigned)((lz & 31) * DHB + ((lz >> 5) & 1) * 8);
    f32x16 st = f32x16{};
#pragma unroll
    for (int ks = 0; ks < 8; ++ks) {
      st = MFMA32(*(const bf16x8*)(kp + ks * 16), qf[ks], st);
      asm volatile("" ::: "memory");
      SBAR();
    }
    return st;
  };
  auto pv = [&](unsigned off, const f32x16& p) {
#pragma unroll
    for (int sx = 0; sx < 2; ++sx) {
      const bf16x8 pf = pack8(p, sx);
#pragma unroll
      for (int db = 0; db < 4; ++db) {
        const bf16x8 vf = cat8(lds_read_tr(off + va[db] + (16 * sx) * 256), lds_read_tr(off + va[db] + (16 * sx + 8) * 256));
        o[db] = MFMA32(vf, pf, o[db]);
      }
    }
  };
#define EXP2(x) x = __builtin_amdgcn_exp2f(x)
#define VRD(dst, off) dst = cat8(lds_read_tr(off), lds_read_tr((off) + 2048))

#pragma unroll 1
  for (int pass = 0; pass < 2; ++pass) {
    const int qb = pass ? jitem : 15 - jitem;
    const int ntile = 2 * qb + 2;
    const int kbd = 4 * qb + wid;
    auto maskblk = [&](f32x16& s, int kb) {
      int lz = threadIdx.x;
      asm volatile("" : "+v"(lz));
      const int dq = (kbd - kb) * 32 + (lz & 31) - 4 * ((lz >> 5) & 1);
#pragma unroll
      for (int i = 0; i < 16; ++i)
        if ((i & 3) + 8 * (i >> 2) > dq) s[i] = -__builtin_inff();
    };
    {
      float z = 0.f;
      asm volatile("" : "+v"(z));
#pragma unroll
      for (int db = 0; db < 4; ++db) o[db] = splat16(z);
      lsum = z;
    }
#pragma unroll
    for (int ks = 0; ks < 8; ++ks) asm volatile("" : "+v"(qf[ks]));
    WAIT_VM0();
    BARRIER();
    {
      f32x16 sa, sb;
      bf16x8 kg[8];
      float pa;
      {
        f32x16 st = qk_abs(0);
        if (kbd == 0) maskblk(st, 0);
        const float m = xhalf_max(max16(st));
        mcur = m;
        negm = splat16(-m);
#pragma unroll
        for (int i = 0; i < 8; ++i) sa[i] = __builtin_amdgcn_exp2f(st[i] - m);
#pragma unroll
        for (int i = 8; i < 16; ++i) sa[i] = st[i] - m;
        pa = ((sa[0] + sa[1]) + (sa[2] + sa[3])) + ((sa[4] + sa[5]) + (sa[6] + sa[7]));
#pragma unroll
        for (int ks = 0; ks < 8; ++ks) kg[ks] = lds_read128(ka[ks] + 8192);
      }
      auto tile = [&](auto BUFC, auto LASTC, int t) {
        constexpr unsigned B_ = decltype(BUFC)::value;
        constexpr bool LAST = decltype(LASTC)::value;
        constexpr unsigned vo = B_ * TB, vn = TB - vo;
        constexpr unsigned kn = TB - vo, kd = vo;
        const u16* kbn = Kh + (size_t)min(t + 2, 31) * 64 * DHB;
        const u16* vbn = Vh + (size_t)min(t + 1, 31) * 64 * DHB;
#define DMAK(i) do { if ((i) == 0) { dma_base(KBUF + kd + wid * 4096); dma_piece<0>(kbn, koff); } else dma_piece_x<(i) * 1024, (i) * 64>(kbn, koff); } while (0)
#define DMAV(i) do { if ((i) == 0) dma_base(VBUF + vn + wid * 4096); dma_piece<(i) * 1024>(vbn, voff); } while (0)
        bf16x8 kf[8], vf[8], vg[8];
#define KF(i) kf[i] = lds_read128(kn + ka[i])
#define KG(i) kg[i] = lds_read128(kn + ka[i] + 8192)
        sb = MFMA32(kg[0], qf[0], negm);
        VRD(vf[0], vo + va[0]); DMAV(0);
        EXP2(sa[8]); EXP2(sa[9]);
        SBAR();
        sb = MFMA32(kg[1], qf[1], sb);
        VRD(vf[1], vo + va[1]); DMAV(1);
        EXP2(sa[10]); EXP2(sa[11]);
        SBAR();
        sb = MFMA32(kg[2], qf[2], sb);
        VRD(vf[2], vo + va[2]); DMAV(2);
        EXP2(sa[12]); EXP2(sa[13]);
        float pa2 = (sa[8] + sa[9]) + (sa[10] + sa[11]);
        SBAR();
        sb = MFMA32(kg[3], qf[3], sb);
        VRD(vf[3], vo + va[3]); DMAV(3);
        EXP2(sa[14]); EXP2(sa[15]);
        pa += sa[12] + sa[13];
        SBAR();
        sb = MFMA32(kg[4], qf[4], sb);
        DMAK(0);
        pa2 += sa[14] + sa[15];
        pa += pa2;
        if (__any(!(pa <= BIGSUM))) {
          f32x16 st = slow(2 * t);
          if (2 * t >= kbd) maskblk(st, 2 * t);
          const float delta = remax(st, false, sa);
#pragma unroll
          for (int i = 0; i < 16; ++i) sb[i] -= delta;
          pa = sum16(sa);
        }
        sb = MFMA32(kg[5], qf[5], sb);
        VRD(vf[4], vo + va[0] + 4096); VRD(vf[5], vo + va[1] + 4096);
        const bf16x8 p0 = pack8(sa, 0);
        lsum += pa;
        SBAR();
        sb = MFMA32(kg[6], qf[6], sb);
        VRD(vf[6], vo + va[2] + 4096); DMAK(1);
        const bf16x8 p1 = pack8(sa, 1);
        SBAR();
        sb = MFMA32(kg[7], qf[7], sb);
        VRD(vf[7], vo + va[3] + 4096); DMAK(2);
        SBAR();
        o[0] = MFMA32(vf[0], p0, o[0]);
        KF(0); DMAK(3);
        if (2 * t + 1 >= kbd) maskblk(sb, 2 * t + 1);
        SBAR();
        o[1] = MFMA32(vf[1], p0, o[1]);
        KF(1);
        EXP2(sb[0]); EXP2(sb[1]); EXP2(sb[2]);
        SBAR();
        o[2] = MFMA32(vf[2], p0, o[2]);
        KF(2);
        EXP2(sb[3]); EXP2(sb[4]);
        SBAR();
        o[3] = MFMA32(vf[3], p0, o[3]);
        KF(3);
        EXP2(sb[5]); EXP2(sb[6]);
        float pb = (sb[0] + sb[1]) + (sb[2] + sb[3]);
        SBAR();
        o[0] = MFMA32(vf[4], p1, o[0]);
        KF(4);
        EXP2(sb[7]); EXP2(sb[8]);
        SBAR();
        o[1] = MFMA32(vf[5], p1, o[1]);
        KF(5);
        EXP2(sb[9]); EXP2(sb[10]);
        float pb2 = (sb[4] + sb[5]) + (sb[6] + sb[7]);
        SBAR();
        o[2] = MFMA32(vf[6], p1, o[2]);
        KF(6);
        EXP2(sb[11]); EXP2(sb[12]);
        SBAR();
        o[3] = MFMA32(vf[7], p1, o[3]);
        KF(7);
        EXP2(sb[13]); EXP2(sb[14]);
        pb += (sb[8] + sb[9]) + sb[10];
        SBAR();
        sa = MFMA32(kf[0], qf[0], negm);
        VRD(vg[0], vo + va[0] + 8192);
        EXP2(sb[15]);
        pb2 += (sb[11] + sb[12]) + sb[13];
        SBAR();
        sa = MFMA32(kf[1], qf[1], sa);
        VRD(vg[1], vo + va[1] + 8192);
        pb += sb[14] + sb[15];
        pb += pb2;
        if (__any(!(pb <= BIGSUM))) {
          f32x16 st = slow(2 * t + 1);
          if (2 * t + 1 >= kbd) maskblk(st, 2 * t + 1);
          const float delta = remax(st, false, sb);
#pragma unroll
          for (int i = 0; i < 16; ++i) sa[i] -= delta;
          pb = sum16(sb);
        }
        sa = MFMA32(kf[2], qf[2], sa);
        VRD(vg[2], vo + va[2] + 8192);
        const bf16x8 r0 = pack8(sb, 0);
        lsum += pb;
        SBAR();
        sa = MFMA32(kf[3], qf[3], sa);
        VRD(vg[3], vo + va[3] + 8192);
        const bf16x8 r1 = pack8(sb, 1);
        SBAR();
        sa = MFMA32(kf[4], qf[4], sa);
        VRD(vg[4], vo + va[0] + 12288);
        SBAR();
        sa = MFMA32(kf[5], qf[5], sa);
        VRD(vg[5], vo + va[1] + 12288);
        SBAR();
        sa = MFMA32(kf[6], qf[6], sa);
        VRD(vg[6], vo + va[2] + 12288);
        SBAR();
        sa = MFMA32(kf[7], qf[7], sa);
        VRD(vg[7], vo + va[3] + 12288);
        SBAR();
        o[0] = MFMA32(vg[0], r0, o[0]);
        KG(0);
        if (2 * t + 2 >= kbd) maskblk(sa, 2 * t + 2);
        SBAR();
        o[1] = MFMA32(vg[1], r0, o[1]);
        KG(1); EXP2(sa[0]); EXP2(sa[1]);
        SBAR();
        o[2] = MFMA32(vg[2], r0, o[2]);
        KG(2); EXP2(sa[2]); EXP2(sa[3]);
        SBAR();
        o[3] = MFMA32(vg[3], r0, o[3]);
        KG(3); EXP2(sa[4]); EXP2(sa[5]);
        SBAR();
        o[0] = MFMA32(vg[4], r1, o[0]);
        KG(4); EXP2(sa[6]); EXP2(sa[7]);
        pa = (sa[0] + sa[1]) + (sa[2] + sa[3]);
        SBAR();
        o[1] = MFMA32(vg[5], r1, o[1]);
        KG(5);
        pa += (sa[4] + sa[5]) + (sa[6] + sa[7]);
        SBAR();
        o[2] = MFMA32(vg[6], r1, o[2]);
        KG(6); KG(7);
        SBAR();
        o[3] = MFMA32(vg[7], r1, o[3]);
#undef KF
#undef KG
        WAIT_VM0();
        BARRIER();
#undef DMAK
#undef DMAV
      };
      typedef std::integral_constant<unsigned, 0> C0;
      typedef std::integral_constant<unsigned, 1> C1;
      int t = 0;
      do {
        tile(C0{}, std::false_type{}, t);
        tile(C1{}, std::false_type{}, t + 1);
        t += 2;
      } while (t < ntile);
    }
    if (pass == 0) { stageK(0, 0); stageK(1, 1); stageV(0, 0); }
    const float inv = 1.f / xhalf_sum(lsum);
    {
      int lz = threadIdx.x;
      asm volatile("" : "+v"(lz));
      const int lane = lz & 63, r = lz & 31, h = (lz >> 5) & 1;
      const unsigned sb_ = STG + wid * 4096;
      u16* op = ctx + ((size_t)head * S_ + qb * 128 + wid * 32 + (lane >> 3)) * DHB + (lane & 7) * 8;
#pragma unroll
      for (int hf = 0; hf < 2; ++hf) {
#pragma unroll
        for (int d2 = 0; d2 < 2; ++d2)
#pragma unroll
          for (int g = 0; g < 4; ++g) {
            const f32x16& x = o[2 * hf + d2];
            u32x2 v = {cvtpk(x[4 * g] * inv, x[4 * g + 1] * inv), cvtpk(x[4 * g + 2] * inv, x[4 * g + 3] * inv)};
            *(lds_u32x2*)(uintptr_t)(sb_ + r * 128 + (((4 * d2 + g) ^ (r & 7)) * 16) + h * 8) = v;
          }
#pragma unroll
        for (int it = 0; it < 4; ++it) {
          const int rr = it * 8 + (lane >> 3);
          const bf16x8 v = lds_read128(sb_ + rr * 128 + (((lane & 7) ^ (rr & 7)) * 16));
          *(bf16x8*)(op + (size_t)it * 8 * DHB + hf * 64) = v;
        }
      }
    }
    if (pass == 0) loadQ(jitem);
  }
#undef EXP2
#undef VRD
}

DI void attnA_block(const u16* __restrict__ qkv, u16* __restrict__ oA, int head, int qb) {
  const int tid = threadIdx.x, lane = tid & 63, wid = __builtin_amdgcn_readfirstlane(tid >> 6);
  const int r = lane & 31, h = lane >> 5;
  const int q0 = qb * 256 + wid * 64;
  constexpr unsigned KBUF = 0, VBUF = 16384, TB = 8192;
  const u16* Qh = qkv + (size_t)(0 * HA + head) * S_ * 32;
  const u16* Kh = qkv + (size_t)(1 * HA + head) * S_ * 32;
  const u16* Vh = qkv + (size_t)(2 * HA + head) * S_ * 32;
  unsigned koff, voff;
  {
    const int row = 32 * wid + (lane >> 2), c = lane & 3;
    koff = (unsigned)(row * 64 + ((c ^ ((row >> 2) & 3)) * 16));
    voff = (unsigned)(row * 64 + c * 16);
  }
  auto stage = [&](int buf, int t) {
    dma_base(KBUF + buf * TB + wid * 2048);
    dma_piece<0>(Kh + (size_t)t * 128 * 32, koff); dma_piece<1024>(Kh + (size_t)t * 128 * 32, koff);
    dma_base(VBUF + buf * TB + wid * 2048);
    dma_piece<0>(Vh + (size_t)t * 128 * 32, voff); dma_piece<1024>(Vh + (size_t)t * 128 * 32, voff);
  };
  stage(0, 0);
  bf16x8 qf[2][2];
#pragma unroll
  for (int qt = 0; qt < 2; ++qt)
#pragma unroll
    for (int ks = 0; ks < 2; ++ks) qf[qt][ks] = *(const bf16x8*)(Qh + (size_t)(q0 + 32 * qt + r) * 32 + ks * 16 + h * 8);
  unsigned ka[2], va;
#pragma unroll
  for (int ks = 0; ks < 2; ++ks) {
    ka[ks] = KBUF + r * 64 + (((2 * ks + h) ^ ((r >> 2) & 3)) * 16);
    asm volatile("" : "+v"(ka[ks]));
  }
  {
    const int q = (lane & 15) >> 2, p = lane & 3, cb = (lane >> 4) & 1;
    va = VBUF + (4 * h + q) * 64 + cb * 32 + p * 8;
  }
  f32x16 o[2], negm[2];
  float mcur[2];
#pragma unroll
  for (int qt = 0; qt < 2; ++qt) { o[qt] = f32x16{}; negm[qt] = f32x16{}; mcur[qt] = 0.f; }
#pragma unroll
  for (int qt = 0; qt < 2; ++qt) { asm volatile("" : "+v"(qf[qt][0])); asm volatile("" : "+v"(qf[qt][1])); }
  WAIT_VM0();
  BARRIER();
  constexpr int NKT = S_ / 128;
#define EXP2(x) x = __builtin_amdgcn_exp2f(x)
#define EXP4(s, i) EXP2(s[i]); EXP2(s[i + 1]); EXP2(s[i + 2]); EXP2(s[i + 3])
#define VRDA(dst, off) dst = cat8(lds_read_tr(off), lds_read_tr((off) + 512))
  auto slow_tile = [&](bool first) {
#pragma unroll 1
    for (int kb = 0; kb < 4; ++kb) {
      const bf16x8 k0 = lds_read128(ka[0] + kb * 2048), k1 = lds_read128(ka[1] + kb * 2048);
      bf16x8 v0, v1;
      VRDA(v0, va + kb * 2048); VRDA(v1, va + kb * 2048 + 1024);
#pragma unroll
      for (int qt = 0; qt < 2; ++qt) {
        f32x16 st = MFMA32(k0, qf[qt][0], f32x16{});
        st = MFMA32(k1, qf[qt][1], st);
        const float tm = xhalf_max(max16(st));
        const float mnew = (first && kb == 0) ? tm : fmaxf(tm, mcur[qt]);
        const float f = __builtin_amdgcn_exp2f(-fmaxf(mnew - mcur[qt], 0.f));
        mcur[qt] = mnew;
#pragma unroll
        for (int i = 0; i < 16; ++i) { st[i] = __builtin_amdgcn_exp2f(st[i] - mnew); o[qt][i] *= f; }
        o[qt] = MFMA32(v0, pack8(st, 0), o[qt]);
        o[qt] = MFMA32(v1, pack8(st, 1), o[qt]);
      }
    }
    negm[0] = splat16(-mcur[0]);
    negm[1] = splat16(-mcur[1]);
  };
  for (int t = 0; t < NKT; ++t) {
    if (t + 1 < NKT) stage((t + 1) & 1, t + 1);
    const f32x16 os0 = o[0], os1 = o[1];
    f32x16 sa0, sa1, sb0, sb1;
    bf16x8 k0, k1, v0, v1;
    k0 = lds_read128(ka[0]); k1 = lds_read128(ka[1]);
    sa0 = MFMA32(k0, qf[0][0], negm[0]); sa1 = MFMA32(k0, qf[1][0], negm[1]);
    sa0 = MFMA32(k1, qf[0][1], sa0); sa1 = MFMA32(k1, qf[1][1], sa1);
    k0 = lds_read128(ka[0] + 2048); k1 = lds_read128(ka[1] + 2048);
#define UNIT(C0, C1, N0, N1, kb)                                                                   \
    do {                                                                                             \
      VRDA(v0, va + (kb) * 2048); VRDA(v1, va + (kb) * 2048 + 1024);                                 \
      SBAR();                                                                                        \
      if ((kb) < 3) N0 = MFMA32(k0, qf[0][0], negm[0]);                                              \
      EXP4(C0, 0);                                                                                   \
      SBAR();                                                                                        \
      if ((kb) < 3) N1 = MFMA32(k0, qf[1][0], negm[1]);                                              \
      EXP4(C0, 4);                                                                                   \
      SBAR();                                                                                        \
      if ((kb) < 3) N0 = MFMA32(k1, qf[0][1], N0);                                                   \
      EXP4(C0, 8);                                                                                   \
      const bf16x8 p00 = pack8(C0, 0);                                                               \
      SBAR();                                                                                        \
      if ((kb) < 3) N1 = MFMA32(k1, qf[1][1], N1);                                                   \
      EXP4(C0, 12);                                                                                  \
      if ((kb) < 2) { k0 = lds_read128(ka[0] + ((kb) + 2) * 2048); k1 = lds_read128(ka[1] + ((kb) + 2) * 2048); } \
      SBAR();                                                                                        \
      o[0] = MFMA32(v0, p00, o[0]);                                                                  \
      EXP4(C1, 0);                                                                                   \
      const bf16x8 p01 = pack8(C0, 1);                                                               \
      SBAR();                                                                                        \
      o[0] = MFMA32(v1, p01, o[0]);                                                                  \
      EXP4(C1, 4); EXP4(C1, 8);                                                                      \
      const bf16x8 p10 = pack8(C1, 0);                                                               \
      SBAR();                                                                                        \
      o[1] = MFMA32(v0, p10, o[1]);                                                                  \
      EXP4(C1, 12);                                                                                  \
      const bf16x8 p11 = pack8(C1, 1);                                                               \
      SBAR();                                                                                        \
      o[1] = MFMA32(v1, p11, o[1]);                                                                  \
    } while (0)
    UNIT(sa0, sa1, sb0, sb1, 0);
    UNIT(sb0, sb1, sa0, sa1, 1);
    UNIT(sa0, sa1, sb0, sb1, 2);
    UNIT(sb0, sb1, sa0, sa1, 3);
#undef UNIT
    const float lim = t == 0 ? -1.f : 1048576.f;
    const bool bad = h && !((o[0][8] - os0[8] <= lim) && (o[1][8] - os1[8] <= lim));
    if (__any(bad)) {
      o[0] = os0; o[1] = os1;
      slow_tile(t == 0);
    }
    ka[0] ^= TB; ka[1] ^= TB; va ^= TB;
    WAIT_VM0();
    BARRIER();
  }
#undef EXP2
#undef EXP4
#undef VRDA
#pragma unroll
  for (int qt = 0; qt < 2; ++qt) {
    const float l = __shfl(o[qt][8], r + 32);
    const float inv = 1.f / l;
    u16* op = oA + (size_t)(q0 + 32 * qt + r) * DM + head * DHA + 4 * h;
#pragma unroll
    for (int g = 0; g < 3; ++g) {
      if (g < 2 || h == 0) {
        u32x2 v = {cvtpk(o[qt][4 * g] * inv, o[qt][4 * g + 1] * inv), cvtpk(o[qt][4 * g + 2] * inv, o[qt][4 * g + 3] * inv)};
        *(u32x2*)(op + 8 * g) = v;
      }
    }
  }
}

constexpr int AQT = 2;
constexpr int NBLK_B = HB * 8, NBLK_A = HA * (S_ / (128 * AQT));
__global__ __launch_bounds__(256, 2) void k_attn(const u16* __restrict__ qB, const u16* __restrict__ kB,
                                                 const u16* __restrict__ vB, u16* __restrict__ ctx,
                                                 const u16* __restrict__ qkvA, u16* __restrict__ oA) {
  const int b = blockIdx.x;
  if (b < NBLK_B) {
    const int head = (b & 7) + 8 * (b >> 6), j = (b >> 3) & 7;
    attnB_item(qB, kB, vB, ctx, head, j);
  } else {
    const int a = b - NBLK_B;
    constexpr int PER = S_ / (128 * AQT);
    const int head = (a & 7) + 8 * (a / (8 * PER)), qb = (a >> 3) % PER;
    attnA_block(qkvA, oA, head, qb);
  }
}

extern "C" void kernel_launch(void* const* d_in, const int* in_sizes, int n_in, void* d_out, int out_size, void* d_ws,
                              size_t ws_size, hipStream_t stream) {
  const u16* Ax = (const u16*)d_in[0];
  const u16* Awqkv = (const u16*)d_in[1];
  const u16* Awo = (const u16*)d_in[2];
  const u16* Bx = (const u16*)d_in[3];
  const u16* Bckv = (const u16*)d_in[4];
  const u16* Bwq = (const u16*)d_in[5];
  const u16* Bwuk = (const u16*)d_in[6];
  const u16* Bwuv = (const u16*)d_in[7];
  const u16* Bwo = (const u16*)d_in[8];
  u16* out = (u16*)d_out;
  char* ws = (char*)d_ws;
  u16* qkvA = (u16*)ws;                 ws += (size_t)3 * HA * S_ * 32 * 2;
  u16* qB = (u16*)ws;                   ws += (size_t)S_ * NB * 2;
  u16* kB = (u16*)ws;                   ws += (size_t)S_ * NB * 2;
  u16* vB = (u16*)ws;                   ws += (size_t)S_ * NB * 2;
  u16* ctx = (u16*)ws;                  ws += (size_t)S_ * NB * 2;
  u16* oA = (u16*)ws;                   ws += (size_t)S_ * DM * 2;
  float* part = (float*)ws;             ws += (size_t)SPLITK * S_ * DM * 4;

  (void)hipFuncSetAttribute((const void*)k_proj, hipFuncAttributeMaxDynamicSharedMemorySize, G1::LDS);
  (void)hipFuncSetAttribute((const void*)k_out, hipFuncAttributeMaxDynamicSharedMemorySize, G2::LDS);
  (void)hipFuncSetAttribute((const void*)k_attn, hipFuncAttributeMaxDynamicSharedMemorySize, 81920);
  k_proj<<<448, 512, G1::LDS, stream>>>(Ax, Awqkv, Bx, Bckv, Bwq, Bwuk, Bwuv, qkvA, qB, kB, vB);
  k_attn<<<NBLK_B + NBLK_A, 256, 81920, stream>>>(qB, kB, vB, ctx, qkvA, oA);
  k_out<<<40 * SPLITK + 40, 512, G2::LDS, stream>>>(oA, Awo, ctx, Bwo, out, part);
  k_reduce<<<S_ * DM / 4 / 256, 256, 0, stream>>>(part, out);
}
```

```cpp
#include <hip/hip_runtime.h>
#include <hip/hip_bf16.h>
#include <stdint.h>
#include <type_traits>

constexpr int S_ = 2048;
constexpr int DM = 640;
constexpr int HA = 32, DHA = 20;
constexpr int HB = 32, DHB = 128, DC = 512;
constexpr int NB = HB * DHB;

typedef unsigned short u16;
typedef short bf16x8 __attribute__((ext_vector_type(8)));
typedef short s16x4 __attribute__((ext_vector_type(4)));
typedef float f32x16 __attribute__((ext_vector_type(16)));
typedef float f32x4 __attribute__((ext_vector_type(4)));
typedef float f32x2 __attribute__((ext_vector_type(2)));
typedef __bf16 bf16x2 __attribute__((ext_vector_type(2)));
typedef unsigned u32x2 __attribute__((ext_vector_type(2)));
typedef unsigned u32x4 __attribute__((ext_vector_type(4)));
typedef __attribute__((address_space(3))) s16x4 lds_s16x4;
typedef __attribute__((address_space(3))) bf16x8 lds_bf16x8;
typedef __attribute__((address_space(3))) u32x2 lds_u32x2;

#define DI __device__ __forceinline__
#define MFMA32(a, b, c) __builtin_amdgcn_mfma_f32_32x32x16_bf16((a), (b), (c), 0, 0, 0)

DI unsigned cvtpk(float lo, float hi) {
  f32x2 v = {lo, hi};
  bf16x2 b = __builtin_convertvector(v, bf16x2);
  return __builtin_bit_cast(unsigned, b);
}
DI void dma16(const void* sbase, unsigned voff, unsigned lds_addr) {
  unsigned keep;
  asm volatile("s_mov_b32 %0, m0\n\ts_mov_b32 m0, %3\n\ts_nop 0\n\tglobal_load_lds_dwordx4 %1, %2\n\ts_mov_b32 m0, %0"
               : "=&s"(keep) : "v"(voff), "s"(sbase), "s"(lds_addr) : "memory");
}
DI void dma_base(unsigned lds_addr) { asm volatile("s_mov_b32 m0, %0\n\ts_nop 0" :: "s"(lds_addr) : "memory"); }
template <int OFF> DI void dma_piece(const void* sbase, unsigned voff) {
  asm volatile("global_load_lds_dwordx4 %0, %1 offset:%2" :: "v"(voff), "s"(sbase), "i"(OFF) : "memory");
}
template <int OFF, int X> DI void dma_piece_x(const void* sbase, unsigned voff) {
  unsigned t;
  asm volatile("v_xor_b32 %0, %4, %1\n\tglobal_load_lds_dwordx4 %0, %2 offset:%3" : "=&v"(t) : "v"(voff), "s"(sbase), "i"(OFF), "i"(X) : "memory");
}
#define WAIT_VM0() asm volatile("s_waitcnt vmcnt(0)" ::: "memory")
#define BARRIER() do { asm volatile("s_waitcnt lgkmcnt(0)" ::: "memory"); __builtin_amdgcn_s_barrier(); asm volatile("" ::: "memory"); } while (0)

DI bf16x8 lds_read128(unsigned off) { return *(const lds_bf16x8*)(uintptr_t)off; }
DI s16x4 lds_read_tr(unsigned off) { return __builtin_amdgcn_ds_read_tr16_b64_v4i16((lds_s16x4*)(uintptr_t)off); }
DI bf16x8 cat8(s16x4 a, s16x4 b) { return __builtin_shufflevector(a, b, 0, 1, 2, 3, 4, 5, 6, 7); }
DI float xhalf_max(float x) {
  auto r = __builtin_amdgcn_permlane32_swap(__float_as_uint(x), __float_as_uint(x), false, false);
  return fmaxf(__uint_as_float(r[0]), __uint_as_float(r[1]));
}
DI float xhalf_sum(float x) {
  auto r = __builtin_amdgcn_permlane32_swap(__float_as_uint(x), __float_as_uint(x), false, false);
  return __uint_as_float(r[0]) + __uint_as_float(r[1]);
}

template <int BM, int BN, int WM, int WN, int NST_>
struct GCfg {
  static constexpr int NST = NST_;
  static constexpr int NW = WM * WN, TM = BM / WM, TN = BN / WN, MT = TM / 32, NT = TN / 32;
  static constexpr int XB = BM * 128, WROW = BN * 2, WB = 64 * WROW, STAGE = XB + WB;
  static constexpr int XI = BM / 8 / NW;
  static constexpr int WI = WB / 1024 / NW;
  static constexpr int WCH = BN / 8;
  static constexpr int WRPI = 64 / WCH;
  static constexpr int LDS = NST * STAGE;
};

template <class C, class Epi, bool XHEAD = false>
DI void gemm_tile(const u16* __restrict__ X, int ldx, const u16* __restrict__ W, int ldw, int m0, int n0, int ncols,
                  int kbeg, int nk, const Epi& epi) {
  const int tid = threadIdx.x, lane = tid & 63, wid = __builtin_amdgcn_readfirstlane(tid >> 6);
  const int wm = wid / C::WN_, wn = wid % C::WN_;
  const int r = lane & 31, h = lane >> 5;
  unsigned xoff[C::XI], woff[C::WI];
#pragma unroll
  for (int i = 0; i < C::XI; ++i) {
    const int row = 8 * (wid * C::XI + i) + (lane >> 3);
    xoff[i] = (unsigned)(row * ldx + (((lane & 7) ^ ((row >> 1) & 7)) * 8)) * 2u;
  }
#pragma unroll
  for (int i = 0; i < C::WI; ++i) {
    const int k = C::WRPI * (wid * C::WI + i) + lane / C::WCH;
    int col = (((lane % C::WCH) ^ ((k & 3) << 2)) * 8);
    if (n0 + col > ncols - 8) col = ncols - 8 - n0;
    woff[i] = (unsigned)(k * ldw + col) * 2u;
  }
  const u16* xs = XHEAD ? X + (size_t)m0 * ldx : X + (size_t)m0 * ldx + kbeg;
  const int kt0 = kbeg >> 6;
  auto xk = [&](int kt) { return XHEAD ? xs + (size_t)((kt + kt0) >> 1) * S_ * DHB + ((kt + kt0) & 1) * 64 : xs + kt * 64; };
  const u16* ws = W + (size_t)kbeg * ldw + n0;
  auto stage = [&](int buf, int kt) {
    const u16* xb = xk(kt);
    const u16* wb = ws + (size_t)kt * 64 * ldw;
    const unsigned lb = buf * C::STAGE;
#pragma unroll
    for (int i = 0; i < C::XI; ++i) dma16(xb, xoff[i], lb + (wid * C::XI + i) * 1024);
#pragma unroll
    for (int i = 0; i < C::WI; ++i) dma16(wb, woff[i], lb + C::XB + (wid * C::WI + i) * 1024);
  };
  unsigned xa[4];
#pragma unroll
  for (int ks = 0; ks < 4; ++ks) xa[ks] = (wm * C::TM + r) * 128 + (((2 * ks + h) ^ ((r >> 1) & 7)) * 16);
  unsigned wa[C::NT];
  {
    const int q = (lane & 15) >> 2, p = lane & 3, cb = (lane >> 4) & 1;
#pragma unroll
    for (int nt = 0; nt < C::NT; ++nt) {
      const int nb = wn * C::NT + nt;
      wa[nt] = C::XB + (8 * h + q) * C::WROW + ((nb ^ q) * 64) + cb * 32 + p * 8;
    }
  }
  f32x16 acc[C::NT][C::MT];
#pragma unroll
  for (int nt = 0; nt < C::NT; ++nt)
#pragma unroll
    for (int mt = 0; mt < C::MT; ++mt) acc[nt][mt] = f32x16{};

  constexpr int AHEAD = C::NST - 1, PER = C::XI + C::WI;
  stage(0, 0);
  u32x4 pfd = {0u, 0u, 0u, 0u};
  if constexpr (Epi::PREFW) {
    const int rows = nk * 8, r0 = ((m0 >> 8) & 7) * rows;
    int col = (tid & 31) * 8;
    if (n0 + col > ncols - 8) col = ncols - 8 - n0;
    const u16* pw = ws + (size_t)(r0 + (tid >> 5)) * ldw + col;
    for (int i = 0; i < rows; i += 16)
      asm volatile("global_load_dwordx4 %0, %1, off" : "+v"(pfd) : "v"(pw + (size_t)i * ldw) : "memory");
  }
  if constexpr (AHEAD == 2) { stage(1, 1); asm volatile("s_waitcnt vmcnt(%0)" :: "i"(PER) : "memory"); }
  else WAIT_VM0();
  asm volatile("" :: "v"(pfd));
  BARRIER();
  int cur = 0, nxt = AHEAD;
  for (int kt = 0; kt < nk; ++kt) {
    const unsigned lb = cur * C::STAGE;
    const bool more = kt + AHEAD < nk;
    const u16* xb = xk(kt + AHEAD);
    const u16* wb = ws + (size_t)(kt + AHEAD) * 64 * ldw;
    const unsigned nb = nxt * C::STAGE;
    cur = cur + 1 == C::NST ? 0 : cur + 1;
    nxt = nxt + 1 == C::NST ? 0 : nxt + 1;
#pragma unroll
    for (int ks = 0; ks < 4; ++ks) {
      bf16x8 xf[C::MT], wf[C::NT];
#pragma unroll
      for (int mt = 0; mt < C::MT; ++mt) xf[mt] = lds_read128(lb + xa[ks] + mt * 4096);
#pragma unroll
      for (int nt = 0; nt < C::NT; ++nt)
        wf[nt] = cat8(lds_read_tr(lb + wa[nt] + (16 * ks) * C::WROW), lds_read_tr(lb + wa[nt] + (16 * ks + 4) * C::WROW));
      if (more) {
        constexpr int TOT = C::XI + C::WI;
#pragma unroll
        for (int i = ks * TOT / 4; i < (ks + 1) * TOT / 4; ++i) {
          if (i < C::XI) dma16(xb, xoff[i], nb + (wid * C::XI + i) * 1024);
          else dma16(wb, woff[i - C::XI], nb + C::XB + (wid * C::WI + i - C::XI) * 1024);
        }
      }
#pragma unroll
      for (int nt = 0; nt < C::NT; ++nt)
#pragma unroll
        for (int mt = 0; mt < C::MT; ++mt) acc[nt][mt] = MFMA32(wf[nt], xf[mt], acc[nt][mt]);
    }
    if (AHEAD == 2 && more) asm volatile("s_waitcnt vmcnt(%0)" :: "i"(PER) : "memory");
    else WAIT_VM0();
    BARRIER();
  }
  if constexpr (Epi::DIRECT) {
    f32x4* dst = (f32x4*)epi.out + (size_t)wid * (C::NT * C::MT * 4 * 64) + lane;
#pragma unroll
    for (int nt = 0; nt < C::NT; ++nt)
#pragma unroll
      for (int mt = 0; mt < C::MT; ++mt)
#pragma unroll
        for (int g = 0; g < 4; ++g) {
          f32x4 v = {acc[nt][mt][4 * g], acc[nt][mt][4 * g + 1], acc[nt][mt][4 * g + 2], acc[nt][mt][4 * g + 3]};
          dst[((nt * C::MT + mt) * 4 + g) * 64] = v;
        }
  } else {
    static_assert(C::TN == 64, "staging layout");
    const unsigned sb = wid * (C::TM * 128);
#pragma unroll
    for (int nt = 0; nt < C::NT; ++nt)
#pragma unroll
      for (int g = 0; g < 4; ++g) {
        const int nl = nt * 32 + 8 * g + 4 * h;
        const float sc = epi.scale_of(n0 + wn * C::TN + nl);
        const unsigned a = sb + r * 128 + (((nl >> 3) ^ (r & 7)) * 16) + (nl & 4) * 2;
#pragma unroll
        for (int mt = 0; mt < C::MT; ++mt) {
          u32x2 v = {cvtpk(acc[nt][mt][4 * g] * sc, acc[nt][mt][4 * g + 1] * sc),
                     cvtpk(acc[nt][mt][4 * g + 2] * sc, acc[nt][mt][4 * g + 3] * sc)};
          *(lds_u32x2*)(uintptr_t)(a + mt * 4096) = v;
        }
      }
    epi.template store<C::TM>(sb, m0 + wm * C::TM, n0 + wn * C::TN, lane);
  }
}

template <int BM, int BN, int WM, int WN, int NST>
struct GC : GCfg<BM, BN, WM, WN, NST> { static constexpr int WN_ = WN; };

struct EpiPlain {
  static constexpr bool DIRECT = false, PREFW = false;
  u16* out; int ld; float scale;
  DI float scale_of(int) const { return scale; }
  template <int TM> DI void store(unsigned sb, int mb, int nb, int lane) const {
    const int rr = lane >> 3, c = lane & 7;
    u16* dst = out + (size_t)(mb + rr) * ld + nb + c * 8;
    const unsigned a = sb + rr * 128 + ((c ^ (rr & 7)) * 16);
#pragma unroll
    for (int it = 0; it < TM / 8; ++it) {
      const bf16x8 v = lds_read128(a + it * 1024);
      *(bf16x8*)(dst + (size_t)it * 8 * ld) = v;
    }
  }
};
struct EpiHead {
  static constexpr bool DIRECT = false, PREFW = true;
  u16* out; float scale;
  DI float scale_of(int) const { return scale; }
  template <int TM> DI void store(unsigned sb, int mb, int nb, int lane) const {
    const int rr = lane >> 3, c = lane & 7;
    u16* dst = out + ((size_t)(nb >> 7) * S_ + mb + rr) * DHB + (nb & 127) + c * 8;
    const unsigned a = sb + rr * 128 + ((c ^ (rr & 7)) * 16);
#pragma unroll
    for (int it = 0; it < TM / 8; ++it) {
      const bf16x8 v = lds_read128(a + it * 1024);
      *(bf16x8*)(dst + (size_t)it * 8 * DHB) = v;
    }
  }
};
struct EpiQKVA {
  static constexpr bool DIRECT = false, PREFW = true;
  u16* out; float qscale;
  DI float scale_of(int n) const { return n < DM ? qscale : 1.f; }
  template <int TM> DI void store(unsigned sb, int mb, int nb, int lane) const {
    const int rr = lane >> 4, gq = lane & 15;
    const int n = nb + gq * 4;
    if (n >= 3 * DM) return;
    const int which = n / DM, rem = n - which * DM, head = rem / DHA, dd = rem - head * DHA;
    u16* dst = out + ((size_t)((which * HA + head) * S_ + mb + rr)) * 32;
#pragma unroll 4
    for (int it = 0; it < TM / 4; ++it) {
      const int row = it * 4 + rr;
      const u32x2 v = *(const lds_u32x2*)(uintptr_t)(sb + row * 128 + (((gq >> 1) ^ (row & 7)) * 16) + (gq & 1) * 8);
      u16* d = dst + it * 4 * 32;
      *(u32x2*)(d + dd) = v;
      if (dd == 16) {
        u32x2 z0 = {which == 2 ? 0x3F80u : 0u, 0u};
        u32x4 z1 = {0u, 0u, 0u, 0u};
        *(u32x2*)(d + 20) = z0;
        *(u32x4*)(d + 24) = z1;
      }
    }
  }
};
struct EpiF32 {
  static constexpr bool DIRECT = true, PREFW = false;
  float* out;
};

typedef GC<256, 256, 2, 4, 2> G1;
typedef GC<256, 128, 4, 2, 3> G2;

constexpr float LOG2E = 1.4426950408889634f;

__global__ __launch_bounds__(512, 2) void k_proj(const u16* __restrict__ Ax, const u16* __restrict__ Awqkv,
                                                 const u16* __restrict__ Bx, const u16* __restrict__ Bckv,
                                                 const u16* __restrict__ Bwq, const u16* __restrict__ Bwuk,
                                                 const u16* __restrict__ Bwuv, u16* __restrict__ qkvA,
                                                 u16* __restrict__ qB, u16* __restrict__ kB, u16* __restrict__ vB) {
  const int b = blockIdx.x, xcd = b & 7, idx = b >> 3;
  const int tn = xcd + 8 * (idx >> 3), tm = idx & 7;
  const int m0 = tm * 256;
  if (tn < 8) {
    EpiQKVA e{qkvA, 0.22360679774997896f * LOG2E};
    gemm_tile<G1>(Ax, DM, Awqkv, 3 * DM, m0, tn * 256, 3 * DM, 0, DM / 64, e);
  } else if (tn < 24) {
    EpiHead e{qB, 0.08838834764831845f * LOG2E};
    gemm_tile<G1>(Bx, DM, Bwq, NB, m0, (tn - 8) * 256, NB, 0, DM / 64, e);
  } else {
    const bool isv = tn >= 40;
    EpiHead e{isv ? vB : kB, 1.f};
    gemm_tile<G1>(Bckv, DC, isv ? Bwuv : Bwuk, NB, m0, (tn - (isv ? 40 : 24)) * 256, NB, 0, DC / 64, e);
  }
}

constexpr int SPLITK = 5;
__global__ __launch_bounds__(512, 2) void k_out(const u16* __restrict__ oA, const u16* __restrict__ Awo,
                                                const u16* __restrict__ ctx, const u16* __restrict__ Bwo,
                                                u16* __restrict__ out, float* __restrict__ part) {
  const int b = blockIdx.x;
  if (b < 40 * SPLITK) {
    const int tm = b & 7, rest = b >> 3, tn = rest % 5, sp = rest / 5;
    EpiF32 e{part + ((size_t)sp * 40 + tm * 5 + tn) * (256 * 128)};
    gemm_tile<G2, EpiF32, true>(ctx, DHB, Bwo, DM, tm * 256, tn * 128, DM, sp * 13 * 64, sp == 4 ? 12 : 13, e);
  } else {
    const int t = b - 40 * SPLITK, tm = t & 7, tn = t >> 3;
    EpiPlain e{out, 2 * DM, 1.f};
    gemm_tile<G2>(oA, DM, Awo, DM, tm * 256, tn * 128, DM, 0, DM / 64, e);
  }
}

__global__ __launch_bounds__(256) void k_reduce(const float* __restrict__ part, u16* __restrict__ out) {
  const int i = blockIdx.x * 256 + threadIdx.x;
  const int tile = i >> 13, w = (i >> 10) & 7, j = (i >> 6) & 15, lane = i & 63;
  const int nt = j >> 3, mt = (j >> 2) & 1, g = j & 3;
  const int tm = tile / 5, tn = tile - tm * 5;
  const int m = tm * 256 + (w >> 1) * 64 + mt * 32 + (lane & 31);
  const int n = tn * 128 + (w & 1) * 64 + nt * 32 + 8 * g + 4 * (lane >> 5);
  const f32x4* p = (const f32x4*)part + i;
  f32x4 s = p[0];
#pragma unroll
  for (int sp = 1; sp < SPLITK; ++sp) s += p[(size_t)sp * 40 * 8192];
  u32x2 v = {cvtpk(s[0], s[1]), cvtpk(s[2], s[3])};
  *(u32x2*)(out + (size_t)m * 2 * DM + DM + n) = v;
}

constexpr float THR = 8.f;
DI bf16x8 pack8(const f32x16& x, int s) {
  u32x4 w = {cvtpk(x[8 * s], x[8 * s + 1]), cvtpk(x[8 * s + 2], x[8 * s + 3]), cvtpk(x[8 * s + 4], x[8 * s + 5]),
             cvtpk(x[8 * s + 6], x[8 * s + 7])};
  return __builtin_bit_cast(bf16x8, w);
}
DI f32x16 splat16(float v) {
  f32x16 x;
#pragma unroll
  for (int i = 0; i < 16; ++i) x[i] = v;
  return x;
}
DI float max16(const f32x16& x) {
  float a = fmaxf(fmaxf(x[0], x[1]), x[2]);
#pragma unroll
  for (int i = 3; i < 15; i += 2) a = fmaxf(fmaxf(a, x[i]), x[i + 1]);
  return fmaxf(a, x[15]);
}

#define SBAR() __builtin_amdgcn_sched_barrier(0)
constexpr float BIGSUM = 4096.f;
DI float sum16(const f32x16& x) {
  float a = (x[0] + x[1]) + (x[2] + x[3]), b = (x[4] + x[5]) + (x[6] + x[7]);
  float c = (x[8] + x[9]) + (x[10] + x[11]), d = (x[12] + x[13]) + (x[14] + x[15]);
  return (a + b) + (c + d);
}
DI void exp16(f32x16& x) {
#pragma unroll
  for (int i = 0; i < 16; ++i) x[i] = __builtin_amdgcn_exp2f(x[i]);
}
DI void attnB_item(const u16* __restrict__ Q, const u16* __restrict__ K, const u16* __restrict__ V,
                   u16* __restrict__ ctx, int head, int jitem) {
  const int tid = threadIdx.x, lane = tid & 63, wid = __builtin_amdgcn_readfirstlane(tid >> 6);
  const int r = lane & 31, h = lane >> 5;
  constexpr unsigned KBUF = 0, VBUF = 32768, TB = 16384, STG = 65536;
  unsigned koff, voff;
  {
    const int l4 = lane >> 4, c = lane & 15;
    koff = (unsigned)((16 * wid + l4) * DHB + ((c ^ l4) * 8)) * 2u;
    voff = (unsigned)((16 * wid + l4) * DHB + ((c ^ (l4 << 2)) * 8)) * 2u;
  }
  const u16* Kh = K + (size_t)head * S_ * DHB;
  const u16* Vh = V + (size_t)head * S_ * DHB;
  auto stageK = [&](int buf, int t) {
    const u16* g = Kh + (size_t)t * 64 * DHB;
    dma_base(KBUF + buf * TB + wid * 4096);
    dma_piece<0>(g, koff); dma_piece_x<1024, 64>(g, koff); dma_piece_x<2048, 128>(g, koff); dma_piece_x<3072, 192>(g, koff);
  };
  auto stageV = [&](int buf, int t) {
    const u16* g = Vh + (size_t)t * 64 * DHB;
    dma_base(VBUF + buf * TB + wid * 4096);
    dma_piece<0>(g, voff); dma_piece<1024>(g, voff); dma_piece<2048>(g, voff); dma_piece<3072>(g, voff);
  };
  unsigned ka[8], va[4];
#pragma unroll
  for (int ks = 0; ks < 8; ++ks) {
    ka[ks] = KBUF + r * 256 + (((2 * ks + h) ^ (r & 15)) * 16);
    asm volatile("" : "+v"(ka[ks]));
  }
  {
    const int q = (lane & 15) >> 2, p = lane & 3, cb = (lane >> 4) & 1;
#pragma unroll
    for (int db = 0; db < 4; ++db) va[db] = VBUF + (4 * h + q) * 256 + ((db ^ q) * 64) + cb * 32 + p * 8;
  }
  bf16x8 qf[8];
  f32x16 o[4], negm;
  float mcur, lsum;
  auto loadQ = [&](int qb) {
    int lz = threadIdx.x;
    asm volatile("" : "+v"(lz));
    const u16* qp = Q + ((size_t)head * S_ + qb * 128) * DHB + (unsigned)((wid * 32 + (lz & 31)) * DHB + ((lz >> 5) & 1) * 8);
#pragma unroll
    for (int ks = 0; ks < 8; ++ks) qf[ks] = *(const bf16x8*)(qp + ks * 16);
  };
  stageK(0, 0); stageK(1, 1); stageV(0, 0);
  loadQ(15 - jitem);

  auto qk_abs = [&](unsigned off) {
    bf16x8 kf[8];
#pragma unroll
    for (int ks = 0; ks < 8; ++ks) kf[ks] = lds_read128(off + ka[ks]);
    f32x16 s = MFMA32(kf[0], qf[0], f32x16{});
#pragma unroll
    for (int ks = 1; ks < 8; ++ks) s = MFMA32(kf[ks], qf[ks], s);
    return s;
  };
  auto remax = [&](const f32x16& st, bool first, f32x16& s) {
    const float tm = xhalf_max(max16(st));
    const float mnew = first ? tm : fmaxf(tm, mcur);
    const float delta = mnew - mcur;
    const float f = __builtin_amdgcn_exp2f(-fmaxf(delta, 0.f));
    mcur = mnew;
    negm = splat16(-mnew);
#pragma unroll
    for (int i = 0; i < 16; ++i) s[i] = __builtin_amdgcn_exp2f(st[i] - mnew);
#pragma unroll
    for (int db = 0; db < 4; ++db)
#pragma unroll
      for (int i = 0; i < 16; ++i) o[db][i] *= f;
    lsum *= f;
    return delta;
  };
  auto slow = [&](int blk) {
    int lz = threadIdx.x;
    asm volatile("" : "+v"(lz));
    const u16* kp = Kh + (size_t)blk * 32 * DHB + (unsigned)((lz & 31) * DHB + ((lz >> 5) & 1) * 8);
    f32x16 st = f32x16{};
#pragma unroll
    for (int ks = 0; ks < 8; ++ks) {
      st = MFMA32(*(const bf16x8*)(kp + ks * 16), qf[ks], st);
      asm volatile("" ::: "memory");
      SBAR();
    }
    return st;
  };
  auto pv = [&](unsigned off, const f32x16& p) {
#pragma unroll
    for (int sx = 0; sx < 2; ++sx) {
      const bf16x8 pf = pack8(p, sx);
#pragma unroll
      for (int db = 0; db < 4; ++db) {
        const bf16x8 vf = cat8(lds_read_tr(off + va[db] + (16 * sx) * 256), lds_read_tr(off + va[db] + (16 * sx + 8) * 256));
        o[db] = MFMA32(vf, pf, o[db]);
      }
    }
  };
#define EXP2(x) x = __builtin_amdgcn_exp2f(x)
#define VRD(dst, off) dst = cat8(lds_read_tr(off), lds_read_tr((off) + 2048))

#pragma unroll 1
  for (int pass = 0; pass < 2; ++pass) {
    const int qb = pass ? jitem : 15 - jitem;
    const int ntile = 2 * qb + 2;
    const int kbd = 4 * qb + wid;
    auto maskblk = [&](f32x16& s, int kb) {
      int lz = threadIdx.x;
      asm volatile("" : "+v"(lz));
      const int dq = (kbd - kb) * 32 + (lz & 31) - 4 * ((lz >> 5) & 1);
#pragma unroll
      for (int i = 0; i < 16; ++i)
        if ((i & 3) + 8 * (i >> 2) > dq) s[i] = -__builtin_inff();
    };
    {
      float z = 0.f;
      asm volatile("" : "+v"(z));
#pragma unroll
      for (int db = 0; db < 4; ++db) o[db] = splat16(z);
      lsum = z;
    }
#pragma unroll
    for (int ks = 0; ks < 8; ++ks) asm volatile("" : "+v"(qf[ks]));
    WAIT_VM0();
    BARRIER();
    {
      f32x16 sa, sb;
      bf16x8 kg[8];
      float pa;
      {
        f32x16 st = qk_abs(0);
        if (kbd == 0) maskblk(st, 0);
        const float m = xhalf_max(max16(st));
        mcur = m;
        negm = splat16(-m);
#pragma unroll
        for (int i = 0; i < 8; ++i) sa[i] = __builtin_amdgcn_exp2f(st[i] - m);
#pragma unroll
        for (int i = 8; i < 16; ++i) sa[i] = st[i] - m;
        pa = ((sa[0] + sa[1]) + (sa[2] + sa[3])) + ((sa[4] + sa[5]) + (sa[6] + sa[7]));
#pragma unroll
        for (int ks = 0; ks < 8; ++ks) kg[ks] = lds_read128(ka[ks] + 8192);
      }
      auto tile = [&](auto BUFC, auto LASTC, int t) {
        constexpr unsigned B_ = decltype(BUFC)::value;
        constexpr bool LAST = decltype(LASTC)::value;
        constexpr unsigned vo = B_ * TB, vn = TB - vo;
        constexpr unsigned kn = TB - vo, kd = vo;
        const u16* kbn = Kh + (size_t)min(t + 2, 31) * 64 * DHB;
        const u16* vbn = Vh + (size_t)min(t + 1, 31) * 64 * DHB;
#define DMAK(i) do { if ((i) == 0) { dma_base(KBUF + kd + wid * 4096); dma_piece<0>(kbn, koff); } else dma_piece_x<(i) * 1024, (i) * 64>(kbn, koff); } while (0)
#define DMAV(i) do { if ((i) == 0) dma_base(VBUF + vn + wid * 4096); dma_piece<(i) * 1024>(vbn, voff); } while (0)
        bf16x8 kf[8], vf[8], vg[8];
#define KF(i) kf[i] = lds_read128(kn + ka[i])
#define KG(i) kg[i] = lds_read128(kn + ka[i] + 8192)
        sb = MFMA32(kg[0], qf[0], negm);
        VRD(vf[0], vo + va[0]); DMAV(0);
        EXP2(sa[8]); EXP2(sa[9]);
        SBAR();
        sb = MFMA32(kg[1], qf[1], sb);
        VRD(vf[1], vo + va[1]); DMAV(1);
        EXP2(sa[10]); EXP2(sa[11]);
        SBAR();
        sb = MFMA32(kg[2], qf[2], sb);
        VRD(vf[2], vo + va[2]); DMAV(2);
        EXP2(sa[12]); EXP2(sa[13]);
        float pa2 = (sa[8] + sa[9]) + (sa[10] + sa[11]);
        SBAR();
        sb = MFMA32(kg[3], qf[3], sb);
        VRD(vf[3], vo + va[3]); DMAV(3);
        EXP2(sa[14]); EXP2(sa[15]);
        pa += sa[12] + sa[13];
        SBAR();
        sb = MFMA32(kg[4], qf[4], sb);
        DMAK(0);
        pa2 += sa[14] + sa[15];
        pa += pa2;
        if (__any(!(pa <= BIGSUM))) {
          f32x16 st = slow(2 * t);
          if (2 * t >= kbd) maskblk(st, 2 * t);
          const float delta = remax(st, false, sa);
#pragma unroll
          for (int i = 0; i < 16; ++i) sb[i] -= delta;
          pa = sum16(sa);
        }
        sb = MFMA32(kg[5], qf[5], sb);
        VRD(vf[4], vo + va[0] + 4096); VRD(vf[5], vo + va[1] + 4096);
        const bf16x8 p0 = pack8(sa, 0);
        lsum += pa;
        SBAR();
        sb = MFMA32(kg[6], qf[6], sb);
        VRD(vf[6], vo + va[2] + 4096); DMAK(1);
        const bf16x8 p1 = pack8(sa, 1);
        SBAR();
        sb = MFMA32(kg[7], qf[7], sb);
        VRD(vf[7], vo + va[3] + 4096); DMAK(2);
        SBAR();
        o[0] = MFMA32(vf[0], p0, o[0]);
        KF(0); DMAK(3);
        if (2 * t + 1 >= kbd) maskblk(sb, 2 * t + 1);
        SBAR();
        o[1] = MFMA32(vf[1], p0, o[1]);
        KF(1);
        EXP2(sb[0]); EXP2(sb[1]); EXP2(sb[2]);
        SBAR();
        o[2] = MFMA32(vf[2], p0, o[2]);
        KF(2);
        EXP2(sb[3]); EXP2(sb[4]);
        SBAR();
        o[3] = MFMA32(vf[3], p0, o[3]);
        KF(3);
        EXP2(sb[5]); EXP2(sb[6]);
        float pb = (sb[0] + sb[1]) + (sb[2] + sb[3]);
        SBAR();
        o[0] = MFMA32(vf[4], p1, o[0]);
        KF(4);
        EXP2(sb[7]); EXP2(sb[8]);
        SBAR();
        o[1] = MFMA32(vf[5], p1, o[1]);
        KF(5);
        EXP2(sb[9]); EXP2(sb[10]);
        float pb2 = (sb[4] + sb[5]) + (sb[6] + sb[7]);
        SBAR();
        o[2] = MFMA32(vf[6], p1, o[2]);
        KF(6);
        EXP2(sb[11]); EXP2(sb[12]);
        SBAR();
        o[3] = MFMA32(vf[7], p1, o[3]);
        KF(7);
        EXP2(sb[13]); EXP2(sb[14]);
        pb += (sb[8] + sb[9]) + sb[10];
        SBAR();
        sa = MFMA32(kf[0], qf[0], negm);
        VRD(vg[0], vo + va[0] + 8192);
        EXP2(sb[15]);
        pb2 += (sb[11] + sb[12]) + sb[13];
        SBAR();
        sa = MFMA32(kf[1], qf[1], sa);
        VRD(vg[1], vo + va[1] + 8192);
        pb += sb[14] + sb[15];
        pb += pb2;
        if (__any(!(pb <= BIGSUM))) {
          f32x16 st = slow(2 * t + 1);
          if (2 * t + 1 >= kbd) maskblk(st, 2 * t + 1);
          const float delta = remax(st, false, sb);
#pragma unroll
          for (int i = 0; i < 16; ++i) sa[i] -= delta;
          pb = sum16(sb);
        }
        sa = MFMA32(kf[2], qf[2], sa);
        VRD(vg[2], vo + va[2] + 8192);
        const bf16x8 r0 = pack8(sb, 0);
        lsum += pb;
        SBAR();
        sa = MFMA32(kf[3], qf[3], sa);
        VRD(vg[3], vo + va[3] + 8192);
        const bf16x8 r1 = pack8(sb, 1);
        SBAR();
        sa = MFMA32(kf[4], qf[4], sa);
        VRD(vg[4], vo + va[0] + 12288);
        SBAR();
        sa = MFMA32(kf[5], qf[5], sa);
        VRD(vg[5], vo + va[1] + 12288);
        SBAR();
        sa = MFMA32(kf[6], qf[6], sa);
        VRD(vg[6], vo + va[2] + 12288);
        SBAR();
        sa = MFMA32(kf[7], qf[7], sa);
        VRD(vg[7], vo + va[3] + 12288);
        SBAR();
        o[0] = MFMA32(vg[0], r0, o[0]);
        KG(0);
        if (2 * t + 2 >= kbd) maskblk(sa, 2 * t + 2);
        SBAR();
        o[1] = MFMA32(vg[1], r0, o[1]);
        KG(1); EXP2(sa[0]); EXP2(sa[1]);
        SBAR();
        o[2] = MFMA32(vg[2], r0, o[2]);
        KG(2); EXP2(sa[2]); EXP2(sa[3]);
        SBAR();
        o[3] = MFMA32(vg[3], r0, o[3]);
        KG(3); EXP2(sa[4]); EXP2(sa[5]);
        SBAR();
        o[0] = MFMA32(vg[4], r1, o[0]);
        KG(4); EXP2(sa[6]); EXP2(sa[7]);
        pa = (sa[0] + sa[1]) + (sa[2] + sa[3]);
        SBAR();
        o[1] = MFMA32(vg[5], r1, o[1]);
        KG(5);
        pa += (sa[4] + sa[5]) + (sa[6] + sa[7]);
        SBAR();
        o[2] = MFMA32(vg[6], r1, o[2]);
        KG(6); KG(7);
        SBAR();
        o[3] = MFMA32(vg[7], r1, o[3]);
#undef KF
#undef KG
        WAIT_VM0();
        BARRIER();
#undef DMAK
#undef DMAV
      };
      typedef std::integral_constant<unsigned, 0> C0;
      typedef std::integral_constant<unsigned, 1> C1;
      int t = 0;
      do {
        tile(C0{}, std::false_type{}, t);
        tile(C1{}, std::false_type{}, t + 1);
        t += 2;
      } while (t < ntile);
    }
    if (pass == 0) { stageK(0, 0); stageK(1, 1); stageV(0, 0); }
    const float inv = 1.f / xhalf_sum(lsum);
    {
      int lz = threadIdx.x;
      asm volatile("" : "+v"(lz));
      const int lane = lz & 63, r = lz & 31, h = (lz >> 5) & 1;
      const unsigned sb_ = STG + wid * 4096;
      u16* op = ctx + ((size_t)head * S_ + qb * 128 + wid * 32 + (lane >> 3)) * DHB + (lane & 7) * 8;
#pragma unroll
      for (int hf = 0; hf < 2; ++hf) {
#pragma unroll
        for (int d2 = 0; d2 < 2; ++d2)
#pragma unroll
          for (int g = 0; g < 4; ++g) {
            const f32x16& x = o[2 * hf + d2];
            u32x2 v = {cvtpk(x[4 * g] * inv, x[4 * g + 1] * inv), cvtpk(x[4 * g + 2] * inv, x[4 * g + 3] * inv)};
            *(lds_u32x2*)(uintptr_t)(sb_ + r * 128 + (((4 * d2 + g) ^ (r & 7)) * 16) + h * 8) = v;
          }
#pragma unroll
        for (int it = 0; it < 4; ++it) {
          const int rr = it * 8 + (lane >> 3);
          const bf16x8 v = lds_read128(sb_ + rr * 128 + (((lane & 7) ^ (rr & 7)) * 16));
          *(bf16x8*)(op + (size_t)it * 8 * DHB + hf * 64) = v;
        }
      }
    }
    if (pass == 0) loadQ(jitem);
  }
#undef EXP2
#undef VRD
}

DI void attnA_block(const u16* __restrict__ qkv, u16* __restrict__ oA, int head, int qb) {
  const int tid = threadIdx.x, lane = tid & 63, wid = __builtin_amdgcn_readfirstlane(tid >> 6);
  const int r = lane & 31, h = lane >> 5;
  const int q0 = qb * 256 + wid * 64;
  constexpr unsigned KBUF = 0, VBUF = 16384, TB = 8192;
  const u16* Qh = qkv + (size_t)(0 * HA + head) * S_ * 32;
  const u16* Kh = qkv + (size_t)(1 * HA + head) * S_ * 32;
  const u16* Vh = qkv + (size_t)(2 * HA + head) * S_ * 32;
  unsigned koff, voff;
  {
    const int row = 32 * wid + (lane >> 2), c = lane & 3;
    koff = (unsigned)(row * 64 + ((c ^ ((row >> 2) & 3)) * 16));
    voff = (unsigned)(row * 64 + c * 16);
  }
  auto stage = [&](int buf, int t) {
    dma_base(KBUF + buf * TB + wid * 2048);
    dma_piece<0>(Kh + (size_t)t * 128 * 32, koff); dma_piece<1024>(Kh + (size_t)t * 128 * 32, koff);
    dma_base(VBUF + buf * TB + wid * 2048);
    dma_piece<0>(Vh + (size_t)t * 128 * 32, voff); dma_piece<1024>(Vh + (size_t)t * 128 * 32, voff);
  };
  stage(0, 0);
  bf16x8 qf[2][2];
#pragma unroll
  for (int qt = 0; qt < 2; ++qt)
#pragma unroll
    for (int ks = 0; ks < 2; ++ks) qf[qt][ks] = *(const bf16x8*)(Qh + (size_t)(q0 + 32 * qt + r) * 32 + ks * 16 + h * 8);
  unsigned ka[2], va;
#pragma unroll
  for (int ks = 0; ks < 2; ++ks) {
    ka[ks] = KBUF + r * 64 + (((2 * ks + h) ^ ((r >> 2) & 3)) * 16);
    asm volatile("" : "+v"(ka[ks]));
  }
  {
    const int q = (lane & 15) >> 2, p = lane & 3, cb = (lane >> 4) & 1;
    va = VBUF + (4 * h + q) * 64 + cb * 32 + p * 8;
  }
  f32x16 o[2], negm[2];
  float mcur[2];
#pragma unroll
  for (int qt = 0; qt < 2; ++qt) { o[qt] = f32x16{}; negm[qt] = f32x16{}; mcur[qt] = 0.f; }
#pragma unroll
  for (int qt = 0; qt < 2; ++qt) { asm volatile("" : "+v"(qf[qt][0])); asm volatile("" : "+v"(qf[qt][1])); }
  WAIT_VM0();
  BARRIER();
  constexpr int NKT = S_ / 128;
#define EXP2(x) x = __builtin_amdgcn_exp2f(x)
#define EXP4(s, i) EXP2(s[i]); EXP2(s[i + 1]); EXP2(s[i + 2]); EXP2(s[i + 3])
#define VRDA(dst, off) dst = cat8(lds_read_tr(off), lds_read_tr((off) + 512))
  auto slow_tile = [&](bool first) {
#pragma unroll 1
    for (int kb = 0; kb < 4; ++kb) {
      const bf16x8 k0 = lds_read128(ka[0] + kb * 2048), k1 = lds_read128(ka[1] + kb * 2048);
      bf16x8 v0, v1;
      VRDA(v0, va + kb * 2048); VRDA(v1, va + kb * 2048 + 1024);
#pragma unroll
      for (int qt = 0; qt < 2; ++qt) {
        f32x16 st = MFMA32(k0, qf[qt][0], f32x16{});
        st = MFMA32(k1, qf[qt][1], st);
        const float tm = xhalf_max(max16(st));
        const float mnew = (first && kb == 0) ? tm : fmaxf(tm, mcur[qt]);
        const float f = __builtin_amdgcn_exp2f(-fmaxf(mnew - mcur[qt], 0.f));
        mcur[qt] = mnew;
#pragma unroll
        for (int i = 0; i < 16; ++i) { st[i] = __builtin_amdgcn_exp2f(st[i] - mnew); o[qt][i] *= f; }
        o[qt] = MFMA32(v0, pack8(st, 0), o[qt]);
        o[qt] = MFMA32(v1, pack8(st, 1), o[qt]);
      }
    }
    negm[0] = splat16(-mcur[0]);
    negm[1] = splat16(-mcur[1]);
  };
  for (int t = 0; t < NKT; ++t) {
    if (t + 1 < NKT) stage((t + 1) & 1, t + 1);
    const f32x16 os0 = o[0], os1 = o[1];
    f32x16 sa0, sa1, sb0, sb1;
    bf16x8 k0, k1, v0, v1;
    k0 = lds_read128(ka[0]); k1 = lds_read128(ka[1]);
    sa0 = MFMA32(k0, qf[0][0], negm[0]); sa1 = MFMA32(k0, qf[1][0], negm[1]);
    sa0 = MFMA32(k1, qf[0][1], sa0); sa1 = MFMA32(k1, qf[1][1], sa1);
    k0 = lds_read128(ka[0] + 2048); k1 = lds_read128(ka[1] + 2048);
#define UNIT(C0, C1, N0, N1, kb)                                                                   \
    do {                                                                                             \
      VRDA(v0, va + (kb) * 2048); VRDA(v1, va + (kb) * 2048 + 1024);                                 \
      SBAR();                                                                                        \
      if ((kb) < 3) N0 = MFMA32(k0, qf[0][0], negm[0]);                                              \
      EXP4(C0, 0);                                                                                   \
      SBAR();                                                                                        \
      if ((kb) < 3) N1 = MFMA32(k0, qf[1][0], negm[1]);                                              \
      EXP4(C0, 4);                                                                                   \
      SBAR();                                                                                        \
      if ((kb) < 3) N0 = MFMA32(k1, qf[0][1], N0);                                                   \
      EXP4(C0, 8);                                                                                   \
      const bf16x8 p00 = pack8(C0, 0);                                                               \
      SBAR();                                                                                        \
      if ((kb) < 3) N1 = MFMA32(k1, qf[1][1], N1);                                                   \
      EXP4(C0, 12);                                                                                  \
      if ((kb) < 2) { k0 = lds_read128(ka[0] + ((kb) + 2) * 2048); k1 = lds_read128(ka[1] + ((kb) + 2) * 2048); } \
      SBAR();                                                                                        \
      o[0] = MFMA32(v0, p00, o[0]);                                                                  \
      EXP4(C1, 0);                                                                                   \
      const bf16x8 p01 = pack8(C0, 1);                                                               \
      SBAR();                                                                                        \
      o[0] = MFMA32(v1, p01, o[0]);                                                                  \
      EXP4(C1, 4); EXP4(C1, 8);                                                                      \
      const bf16x8 p10 = pack8(C1, 0);                                                               \
      SBAR();                                                                                        \
      o[1] = MFMA32(v0, p10, o[1]);                                                                  \
      EXP4(C1, 12);                                                                                  \
      const bf16x8 p11 = pack8(C1, 1);                                                               \
      SBAR();                                                                                        \
      o[1] = MFMA32(v1, p11, o[1]);                                                                  \
    } while (0)
    UNIT(sa0, sa1, sb0, sb1, 0);
    UNIT(sb0, sb1, sa0, sa1, 1);
    UNIT(sa0, sa1, sb0, sb1, 2);
    UNIT(sb0, sb1, sa0, sa1, 3);
#undef UNIT
    const float lim = t == 0 ? -1.f : 1048576.f;
    const bool bad = h && !((o[0][8] - os0[8] <= lim) && (o[1][8] - os1[8] <= lim));
    if (__any(bad)) {
      o[0] = os0; o[1] = os1;
      slow_tile(t == 0);
    }
    ka[0] ^= TB; ka[1] ^= TB; va ^= TB;
    WAIT_VM0();
    BARRIER();
  }
#undef EXP2
#undef EXP4
#undef VRDA
#pragma unroll
  for (int qt = 0; qt < 2; ++qt) {
    const float l = __shfl(o[qt][8], r + 32);
    const float inv = 1.f / l;
    u16* op = oA + (size_t)(q0 + 32 * qt + r) * DM + head * DHA + 4 * h;
#pragma unroll
    for (int g = 0; g < 3; ++g) {
      if (g < 2 || h == 0) {
        u32x2 v = {cvtpk(o[qt][4 * g] * inv, o[qt][4 * g + 1] * inv), cvtpk(o[qt][4 * g + 2] * inv, o[qt][4 * g + 3] * inv)};
        *(u32x2*)(op + 8 * g) = v;
      }
    }
  }
}

constexpr int AQT = 2;
constexpr int NBLK_B = HB * 8, NBLK_A = HA * (S_ / (128 * AQT));
__global__ __launch_bounds__(256, 2) void k_attn(const u16* __restrict__ qB, const u16* __restrict__ kB,
                                                 const u16* __restrict__ vB, u16* __restrict__ ctx,
                                                 const u16* __restrict__ qkvA, u16* __restrict__ oA,
                                                 const u16* __restrict__ Awo, const u16* __restrict__ Bwo) {
  const int b = blockIdx.x;
  if (b < NBLK_B) {
    const int head = (b & 7) + 8 * (b >> 6), j = (b >> 3) & 7;
    attnB_item(qB, kB, vB, ctx, head, j);
  } else {
    const int a = b - NBLK_B;
    constexpr int PER = S_ / (128 * AQT);
    const int head = (a & 7) + 8 * (a / (8 * PER)), qb = (a >> 3) % PER;
    u32x4 pf0 = {0u, 0u, 0u, 0u};
    const int line = a * 256 + threadIdx.x;
    if (line < 47360) {
      const u16* pl = line < 40960 ? Bwo + (size_t)line * 64 : Awo + (size_t)(line - 40960) * 64;
      asm volatile("global_load_dwordx4 %0, %1, off" : "+v"(pf0) : "v"(pl) : "memory");
    }
    attnA_block(qkvA, oA, head, qb);
    asm volatile("" :: "v"(pf0));
  }
}

extern "C" void kernel_launch(void* const* d_in, const int* in_sizes, int n_in, void* d_out, int out_size, void* d_ws,
                              size_t ws_size, hipStream_t stream) {
  const u16* Ax = (const u16*)d_in[0];
  const u16* Awqkv = (const u16*)d_in[1];
  const u16* Awo = (const u16*)d_in[2];
  const u16* Bx = (const u16*)d_in[3];
  const u16* Bckv = (const u16*)d_in[4];
  const u16* Bwq = (const u16*)d_in[5];
  const u16* Bwuk = (const u16*)d_in[6];
  const u16* Bwuv = (const u16*)d_in[7];
  const u16* Bwo = (const u16*)d_in[8];
  u16* out = (u16*)d_out;
  char* ws = (char*)d_ws;
  u16* qkvA = (u16*)ws;                 ws += (size_t)3 * HA * S_ * 32 * 2;
  u16* qB = (u16*)ws;                   ws += (size_t)S_ * NB * 2;
  u16* kB = (u16*)ws;                   ws += (size_t)S_ * NB * 2;
  u16* vB = (u16*)ws;                   ws += (size_t)S_ * NB * 2;
  u16* ctx = (u16*)ws;                  ws += (size_t)S_ * NB * 2;
  u16* oA = (u16*)ws;                   ws += (size_t)S_ * DM * 2;
  float* part = (float*)ws;             ws += (size_t)SPLITK * S_ * DM * 4;

  (void)hipFuncSetAttribute((const void*)k_proj, hipFuncAttributeMaxDynamicSharedMemorySize, G1::LDS);
  (void)hipFuncSetAttribute((const void*)k_out, hipFuncAttributeMaxDynamicSharedMemorySize, G2::LDS);
  (void)hipFuncSetAttribute((const void*)k_attn, hipFuncAttributeMaxDynamicSharedMemorySize, 81920);
  k_proj<<<448, 512, G1::LDS, stream>>>(Ax, Awqkv, Bx, Bckv, Bwq, Bwuk, Bwuv, qkvA, qB, kB, vB);
  k_attn<<<NBLK_B + NBLK_A, 256, 81920, stream>>>(qB, kB, vB, ctx, qkvA, oA, Awo, Bwo);
  k_out<<<40 * SPLITK + 40, 512, G2::LDS, stream>>>(oA, Awo, ctx, Bwo, out, part);
  k_reduce<<<S_ * DM / 4 / 256, 256, 0, stream>>>(part, out);
}
```

```cpp
#include <hip/hip_runtime.h>
#include <hip/hip_bf16.h>
#include <stdint.h>
#include <type_traits>

constexpr int S_ = 2048;
constexpr int DM = 640;
constexpr int HA = 32, DHA = 20;
constexpr int HB = 32, DHB = 128, DC = 512;
constexpr int NB = HB * DHB;

typedef unsigned short u16;
typedef short bf16x8 __attribute__((ext_vector_type(8)));
typedef short s16x4 __attribute__((ext_vector_type(4)));
typedef float f32x16 __attribute__((ext_vector_type(16)));
typedef float f32x4 __attribute__((ext_vector_type(4)));
typedef float f32x2 __attribute__((ext_vector_type(2)));
typedef __bf16 bf16x2 __attribute__((ext_vector_type(2)));
typedef unsigned u32x2 __attribute__((ext_vector_type(2)));
typedef unsigned u32x4 __attribute__((ext_vector_type(4)));
typedef __attribute__((address_space(3))) s16x4 lds_s16x4;
typedef __attribute__((address_space(3))) bf16x8 lds_bf16x8;
typedef __attribute__((address_space(3))) u32x2 lds_u32x2;

#define DI __device__ __forceinline__
#define MFMA32(a, b, c) __builtin_amdgcn_mfma_f32_32x32x16_bf16((a), (b), (c), 0, 0, 0)

DI unsigned cvtpk(float lo, float hi) {
  f32x2 v = {lo, hi};
  bf16x2 b = __builtin_convertvector(v, bf16x2);
  return __builtin_bit_cast(unsigned, b);
}
DI void dma16(const void* sbase, unsigned voff, unsigned lds_addr) {
  unsigned keep;
  asm volatile("s_mov_b32 %0, m0\n\ts_mov_b32 m0, %3\n\ts_nop 0\n\tglobal_load_lds_dwordx4 %1, %2\n\ts_mov_b32 m0, %0"
               : "=&s"(keep) : "v"(voff), "s"(sbase), "s"(lds_addr) : "memory");
}
DI void dma_base(unsigned lds_addr) { asm volatile("s_mov_b32 m0, %0\n\ts_nop 0" :: "s"(lds_addr) : "memory"); }
template <int OFF> DI void dma_piece(const void* sbase, unsigned voff) {
  asm volatile("global_load_lds_dwordx4 %0, %1 offset:%2" :: "v"(voff), "s"(sbase), "i"(OFF) : "memory");
}
template <int OFF, int X> DI void dma_piece_x(const void* sbase, unsigned voff) {
  unsigned t;
  asm volatile("v_xor_b32 %0, %4, %1\n\tglobal_load_lds_dwordx4 %0, %2 offset:%3" : "=&v"(t) : "v"(voff), "s"(sbase), "i"(OFF), "i"(X) : "memory");
}
#define WAIT_VM0() asm volatile("s_waitcnt vmcnt(0)" ::: "memory")
#define BARRIER() do { asm volatile("s_waitcnt lgkmcnt(0)" ::: "memory"); __builtin_amdgcn_s_barrier(); asm volatile("" ::: "memory"); } while (0)

DI bf16x8 lds_read128(unsigned off) { return *(const lds_bf16x8*)(uintptr_t)off; }
DI s16x4 lds_read_tr(unsigned off) { return __builtin_amdgcn_ds_read_tr16_b64_v4i16((lds_s16x4*)(uintptr_t)off); }
DI bf16x8 cat8(s16x4 a, s16x4 b) { return __builtin_shufflevector(a, b, 0, 1, 2, 3, 4, 5, 6, 7); }
DI float xhalf_max(float x) {
  auto r = __builtin_amdgcn_permlane32_swap(__float_as_uint(x), __float_as_uint(x), false, false);
  return fmaxf(__uint_as_float(r[0]), __uint_as_float(r[1]));
}
DI float xhalf_sum(float x) {
  auto r = __builtin_amdgcn_permlane32_swap(__float_as_uint(x), __float_as_uint(x), false, false);
  return __uint_as_float(r[0]) + __uint_as_float(r[1]);
}

template <int BM, int BN, int WM, int WN, int NST_>
struct GCfg {
  static constexpr int NST = NST_;
  static constexpr int NW = WM * WN, TM = BM / WM, TN = BN / WN, MT = TM / 32, NT = TN / 32;
  static constexpr int XB = BM * 128, WROW = BN * 2, WB = 64 * WROW, STAGE = XB + WB;
  static constexpr int XI = BM / 8 / NW;
  static constexpr int WI = WB / 1024 / NW;
  static constexpr int WCH = BN / 8;
  static constexpr int WRPI = 64 / WCH;
  static constexpr int LDS = NST == 23 ? 2 * XB + 3 * WB : NST * STAGE;
};

template <class C, class Epi, bool XHEAD = false>
DI void gemm_tile(const u16* __restrict__ X, int ldx, const u16* __restrict__ W, int ldw, int m0, int n0, int ncols,
                  int kbeg, int nk, const Epi& epi) {
  const int tid = threadIdx.x, lane = tid & 63, wid = __builtin_amdgcn_readfirstlane(tid >> 6);
  const int wm = wid / C::WN_, wn = wid % C::WN_;
  const int r = lane & 31, h = lane >> 5;
  unsigned xoff[C::XI], woff[C::WI];
#pragma unroll
  for (int i = 0; i < C::XI; ++i) {
    const int row = 8 * (wid * C::XI + i) + (lane >> 3);
    xoff[i] = (unsigned)(row * ldx + (((lane & 7) ^ ((row >> 1) & 7)) * 8)) * 2u;
  }
#pragma unroll
  for (int i = 0; i < C::WI; ++i) {
    const int k = C::WRPI * (wid * C::WI + i) + lane / C::WCH;
    int col = (((lane % C::WCH) ^ ((k & 3) << 2)) * 8);
    if (n0 + col > ncols - 8) col = ncols - 8 - n0;
    woff[i] = (unsigned)(k * ldw + col) * 2u;
  }
  const u16* xs = XHEAD ? X + (size_t)m0 * ldx : X + (size_t)m0 * ldx + kbeg;
  const int kt0 = kbeg >> 6;
  auto xk = [&](int kt) { return XHEAD ? xs + (size_t)((kt + kt0) >> 1) * S_ * DHB + ((kt + kt0) & 1) * 64 : xs + kt * 64; };
  const u16* ws = W + (size_t)kbeg * ldw + n0;
  auto stage = [&](int buf, int kt) {
    const u16* xb = xk(kt);
    const u16* wb = ws + (size_t)kt * 64 * ldw;
    const unsigned lb = buf * C::STAGE;
#pragma unroll
    for (int i = 0; i < C::XI; ++i) dma16(xb, xoff[i], lb + (wid * C::XI + i) * 1024);
#pragma unroll
    for (int i = 0; i < C::WI; ++i) dma16(wb, woff[i], lb + C::XB + (wid * C::WI + i) * 1024);
  };
  unsigned xa[4];
#pragma unroll
  for (int ks = 0; ks < 4; ++ks) xa[ks] = (wm * C::TM + r) * 128 + (((2 * ks + h) ^ ((r >> 1) & 7)) * 16);
  unsigned wa[C::NT];
  {
    const int q = (lane & 15) >> 2, p = lane & 3, cb = (lane >> 4) & 1;
#pragma unroll
    for (int nt = 0; nt < C::NT; ++nt) {
      const int nb = wn * C::NT + nt;
      wa[nt] = C::XB + (8 * h + q) * C::WROW + ((nb ^ q) * 64) + cb * 32 + p * 8;
    }
  }
  f32x16 acc[C::NT][C::MT];
#pragma unroll
  for (int nt = 0; nt < C::NT; ++nt)
#pragma unroll
    for (int mt = 0; mt < C::MT; ++mt) acc[nt][mt] = f32x16{};

  if constexpr (C::NST == 23) {
    auto stX = [&](int kt, int i) { dma16(xk(kt), xoff[i], (kt & 1) * C::XB + (wid * C::XI + i) * 1024); };
    auto stW = [&](int kt, int slot, int i) { dma16(ws + (size_t)kt * 64 * ldw, woff[i], 2 * C::XB + slot * C::WB + (wid * C::WI + i) * 1024); };
#pragma unroll
    for (int i = 0; i < C::XI; ++i) stX(0, i);
#pragma unroll
    for (int i = 0; i < C::WI; ++i) stW(0, 0, i);
#pragma unroll
    for (int i = 0; i < C::WI; ++i) stW(1, 1, i);
    u32x4 pfd = {0u, 0u, 0u, 0u};
    {
      const int rows = nk * 8, r0 = ((m0 >> 8) & 7) * rows;
      int col = (tid & 31) * 8;
      if (n0 + col > ncols - 8) col = ncols - 8 - n0;
      const u16* pw = ws + (size_t)(r0 + (tid >> 5)) * ldw + col;
#pragma unroll
      for (int i = 0; i < 5; ++i)
        asm volatile("global_load_dwordx4 %0, %1, off" : "+v"(pfd) : "v"(pw + (size_t)min(16 * i, rows - 16) * ldw) : "memory");
    }
    asm volatile("s_waitcnt vmcnt(%0)" :: "i"(C::WI + 5) : "memory");
    BARRIER();
    int wc = 0, wnx = 2;
    for (int kt = 0; kt < nk; ++kt) {
      const unsigned lx = (kt & 1) * C::XB, lw = C::XB + wc * C::WB;
      const bool moreX = kt + 1 < nk, moreW = kt + 2 < nk;
#pragma unroll
      for (int ks = 0; ks < 4; ++ks) {
        bf16x8 xf[C::MT], wf[C::NT];
#pragma unroll
        for (int mt = 0; mt < C::MT; ++mt) xf[mt] = lds_read128(lx + xa[ks] + mt * 4096);
#pragma unroll
        for (int nt = 0; nt < C::NT; ++nt)
          wf[nt] = cat8(lds_read_tr(lw + wa[nt] + (16 * ks) * C::WROW), lds_read_tr(lw + wa[nt] + (16 * ks + 4) * C::WROW));
        if (ks < 2) { if (moreX) { stX(kt + 1, 2 * ks); stX(kt + 1, 2 * ks + 1); } }
        else { if (moreW) { stW(kt + 2, wnx, 2 * (ks - 2)); stW(kt + 2, wnx, 2 * (ks - 2) + 1); } }
#pragma unroll
        for (int nt = 0; nt < C::NT; ++nt)
#pragma unroll
          for (int mt = 0; mt < C::MT; ++mt) acc[nt][mt] = MFMA32(wf[nt], xf[mt], acc[nt][mt]);
      }
      if (moreW) asm volatile("s_waitcnt vmcnt(%0)" :: "i"(C::WI) : "memory");
      else WAIT_VM0();
      BARRIER();
      wc = wc == 2 ? 0 : wc + 1;
      wnx = wnx == 2 ? 0 : wnx + 1;
    }
    asm volatile("" :: "v"(pfd));
  } else {
  constexpr int AHEAD = C::NST - 1, PER = C::XI + C::WI;
  stage(0, 0);
  u32x4 pfd = {0u, 0u, 0u, 0u};
  if constexpr (Epi::PREFW) {
    const int rows = nk * 8, r0 = ((m0 >> 8) & 7) * rows;
    int col = (tid & 31) * 8;
    if (n0 + col > ncols - 8) col = ncols - 8 - n0;
    const u16* pw = ws + (size_t)(r0 + (tid >> 5)) * ldw + col;
    for (int i = 0; i < rows; i += 16)
      asm volatile("global_load_dwordx4 %0, %1, off" : "+v"(pfd) : "v"(pw + (size_t)i * ldw) : "memory");
  }
  if constexpr (AHEAD == 2) { stage(1, 1); asm volatile("s_waitcnt vmcnt(%0)" :: "i"(PER) : "memory"); }
  else WAIT_VM0();
  asm volatile("" :: "v"(pfd));
  BARRIER();
  int cur = 0, nxt = AHEAD;
  for (int kt = 0; kt < nk; ++kt) {
    const unsigned lb = cur * C::STAGE;
    const bool more = kt + AHEAD < nk;
    const u16* xb = xk(kt + AHEAD);
    const u16* wb = ws + (size_t)(kt + AHEAD) * 64 * ldw;
    const unsigned nb = nxt * C::STAGE;
    cur = cur + 1 == C::NST ? 0 : cur + 1;
    nxt = nxt + 1 == C::NST ? 0 : nxt + 1;
#pragma unroll
    for (int ks = 0; ks < 4; ++ks) {
      bf16x8 xf[C::MT], wf[C::NT];
#pragma unroll
      for (int mt = 0; mt < C::MT; ++mt) xf[mt] = lds_read128(lb + xa[ks] + mt * 4096);
#pragma unroll
      for (int nt = 0; nt < C::NT; ++nt)
        wf[nt] = cat8(lds_read_tr(lb + wa[nt] + (16 * ks) * C::WROW), lds_read_tr(lb + wa[nt] + (16 * ks + 4) * C::WROW));
      if (more) {
        constexpr int TOT = C::XI + C::WI;
#pragma unroll
        for (int i = ks * TOT / 4; i < (ks + 1) * TOT / 4; ++i) {
          if (i < C::XI) dma16(xb, xoff[i], nb + (wid * C::XI + i) * 1024);
          else dma16(wb, woff[i - C::XI], nb + C::XB + (wid * C::WI + i - C::XI) * 1024);
        }
      }
#pragma unroll
      for (int nt = 0; nt < C::NT; ++nt)
#pragma unroll
        for (int mt = 0; mt < C::MT; ++mt) acc[nt][mt] = MFMA32(wf[nt], xf[mt], acc[nt][mt]);
    }
    if (AHEAD == 2 && more) asm volatile("s_waitcnt vmcnt(%0)" :: "i"(PER) : "memory");
    else WAIT_VM0();
    BARRIER();
  }
  }
  if constexpr (Epi::DIRECT) {
    f32x4* dst = (f32x4*)epi.out + (size_t)wid * (C::NT * C::MT * 4 * 64) + lane;
#pragma unroll
    for (int nt = 0; nt < C::NT; ++nt)
#pragma unroll
      for (int mt = 0; mt < C::MT; ++mt)
#pragma unroll
        for (int g = 0; g < 4; ++g) {
          f32x4 v = {acc[nt][mt][4 * g], acc[nt][mt][4 * g + 1], acc[nt][mt][4 * g + 2], acc[nt][mt][4 * g + 3]};
          dst[((nt * C::MT + mt) * 4 + g) * 64] = v;
        }
  } else {
    static_assert(C::TN == 64, "staging layout");
    const unsigned sb = wid * (C::TM * 128);
#pragma unroll
    for (int nt = 0; nt < C::NT; ++nt)
#pragma unroll
      for (int g = 0; g < 4; ++g) {
        const int nl = nt * 32 + 8 * g + 4 * h;
        const float sc = epi.scale_of(n0 + wn * C::TN + nl);
        const unsigned a = sb + r * 128 + (((nl >> 3) ^ (r & 7)) * 16) + (nl & 4) * 2;
#pragma unroll
        for (int mt = 0; mt < C::MT; ++mt) {
          u32x2 v = {cvtpk(acc[nt][mt][4 * g] * sc, acc[nt][mt][4 * g + 1] * sc),
                     cvtpk(acc[nt][mt][4 * g + 2] * sc, acc[nt][mt][4 * g + 3] * sc)};
          *(lds_u32x2*)(uintptr_t)(a + mt * 4096) = v;
        }
      }
    epi.template store<C::TM>(sb, m0 + wm * C::TM, n0 + wn * C::TN, lane);
  }
}

template <int BM, int BN, int WM, int WN, int NST>
struct GC : GCfg<BM, BN, WM, WN, NST> { static constexpr int WN_ = WN; };

struct EpiPlain {
  static constexpr bool DIRECT = false, PREFW = false;
  u16* out; int ld; float scale;
  DI float scale_of(int) const { return scale; }
  template <int TM> DI void store(unsigned sb, int mb, int nb, int lane) const {
    const int rr = lane >> 3, c = lane & 7;
    u16* dst = out + (size_t)(mb + rr) * ld + nb + c * 8;
    const unsigned a = sb + rr * 128 + ((c ^ (rr & 7)) * 16);
#pragma unroll
    for (int it = 0; it < TM / 8; ++it) {
      const bf16x8 v = lds_read128(a + it * 1024);
      *(bf16x8*)(dst + (size_t)it * 8 * ld) = v;
    }
  }
};
struct EpiHead {
  static constexpr bool DIRECT = false, PREFW = true;
  u16* out; float scale;
  DI float scale_of(int) const { return scale; }
  template <int TM> DI void store(unsigned sb, int mb, int nb, int lane) const {
    const int rr = lane >> 3, c = lane & 7;
    u16* dst = out + ((size_t)(nb >> 7) * S_ + mb + rr) * DHB + (nb & 127) + c * 8;
    const unsigned a = sb + rr * 128 + ((c ^ (rr & 7)) * 16);
#pragma unroll
    for (int it = 0; it < TM / 8; ++it) {
      const bf16x8 v = lds_read128(a + it * 1024);
      *(bf16x8*)(dst + (size_t)it * 8 * DHB) = v;
    }
  }
};
struct EpiQKVA {
  static constexpr bool DIRECT = false, PREFW = true;
  u16* out; float qscale;
  DI float scale_of(int n) const { return n < DM ? qscale : 1.f; }
  template <int TM> DI void store(unsigned sb, int mb, int nb, int lane) const {
    const int rr = lane >> 4, gq = lane & 15;
    const int n = nb + gq * 4;
    if (n >= 3 * DM) return;
    const int which = n / DM, rem = n - which * DM, head = rem / DHA, dd = rem - head * DHA;
    u16* dst = out + ((size_t)((which * HA + head) * S_ + mb + rr)) * 32;
#pragma unroll 4
    for (int it = 0; it < TM / 4; ++it) {
      const int row = it * 4 + rr;
      const u32x2 v = *(const lds_u32x2*)(uintptr_t)(sb + row * 128 + (((gq >> 1) ^ (row & 7)) * 16) + (gq & 1) * 8);
      u16* d = dst + it * 4 * 32;
      *(u32x2*)(d + dd) = v;
      if (dd == 16) {
        u32x2 z0 = {which == 2 ? 0x3F80u : 0u, 0u};
        u32x4 z1 = {0u, 0u, 0u, 0u};
        *(u32x2*)(d + 20) = z0;
        *(u32x4*)(d + 24) = z1;
      }
    }
  }
};
struct EpiF32 {
  static constexpr bool DIRECT = true, PREFW = false;
  float* out;
};


#define MFMA16(a, b, c) __builtin_amdgcn_mfma_f32_16x16x32_bf16((a), (b), (c), 0, 0, 0)
template <class Epi>
DI void gemm16_tile(const u16* __restrict__ X, int ldx, const u16* __restrict__ W, int ldw, int m0, int n0, int ncols,
                    int nk, const Epi& epi) {
  constexpr unsigned XB = 32768, WB = 32768;
  const int tid = threadIdx.x, lane = tid & 63, wid = __builtin_amdgcn_readfirstlane(tid >> 6);
  const int wm = wid >> 2, wn = wid & 3;
  const int c16 = lane & 15, g = lane >> 4;
  unsigned xoff[4], woff[4];
#pragma unroll
  for (int i = 0; i < 4; ++i) {
    const int row = 8 * (wid * 4 + i) + (lane >> 3);
    xoff[i] = (unsigned)(row * ldx + (((lane & 7) ^ ((row >> 1) & 7)) * 8)) * 2u;
    const int k = 2 * (wid * 4 + i) + (lane >> 5);
    int col = ((lane & 31) ^ ((k & 3) << 2) ^ (((k >> 3) & 1) << 1)) * 8;
    if (n0 + col > ncols - 8) col = ncols - 8 - n0;
    woff[i] = (unsigned)(k * ldw + col) * 2u;
  }
  const u16* xs = X + (size_t)m0 * ldx;
  const u16* ws = W + n0;
  unsigned xa[2], wa[4];
#pragma unroll
  for (int ks = 0; ks < 2; ++ks) xa[ks] = (wm * 128 + c16) * 128 + (((4 * ks + g) ^ ((c16 >> 1) & 7)) * 16);
  {
    const int q = c16 >> 2, p = lane & 3;
#pragma unroll
    for (int nt = 0; nt < 4; ++nt)
      wa[nt] = 2 * XB + (8 * g + q) * 512 + (((2 * (wn * 4 + nt) + (p >> 1)) ^ (q << 2) ^ ((g & 1) << 1)) * 16) + (p & 1) * 8;
  }
  f32x4 acc[4][8];
#pragma unroll
  for (int nt = 0; nt < 4; ++nt)
#pragma unroll
    for (int mt = 0; mt < 8; ++mt) acc[nt][mt] = f32x4{};
  auto stX = [&](int kt, int i) { dma16(xs + kt * 64, xoff[i], (kt & 1) * XB + (wid * 4 + i) * 1024); };
  auto stW = [&](int kt, int slot, int i) { dma16(ws + (size_t)kt * 64 * ldw, woff[i], 2 * XB + slot * WB + (wid * 4 + i) * 1024); };
#pragma unroll
  for (int i = 0; i < 4; ++i) stX(0, i);
#pragma unroll
  for (int i = 0; i < 4; ++i) stW(0, 0, i);
#pragma unroll
  for (int i = 0; i < 4; ++i) stW(1, 1, i);
  u32x4 pfd = {0u, 0u, 0u, 0u};
  {
    const int rows = nk * 8, r0 = ((m0 >> 8) & 7) * rows;
    int col = (tid & 31) * 8;
    if (n0 + col > ncols - 8) col = ncols - 8 - n0;
    const u16* pw = ws + (size_t)(r0 + (tid >> 5)) * ldw + col;
#pragma unroll
    for (int i = 0; i < 5; ++i)
      asm volatile("global_load_dwordx4 %0, %1, off" : "+v"(pfd) : "v"(pw + (size_t)min(16 * i, rows - 16) * ldw) : "memory");
  }
  asm volatile("s_waitcnt vmcnt(9)" ::: "memory");
  BARRIER();
  int wc = 0, wnx = 2;
  for (int kt = 0; kt < nk; ++kt) {
    const unsigned lx = (kt & 1) * XB, lw = wc * WB;
    const bool moreX = kt + 1 < nk, moreW = kt + 2 < nk;
#pragma unroll
    for (int ks = 0; ks < 2; ++ks) {
      bf16x8 xf[8], wf[4];
#pragma unroll
      for (int mt = 0; mt < 8; ++mt) xf[mt] = lds_read128(lx + xa[ks] + mt * 2048);
#pragma unroll
      for (int nt = 0; nt < 4; ++nt)
        wf[nt] = cat8(lds_read_tr(lw + wa[nt] + (32 * ks) * 512), lds_read_tr(lw + wa[nt] + (32 * ks + 4) * 512));
      if (ks == 0) { if (moreX) { stX(kt + 1, 0); stX(kt + 1, 1); stX(kt + 1, 2); stX(kt + 1, 3); } }
      else { if (moreW) { stW(kt + 2, wnx, 0); stW(kt + 2, wnx, 1); stW(kt + 2, wnx, 2); stW(kt + 2, wnx, 3); } }
#pragma unroll
      for (int nt = 0; nt < 4; ++nt)
#pragma unroll
        for (int mt = 0; mt < 8; ++mt) acc[nt][mt] = MFMA16(wf[nt], xf[mt], acc[nt][mt]);
    }
    if (moreW) asm volatile("s_waitcnt vmcnt(4)" ::: "memory");
    else WAIT_VM0();
    BARRIER();
    wc = wc == 2 ? 0 : wc + 1;
    wnx = wnx == 2 ? 0 : wnx + 1;
  }
  asm volatile("" :: "v"(pfd));
  const unsigned sb = wid * 16384;
#pragma unroll
  for (int nt = 0; nt < 4; ++nt) {
    const int nl = nt * 16 + 4 * g;
    const float sc = epi.scale_of(n0 + wn * 64 + nl);
    const unsigned a = sb + c16 * 128 + (((nl >> 3) ^ (c16 & 7)) * 16) + (nl & 4) * 2;
#pragma unroll
    for (int mt = 0; mt < 8; ++mt) {
      u32x2 v = {cvtpk(acc[nt][mt][0] * sc, acc[nt][mt][1] * sc), cvtpk(acc[nt][mt][2] * sc, acc[nt][mt][3] * sc)};
      *(lds_u32x2*)(uintptr_t)(a + mt * 2048) = v;
    }
  }
  epi.template store<128>(sb, m0 + wm * 128, n0 + wn * 64, lane);
}

typedef GC<256, 256, 2, 4, 23> G1;
typedef GC<256, 128, 4, 2, 3> G2;

constexpr float LOG2E = 1.4426950408889634f;

__global__ __launch_bounds__(512, 2) void k_proj(const u16* __restrict__ Ax, const u16* __restrict__ Awqkv,
                                                 const u16* __restrict__ Bx, const u16* __restrict__ Bckv,
                                                 const u16* __restrict__ Bwq, const u16* __restrict__ Bwuk,
                                                 const u16* __restrict__ Bwuv, u16* __restrict__ qkvA,
                                                 u16* __restrict__ qB, u16* __restrict__ kB, u16* __restrict__ vB) {
  const int b = blockIdx.x, xcd = b & 7, idx = b >> 3;
  const int tn = xcd + 8 * (idx >> 3), tm = idx & 7;
  const int m0 = tm * 256;
  if (tn < 8) {
    EpiQKVA e{qkvA, 0.22360679774997896f * LOG2E};
    gemm16_tile(Ax, DM, Awqkv, 3 * DM, m0, tn * 256, 3 * DM, DM / 64, e);
  } else if (tn < 24) {
    EpiHead e{qB, 0.08838834764831845f * LOG2E};
    gemm16_tile(Bx, DM, Bwq, NB, m0, (tn - 8) * 256, NB, DM / 64, e);
  } else {
    const bool isv = tn >= 40;
    EpiHead e{isv ? vB : kB, 1.f};
    gemm16_tile(Bckv, DC, isv ? Bwuv : Bwuk, NB, m0, (tn - (isv ? 40 : 24)) * 256, NB, DC / 64, e);
  }
}

constexpr int SPLITK = 5;
__global__ __launch_bounds__(512, 2) void k_out(const u16* __restrict__ oA, const u16* __restrict__ Awo,
                                                const u16* __restrict__ ctx, const u16* __restrict__ Bwo,
                                                u16* __restrict__ out, float* __restrict__ part) {
  const int b = blockIdx.x;
  if (b < 40 * SPLITK) {
    const int tm = b & 7, rest = b >> 3, tn = rest % 5, sp = rest / 5;
    EpiF32 e{part + ((size_t)sp * 40 + tm * 5 + tn) * (256 * 128)};
    gemm_tile<G2, EpiF32, true>(ctx, DHB, Bwo, DM, tm * 256, tn * 128, DM, sp * 13 * 64, sp == 4 ? 12 : 13, e);
  } else {
    const int t = b - 40 * SPLITK, tm = t & 7, tn = t >> 3;
    EpiPlain e{out, 2 * DM, 1.f};
    gemm_tile<G2>(oA, DM, Awo, DM, tm * 256, tn * 128, DM, 0, DM / 64, e);
  }
}

__global__ __launch_bounds__(256) void k_reduce(const float* __restrict__ part, u16* __restrict__ out) {
  const int i = blockIdx.x * 256 + threadIdx.x;
  const int tile = i >> 13, w = (i >> 10) & 7, j = (i >> 6) & 15, lane = i & 63;
  const int nt = j >> 3, mt = (j >> 2) & 1, g = j & 3;
  const int tm = tile / 5, tn = tile - tm * 5;
  const int m = tm * 256 + (w >> 1) * 64 + mt * 32 + (lane & 31);
  const int n = tn * 128 + (w & 1) * 64 + nt * 32 + 8 * g + 4 * (lane >> 5);
  const f32x4* p = (const f32x4*)part + i;
  f32x4 s = p[0];
#pragma unroll
  for (int sp = 1; sp < SPLITK; ++sp) s += p[(size_t)sp * 40 * 8192];
  u32x2 v = {cvtpk(s[0], s[1]), cvtpk(s[2], s[3])};
  *(u32x2*)(out + (size_t)m * 2 * DM + DM + n) = v;
}

constexpr float THR = 8.f;
DI bf16x8 pack8(const f32x16& x, int s) {
  u32x4 w = {cvtpk(x[8 * s], x[8 * s + 1]), cvtpk(x[8 * s + 2], x[8 * s + 3]), cvtpk(x[8 * s + 4], x[8 * s + 5]),
             cvtpk(x[8 * s + 6], x[8 * s + 7])};
  return __builtin_bit_cast(bf16x8, w);
}
DI f32x16 splat16(float v) {
  f32x16 x;
#pragma unroll
  for (int i = 0; i < 16; ++i) x[i] = v;
  return x;
}
DI float max16(const f32x16& x) {
  float a = fmaxf(fmaxf(x[0], x[1]), x[2]);
#pragma unroll
  for (int i = 3; i < 15; i += 2) a = fmaxf(fmaxf(a, x[i]), x[i + 1]);
  return fmaxf(a, x[15]);
}

#define SBAR() __builtin_amdgcn_sched_barrier(0)
constexpr float BIGSUM = 4096.f;
DI float sum16(const f32x16& x) {
  float a = (x[0] + x[1]) + (x[2] + x[3]), b = (x[4] + x[5]) + (x[6] + x[7]);
  float c = (x[8] + x[9]) + (x[10] + x[11]), d = (x[12] + x[13]) + (x[14] + x[15]);
  return (a + b) + (c + d);
}
DI void exp16(f32x16& x) {
#pragma unroll
  for (int i = 0; i < 16; ++i) x[i] = __builtin_amdgcn_exp2f(x[i]);
}
DI void attnB_item(const u16* __restrict__ Q, const u16* __restrict__ K, const u16* __restrict__ V,
                   u16* __restrict__ ctx, int head, int jitem) {
  const int tid = threadIdx.x, lane = tid & 63, wid = __builtin_amdgcn_readfirstlane(tid >> 6);
  const int r = lane & 31, h = lane >> 5;
  constexpr unsigned KBUF = 0, VBUF = 32768, TB = 16384, STG = 65536;
  unsigned koff, voff;
  {
    const int l4 = lane >> 4, c = lane & 15;
    koff = (unsigned)((16 * wid + l4) * DHB + ((c ^ l4) * 8)) * 2u;
    voff = (unsigned)((16 * wid + l4) * DHB + ((c ^ (l4 << 2)) * 8)) * 2u;
  }
  const u16* Kh = K + (size_t)head * S_ * DHB;
  const u16* Vh = V + (size_t)head * S_ * DHB;
  auto stageK = [&](int buf, int t) {
    const u16* g = Kh + (size_t)t * 64 * DHB;
    dma_base(KBUF + buf * TB + wid * 4096);
    dma_piece<0>(g, koff); dma_piece_x<1024, 64>(g, koff); dma_piece_x<2048, 128>(g, koff); dma_piece_x<3072, 192>(g, koff);
  };
  auto stageV = [&](int buf, int t) {
    const u16* g = Vh + (size_t)t * 64 * DHB;
    dma_base(VBUF + buf * TB + wid * 4096);
    dma_piece<0>(g, voff); dma_piece<1024>(g, voff); dma_piece<2048>(g, voff); dma_piece<3072>(g, voff);
  };
  unsigned ka[8], va[4];
#pragma unroll
  for (int ks = 0; ks < 8; ++ks) {
    ka[ks] = KBUF + r * 256 + (((2 * ks + h) ^ (r & 15)) * 16);
    asm volatile("" : "+v"(ka[ks]));
  }
  {
    const int q = (lane & 15) >> 2, p = lane & 3, cb = (lane >> 4) & 1;
#pragma unroll
    for (int db = 0; db < 4; ++db) va[db] = VBUF + (4 * h + q) * 256 + ((db ^ q) * 64) + cb * 32 + p * 8;
  }
  bf16x8 qf[8];
  f32x16 o[4], negm;
  float mcur, lsum;
  auto loadQ = [&](int qb) {
    int lz = threadIdx.x;
    asm volatile("" : "+v"(lz));
    const u16* qp = Q + ((size_t)head * S_ + qb * 128) * DHB + (unsigned)((wid * 32 + (lz & 31)) * DHB + ((lz >> 5) & 1) * 8);
#pragma unroll
    for (int ks = 0; ks < 8; ++ks) qf[ks] = *(const bf16x8*)(qp + ks * 16);
  };
  stageK(0, 0); stageK(1, 1); stageV(0, 0);
  loadQ(15 - jitem);

  auto qk_abs = [&](unsigned off) {
    bf16x8 kf[8];
#pragma unroll
    for (int ks = 0; ks < 8; ++ks) kf[ks] = lds_read128(off + ka[ks]);
    f32x16 s = MFMA32(kf[0], qf[0], f32x16{});
#pragma unroll
    for (int ks = 1; ks < 8; ++ks) s = MFMA32(kf[ks], qf[ks], s);
    return s;
  };
  auto remax = [&](const f32x16& st, bool first, f32x16& s) {
    const float tm = xhalf_max(max16(st));
    const float mnew = first ? tm : fmaxf(tm, mcur);
    const float delta = mnew - mcur;
    const float f = __builtin_amdgcn_exp2f(-fmaxf(delta, 0.f));
    mcur = mnew;
    negm = splat16(-mnew);
#pragma unroll
    for (int i = 0; i < 16; ++i) s[i] = __builtin_amdgcn_exp2f(st[i] - mnew);
#pragma unroll
    for (int db = 0; db < 4; ++db)
#pragma unroll
      for (int i = 0; i < 16; ++i) o[db][i] *= f;
    lsum *= f;
    return delta;
  };
  auto slow = [&](int blk) {
    int lz = threadIdx.x;
    asm volatile("" : "+v"(lz));
    const u16* kp = Kh + (size_t)blk * 32 * DHB + (unsigned)((lz & 31) * DHB + ((lz >> 5) & 1) * 8);
    f32x16 st = f32x16{};
#pragma unroll
    for (int ks = 0; ks < 8; ++ks) {
      st = MFMA32(*(const bf16x8*)(kp + ks * 16), qf[ks], st);
      asm volatile("" ::: "memory");
      SBAR();
    }
    return st;
  };
  auto pv = [&](unsigned off, const f32x16& p) {
#pragma unroll
    for (int sx = 0; sx < 2; ++sx) {
      const bf16x8 pf = pack8(p, sx);
#pragma unroll
      for (int db = 0; db < 4; ++db) {
        const bf16x8 vf = cat8(lds_read_tr(off + va[db] + (16 * sx) * 256), lds_read_tr(off + va[db] + (16 * sx + 8) * 256));
        o[db] = MFMA32(vf, pf, o[db]);
      }
    }
  };
#define EXP2(x) x = __builtin_amdgcn_exp2f(x)
#define VRD(dst, off) dst = cat8(lds_read_tr(off), lds_read_tr((off) + 2048))

#pragma unroll 1
  for (int pass = 0; pass < 2; ++pass) {
    const int qb = pass ? jitem : 15 - jitem;
    const int ntile = 2 * qb + 2;
    const int kbd = 4 * qb + wid;
    auto maskblk = [&](f32x16& s, int kb) {
      int lz = threadIdx.x;
      asm volatile("" : "+v"(lz));
      const int dq = (kbd - kb) * 32 + (lz & 31) - 4 * ((lz >> 5) & 1);
#pragma unroll
      for (int i = 0; i < 16; ++i)
        if ((i & 3) + 8 * (i >> 2) > dq) s[i] = -__builtin_inff();
    };
    {
      float z = 0.f;
      asm volatile("" : "+v"(z));
#pragma unroll
      for (int db = 0; db < 4; ++db) o[db] = splat16(z);
      lsum = z;
    }
#pragma unroll
    for (int ks = 0; ks < 8; ++ks) asm volatile("" : "+v"(qf[ks]));
    WAIT_VM0();
    BARRIER();
    {
      f32x16 sa, sb;
      bf16x8 kg[8];
      float pa;
      {
        f32x16 st = qk_abs(0);
        if (kbd == 0) maskblk(st, 0);
        const float m = xhalf_max(max16(st));
        mcur = m;
        negm = splat16(-m);
#pragma unroll
        for (int i = 0; i < 8; ++i) sa[i] = __builtin_amdgcn_exp2f(st[i] - m);
#pragma unroll
        for (int i = 8; i < 16; ++i) sa[i] = st[i] - m;
        pa = ((sa[0] + sa[1]) + (sa[2] + sa[3])) + ((sa[4] + sa[5]) + (sa[6] + sa[7]));
#pragma unroll
        for (int ks = 0; ks < 8; ++ks) kg[ks] = lds_read128(ka[ks] + 8192);
      }
      auto tile = [&](auto BUFC, auto LASTC, int t) {
        constexpr unsigned B_ = decltype(BUFC)::value;
        constexpr bool LAST = decltype(LASTC)::value;
        constexpr unsigned vo = B_ * TB, vn = TB - vo;
        constexpr unsigned kn = TB - vo, kd = vo;
        const u16* kbn = Kh + (size_t)min(t + 2, 31) * 64 * DHB;
        const u16* vbn = Vh + (size_t)min(t + 1, 31) * 64 * DHB;
#define DMAK(i) do { if ((i) == 0) { dma_base(KBUF + kd + wid * 4096); dma_piece<0>(kbn, koff); } else dma_piece_x<(i) * 1024, (i) * 64>(kbn, koff); } while (0)
#define DMAV(i) do { if ((i) == 0) dma_base(VBUF + vn + wid * 4096); dma_piece<(i) * 1024>(vbn, voff); } while (0)
        bf16x8 kf[8], vf[8], vg[8];
#define KF(i) kf[i] = lds_read128(kn + ka[i])
#define KG(i) kg[i] = lds_read128(kn + ka[i] + 8192)
        sb = MFMA32(kg[0], qf[0], negm);
        VRD(vf[0], vo + va[0]); DMAV(0);
        EXP2(sa[8]); EXP2(sa[9]);
        SBAR();
        sb = MFMA32(kg[1], qf[1], sb);
        VRD(vf[1], vo + va[1]); DMAV(1);
        EXP2(sa[10]); EXP2(sa[11]);
        SBAR();
        sb = MFMA32(kg[2], qf[2], sb);
        VRD(vf[2], vo + va[2]); DMAV(2);
        EXP2(sa[12]); EXP2(sa[13]);
        float pa2 = (sa[8] + sa[9]) + (sa[10] + sa[11]);
        SBAR();
        sb = MFMA32(kg[3], qf[3], sb);
        VRD(vf[3], vo + va[3]); DMAV(3);
        EXP2(sa[14]); EXP2(sa[15]);
        pa += sa[12] + sa[13];
        SBAR();
        sb = MFMA32(kg[4], qf[4], sb);
        DMAK(0);
        pa2 += sa[14] + sa[15];
        pa += pa2;
        if (__any(!(pa <= BIGSUM))) {
          f32x16 st = slow(2 * t);
          if (2 * t >= kbd) maskblk(st, 2 * t);
          const float delta = remax(st, false, sa);
#pragma unroll
          for (int i = 0; i < 16; ++i) sb[i] -= delta;
          pa = sum16(sa);
        }
        sb = MFMA32(kg[5], qf[5], sb);
        VRD(vf[4], vo + va[0] + 4096); VRD(vf[5], vo + va[1] + 4096);
        const bf16x8 p0 = pack8(sa, 0);
        lsum += pa;
        SBAR();
        sb = MFMA32(kg[6], qf[6], sb);
        VRD(vf[6], vo + va[2] + 4096); DMAK(1);
        const bf16x8 p1 = pack8(sa, 1);
        SBAR();
        sb = MFMA32(kg[7], qf[7], sb);
        VRD(vf[7], vo + va[3] + 4096); DMAK(2);
        SBAR();
        o[0] = MFMA32(vf[0], p0, o[0]);
        KF(0); DMAK(3);
        if (2 * t + 1 >= kbd) maskblk(sb, 2 * t + 1);
        SBAR();
        o[1] = MFMA32(vf[1], p0, o[1]);
        KF(1);
        EXP2(sb[0]); EXP2(sb[1]); EXP2(sb[2]);
        SBAR();
        o[2] = MFMA32(vf[2], p0, o[2]);
        KF(2);
        EXP2(sb[3]); EXP2(sb[4]);
        SBAR();
        o[3] = MFMA32(vf[3], p0, o[3]);
        KF(3);
        EXP2(sb[5]); EXP2(sb[6]);
        float pb = (sb[0] + sb[1]) + (sb[2] + sb[3]);
        SBAR();
        o[0] = MFMA32(vf[4], p1, o[0]);
        KF(4);
        EXP2(sb[7]); EXP2(sb[8]);
        SBAR();
        o[1] = MFMA32(vf[5], p1, o[1]);
        KF(5);
        EXP2(sb[9]); EXP2(sb[10]);
        float pb2 = (sb[4] + sb[5]) + (sb[6] + sb[7]);
        SBAR();
        o[2] = MFMA32(vf[6], p1, o[2]);
        KF(6);
        EXP2(sb[11]); EXP2(sb[12]);
        SBAR();
        o[3] = MFMA32(vf[7], p1, o[3]);
        KF(7);
        EXP2(sb[13]); EXP2(sb[14]);
        pb += (sb[8] + sb[9]) + sb[10];
        SBAR();
        sa = MFMA32(kf[0], qf[0], negm);
        VRD(vg[0], vo + va[0] + 8192);
        EXP2(sb[15]);
        pb2 += (sb[11] + sb[12]) + sb[13];
        SBAR();
        sa = MFMA32(kf[1], qf[1], sa);
        VRD(vg[1], vo + va[1] + 8192);
        pb += sb[14] + sb[15];
        pb += pb2;
        if (__any(!(pb <= BIGSUM))) {
          f32x16 st = slow(2 * t + 1);
          if (2 * t + 1 >= kbd) maskblk(st, 2 * t + 1);
          const float delta = remax(st, false, sb);
#pragma unroll
          for (int i = 0; i < 16; ++i) sa[i] -= delta;
          pb = sum16(sb);
        }
        sa = MFMA32(kf[2], qf[2], sa);
        VRD(vg[2], vo + va[2] + 8192);
        const bf16x8 r0 = pack8(sb, 0);
        lsum += pb;
        SBAR();
        sa = MFMA32(kf[3], qf[3], sa);
        VRD(vg[3], vo + va[3] + 8192);
        const bf16x8 r1 = pack8(sb, 1);
        SBAR();
        sa = MFMA32(kf[4], qf[4], sa);
        VRD(vg[4], vo + va[0] + 12288);
        SBAR();
        sa = MFMA32(kf[5], qf[5], sa);
        VRD(vg[5], vo + va[1] + 12288);
        SBAR();
        sa = MFMA32(kf[6], qf[6], sa);
        VRD(vg[6], vo + va[2] + 12288);
        SBAR();
        sa = MFMA32(kf[7], qf[7], sa);
        VRD(vg[7], vo + va[3] + 12288);
        SBAR();
        o[0] = MFMA32(vg[0], r0, o[0]);
        KG(0);
        if (2 * t + 2 >= kbd) maskblk(sa, 2 * t + 2);
        SBAR();
        o[1] = MFMA32(vg[1], r0, o[1]);
        KG(1); EXP2(sa[0]); EXP2(sa[1]);
        SBAR();
        o[2] = MFMA32(vg[2], r0, o[2]);
        KG(2); EXP2(sa[2]); EXP2(sa[3]);
        SBAR();
        o[3] = MFMA32(vg[3], r0, o[3]);
        KG(3); EXP2(sa[4]); EXP2(sa[5]);
        SBAR();
        o[0] = MFMA32(vg[4], r1, o[0]);
        KG(4); EXP2(sa[6]); EXP2(sa[7]);
        pa = (sa[0] + sa[1]) + (sa[2] + sa[3]);
        SBAR();
        o[1] = MFMA32(vg[5], r1, o[1]);
        KG(5);
        pa += (sa[4] + sa[5]) + (sa[6] + sa[7]);
        SBAR();
        o[2] = MFMA32(vg[6], r1, o[2]);
        KG(6); KG(7);
        SBAR();
        o[3] = MFMA32(vg[7], r1, o[3]);
#undef KF
#undef KG
        WAIT_VM0();
        BARRIER();
#undef DMAK
#undef DMAV
      };
      typedef std::integral_constant<unsigned, 0> C0;
      typedef std::integral_constant<unsigned, 1> C1;
      int t = 0;
      do {
        tile(C0{}, std::false_type{}, t);
        tile(C1{}, std::false_type{}, t + 1);
        t += 2;
      } while (t < ntile);
    }
    if (pass == 0) { stageK(0, 0); stageK(1, 1); stageV(0, 0); }
    const float inv = 1.f / xhalf_sum(lsum);
    {
      int lz = threadIdx.x;
      asm volatile("" : "+v"(lz));
      const int lane = lz & 63, r = lz & 31, h = (lz >> 5) & 1;
      const unsigned sb_ = STG + wid * 4096;
      u16* op = ctx + ((size_t)head * S_ + qb * 128 + wid * 32 + (lane >> 3)) * DHB + (lane & 7) * 8;
#pragma unroll
      for (int hf = 0; hf < 2; ++hf) {
#pragma unroll
        for (int d2 = 0; d2 < 2; ++d2)
#pragma unroll
          for (int g = 0; g < 4; ++g) {
            const f32x16& x = o[2 * hf + d2];
            u32x2 v = {cvtpk(x[4 * g] * inv, x[4 * g + 1] * inv), cvtpk(x[4 * g + 2] * inv, x[4 * g + 3] * inv)};
            *(lds_u32x2*)(uintptr_t)(sb_ + r * 128 + (((4 * d2 + g) ^ (r & 7)) * 16) + h * 8) = v;
          }
#pragma unroll
        for (int it = 0; it < 4; ++it) {
          const int rr = it * 8 + (lane >> 3);
          const bf16x8 v = lds_read128(sb_ + rr * 128 + (((lane & 7) ^ (rr & 7)) * 16));
          *(bf16x8*)(op + (size_t)it * 8 * DHB + hf * 64) = v;
        }
      }
    }
    if (pass == 0) loadQ(jitem);
  }
#undef EXP2
#undef VRD
}

DI void attnA_block(const u16* __restrict__ qkv, u16* __restrict__ oA, int head, int qb) {
  const int tid = threadIdx.x, lane = tid & 63, wid = __builtin_amdgcn_readfirstlane(tid >> 6);
  const int r = lane & 31, h = lane >> 5;
  const int q0 = qb * 256 + wid * 64;
  constexpr unsigned KBUF = 0, VBUF = 16384, TB = 8192;
  const u16* Qh = qkv + (size_t)(0 * HA + head) * S_ * 32;
  const u16* Kh = qkv + (size_t)(1 * HA + head) * S_ * 32;
  const u16* Vh = qkv + (size_t)(2 * HA + head) * S_ * 32;
  unsigned koff, voff;
  {
    const int row = 32 * wid + (lane >> 2), c = lane & 3;
    koff = (unsigned)(row * 64 + ((c ^ ((row >> 2) & 3)) * 16));
    voff = (unsigned)(row * 64 + c * 16);
  }
  auto stage = [&](int buf, int t) {
    dma_base(KBUF + buf * TB + wid * 2048);
    dma_piece<0>(Kh + (size_t)t * 128 * 32, koff); dma_piece<1024>(Kh + (size_t)t * 128 * 32, koff);
    dma_base(VBUF + buf * TB + wid * 2048);
    dma_piece<0>(Vh + (size_t)t * 128 * 32, voff); dma_piece<1024>(Vh + (size_t)t * 128 * 32, voff);
  };
  stage(0, 0);
  bf16x8 qf[2][2];
#pragma unroll
  for (int qt = 0; qt < 2; ++qt)
#pragma unroll
    for (int ks = 0; ks < 2; ++ks) qf[qt][ks] = *(const bf16x8*)(Qh + (size_t)(q0 + 32 * qt + r) * 32 + ks * 16 + h * 8);
  unsigned ka[2], va;
#pragma unroll
  for (int ks = 0; ks < 2; ++ks) {
    ka[ks] = KBUF + r * 64 + (((2 * ks + h) ^ ((r >> 2) & 3)) * 16);
    asm volatile("" : "+v"(ka[ks]));
  }
  {
    const int q = (lane & 15) >> 2, p = lane & 3, cb = (lane >> 4) & 1;
    va = VBUF + (4 * h + q) * 64 + cb * 32 + p * 8;
  }
  f32x16 o[2], negm[2];
  float mcur[2];
#pragma unroll
  for (int qt = 0; qt < 2; ++qt) { o[qt] = f32x16{}; negm[qt] = f32x16{}; mcur[qt] = 0.f; }
#pragma unroll
  for (int qt = 0; qt < 2; ++qt) { asm volatile("" : "+v"(qf[qt][0])); asm volatile("" : "+v"(qf[qt][1])); }
  WAIT_VM0();
  BARRIER();
  constexpr int NKT = S_ / 128;
#define EXP2(x) x = __builtin_amdgcn_exp2f(x)
#define EXP4(s, i) EXP2(s[i]); EXP2(s[i + 1]); EXP2(s[i + 2]); EXP2(s[i + 3])
#define VRDA(dst, off) dst = cat8(lds_read_tr(off), lds_read_tr((off) + 512))
  auto slow_tile = [&](bool first) {
#pragma unroll 1
    for (int kb = 0; kb < 4; ++kb) {
      const bf16x8 k0 = lds_read128(ka[0] + kb * 2048), k1 = lds_read128(ka[1] + kb * 2048);
      bf16x8 v0, v1;
      VRDA(v0, va + kb * 2048); VRDA(v1, va + kb * 2048 + 1024);
#pragma unroll
      for (int qt = 0; qt < 2; ++qt) {
        f32x16 st = MFMA32(k0, qf[qt][0], f32x16{});
        st = MFMA32(k1, qf[qt][1], st);
        const float tm = xhalf_max(max16(st));
        const float mnew = (first && kb == 0) ? tm : fmaxf(tm, mcur[qt]);
        const float f = __builtin_amdgcn_exp2f(-fmaxf(mnew - mcur[qt], 0.f));
        mcur[qt] = mnew;
#pragma unroll
        for (int i = 0; i < 16; ++i) { st[i] = __builtin_amdgcn_exp2f(st[i] - mnew); o[qt][i] *= f; }
        o[qt] = MFMA32(v0, pack8(st, 0), o[qt]);
        o[qt] = MFMA32(v1, pack8(st, 1), o[qt]);
      }
    }
    negm[0] = splat16(-mcur[0]);
    negm[1] = splat16(-mcur[1]);
  };
  for (int t = 0; t < NKT; ++t) {
    if (t + 1 < NKT) stage((t + 1) & 1, t + 1);
    const f32x16 os0 = o[0], os1 = o[1];
    f32x16 sa0, sa1, sb0, sb1;
    bf16x8 k0, k1, v0, v1;
    k0 = lds_read128(ka[0]); k1 = lds_read128(ka[1]);
    sa0 = MFMA32(k0, qf[0][0], negm[0]); sa1 = MFMA32(k0, qf[1][0], negm[1]);
    sa0 = MFMA32(k1, qf[0][1], sa0); sa1 = MFMA32(k1, qf[1][1], sa1);
    k0 = lds_read128(ka[0] + 2048); k1 = lds_read128(ka[1] + 2048);
#define UNIT(C0, C1, N0, N1, kb)                                                                   \
    do {                                                                                             \
      VRDA(v0, va + (kb) * 2048); VRDA(v1, va + (kb) * 2048 + 1024);                                 \
      SBAR();                                                                                        \
      if ((kb) < 3) N0 = MFMA32(k0, qf[0][0], negm[0]);                                              \
      EXP4(C0, 0);                                                                                   \
      SBAR();                                                                                        \
      if ((kb) < 3) N1 = MFMA32(k0, qf[1][0], negm[1]);                                              \
      EXP4(C0, 4);                                                                                   \
      SBAR();                                                                                        \
      if ((kb) < 3) N0 = MFMA32(k1, qf[0][1], N0);                                                   \
      EXP4(C0, 8);                                                                                   \
      const bf16x8 p00 = pack8(C0, 0);                                                               \
      SBAR();                                                                                        \
      if ((kb) < 3) N1 = MFMA32(k1, qf[1][1], N1);                                                   \
      EXP4(C0, 12);                                                                                  \
      if ((kb) < 2) { k0 = lds_read128(ka[0] + ((kb) + 2) * 2048); k1 = lds_read128(ka[1] + ((kb) + 2) * 2048); } \
      SBAR();                                                                                        \
      o[0] = MFMA32(v0, p00, o[0]);                                                                  \
      EXP4(C1, 0);                                                                                   \
      const bf16x8 p01 = pack8(C0, 1);                                                               \
      SBAR();                                                                                        \
      o[0] = MFMA32(v1, p01, o[0]);                                                                  \
      EXP4(C1, 4); EXP4(C1, 8);                                                                      \
      const bf16x8 p10 = pack8(C1, 0);                                                               \
      SBAR();                                                                                        \
      o[1] = MFMA32(v0, p10, o[1]);                                                                  \
      EXP4(C1, 12);                                                                                  \
      const bf16x8 p11 = pack8(C1, 1);                                                               \
      SBAR();                                                                                        \
      o[1] = MFMA32(v1, p11, o[1]);                                                                  \
    } while (0)
    UNIT(sa0, sa1, sb0, sb1, 0);
    UNIT(sb0, sb1, sa0, sa1, 1);
    UNIT(sa0, sa1, sb0, sb1, 2);
    UNIT(sb0, sb1, sa0, sa1, 3);
#undef UNIT
    const float lim = t == 0 ? -1.f : 1048576.f;
    const bool bad = h && !((o[0][8] - os0[8] <= lim) && (o[1][8] - os1[8] <= lim));
    if (__any(bad)) {
      o[0] = os0; o[1] = os1;
      slow_tile(t == 0);
    }
    ka[0] ^= TB; ka[1] ^= TB; va ^= TB;
    WAIT_VM0();
    BARRIER();
  }
#undef EXP2
#undef EXP4
#undef VRDA
#pragma unroll
  for (int qt = 0; qt < 2; ++qt) {
    const float l = __shfl(o[qt][8], r + 32);
    const float inv = 1.f / l;
    u16* op = oA + (size_t)(q0 + 32 * qt + r) * DM + head * DHA + 4 * h;
#pragma unroll
    for (int g = 0; g < 3; ++g) {
      if (g < 2 || h == 0) {
        u32x2 v = {cvtpk(o[qt][4 * g] * inv, o[qt][4 * g + 1] * inv), cvtpk(o[qt][4 * g + 2] * inv, o[qt][4 * g + 3] * inv)};
        *(u32x2*)(op + 8 * g) = v;
      }
    }
  }
}

constexpr int AQT = 2;
constexpr int NBLK_B = HB * 8, NBLK_A = HA * (S_ / (128 * AQT));
__global__ __launch_bounds__(256, 2) void k_attn(const u16* __restrict__ qB, const u16* __restrict__ kB,
                                                 const u16* __restrict__ vB, u16* __restrict__ ctx,
                                                 const u16* __restrict__ qkvA, u16* __restrict__ oA) {
  const int b = blockIdx.x;
  if (b < NBLK_B) {
    const int head = (b & 7) + 8 * (b >> 6), j = (b >> 3) & 7;
    attnB_item(qB, kB, vB, ctx, head, j);
  } else {
    const int a = b - NBLK_B;
    constexpr int PER = S_ / (128 * AQT);
    const int head = (a & 7) + 8 * (a / (8 * PER)), qb = (a >> 3) % PER;
    attnA_block(qkvA, oA, head, qb);
  }
}

extern "C" void kernel_launch(void* const* d_in, const int* in_sizes, int n_in, void* d_out, int out_size, void* d_ws,
                              size_t ws_size, hipStream_t stream) {
  const u16* Ax = (const u16*)d_in[0];
  const u16* Awqkv = (const u16*)d_in[1];
  const u16* Awo = (const u16*)d_in[2];
  const u16* Bx = (const u16*)d_in[3];
  const u16* Bckv = (const u16*)d_in[4];
  const u16* Bwq = (const u16*)d_in[5];
  const u16* Bwuk = (const u16*)d_in[6];
  const u16* Bwuv = (const u16*)d_in[7];
  const u16* Bwo = (const u16*)d_in[8];
  u16* out = (u16*)d_out;
  char* ws = (char*)d_ws;
  u16* qkvA = (u16*)ws;                 ws += (size_t)3 * HA * S_ * 32 * 2;
  u16* qB = (u16*)ws;                   ws += (size_t)S_ * NB * 2;
  u16* kB = (u16*)ws;                   ws += (size_t)S_ * NB * 2;
  u16* vB = (u16*)ws;                   ws += (size_t)S_ * NB * 2;
  u16* ctx = (u16*)ws;                  ws += (size_t)S_ * NB * 2;
  u16* oA = (u16*)ws;                   ws += (size_t)S_ * DM * 2;
  float* part = (float*)ws;             ws += (size_t)SPLITK * S_ * DM * 4;

  (void)hipFuncSetAttribute((const void*)k_proj, hipFuncAttributeMaxDynamicSharedMemorySize, G1::LDS);
  (void)hipFuncSetAttribute((const void*)k_out, hipFuncAttributeMaxDynamicSharedMemorySize, G2::LDS);
  (void)hipFuncSetAttribute((const void*)k_attn, hipFuncAttributeMaxDynamicSharedMemorySize, 81920);
  k_proj<<<448, 512, G1::LDS, stream>>>(Ax, Awqkv, Bx, Bckv, Bwq, Bwuk, Bwuv, qkvA, qB, kB, vB);
  k_attn<<<NBLK_B + NBLK_A, 256, 81920, stream>>>(qB, kB, vB, ctx, qkvA, oA);
  k_out<<<40 * SPLITK + 40, 512, G2::LDS, stream>>>(oA, Awo, ctx, Bwo, out, part);
  k_reduce<<<S_ * DM / 4 / 256, 256, 0, stream>>>(part, out);
}
```

```cpp
#include <hip/hip_runtime.h>
#include <hip/hip_bf16.h>
#include <stdint.h>
#include <type_traits>

constexpr int S_ = 2048;
constexpr int DM = 640;
constexpr int HA = 32, DHA = 20;
constexpr int HB = 32, DHB = 128, DC = 512;
constexpr int NB = HB * DHB;

typedef unsigned short u16;
typedef short bf16x8 __attribute__((ext_vector_type(8)));
typedef short s16x4 __attribute__((ext_vector_type(4)));
typedef float f32x16 __attribute__((ext_vector_type(16)));
typedef float f32x4 __attribute__((ext_vector_type(4)));
typedef float f32x2 __attribute__((ext_vector_type(2)));
typedef __bf16 bf16x2 __attribute__((ext_vector_type(2)));
typedef unsigned u32x2 __attribute__((ext_vector_type(2)));
typedef unsigned u32x4 __attribute__((ext_vector_type(4)));
typedef __attribute__((address_space(3))) s16x4 lds_s16x4;
typedef __attribute__((address_space(3))) bf16x8 lds_bf16x8;
typedef __attribute__((address_space(3))) u32x2 lds_u32x2;

#define DI __device__ __forceinline__
#define MFMA32(a, b, c) __builtin_amdgcn_mfma_f32_32x32x16_bf16((a), (b), (c), 0, 0, 0)

DI unsigned cvtpk(float lo, float hi) {
  f32x2 v = {lo, hi};
  bf16x2 b = __builtin_convertvector(v, bf16x2);
  return __builtin_bit_cast(unsigned, b);
}
DI void dma16(const void* sbase, unsigned voff, unsigned lds_addr) {
  unsigned keep;
  asm volatile("s_mov_b32 %0, m0\n\ts_mov_b32 m0, %3\n\ts_nop 0\n\tglobal_load_lds_dwordx4 %1, %2\n\ts_mov_b32 m0, %0"
               : "=&s"(keep) : "v"(voff), "s"(sbase), "s"(lds_addr) : "memory");
}
DI void dma_base(unsigned lds_addr) { asm volatile("s_mov_b32 m0, %0\n\ts_nop 0" :: "s"(lds_addr) : "memory"); }
template <int OFF> DI void dma_piece(const void* sbase, unsigned voff) {
  asm volatile("global_load_lds_dwordx4 %0, %1 offset:%2" :: "v"(voff), "s"(sbase), "i"(OFF) : "memory");
}
template <int OFF, int X> DI void dma_piece_x(const void* sbase, unsigned voff) {
  unsigned t;
  asm volatile("v_xor_b32 %0, %4, %1\n\tglobal_load_lds_dwordx4 %0, %2 offset:%3" : "=&v"(t) : "v"(voff), "s"(sbase), "i"(OFF), "i"(X) : "memory");
}
#define WAIT_VM0() asm volatile("s_waitcnt vmcnt(0)" ::: "memory")
#define BARRIER() do { asm volatile("s_waitcnt lgkmcnt(0)" ::: "memory"); __builtin_amdgcn_s_barrier(); asm volatile("" ::: "memory"); } while (0)

DI bf16x8 lds_read128(unsigned off) { return *(const lds_bf16x8*)(uintptr_t)off; }
DI s16x4 lds_read_tr(unsigned off) { return __builtin_amdgcn_ds_read_tr16_b64_v4i16((lds_s16x4*)(uintptr_t)off); }
DI bf16x8 cat8(s16x4 a, s16x4 b) { return __builtin_shufflevector(a, b, 0, 1, 2, 3, 4, 5, 6, 7); }
DI float xhalf_max(float x) {
  auto r = __builtin_amdgcn_permlane32_swap(__float_as_uint(x), __float_as_uint(x), false, false);
  return fmaxf(__uint_as_float(r[0]), __uint_as_float(r[1]));
}
DI float xhalf_sum(float x) {
  auto r = __builtin_amdgcn_permlane32_swap(__float_as_uint(x), __float_as_uint(x), false, false);
  return __uint_as_float(r[0]) + __uint_as_float(r[1]);
}

template <int BM, int BN, int WM, int WN, int NST_>
struct GCfg {
  static constexpr int NST = NST_;
  static constexpr int NW = WM * WN, TM = BM / WM, TN = BN / WN, MT = TM / 32, NT = TN / 32;
  static constexpr int XB = BM * 128, WROW = BN * 2, WB = 64 * WROW, STAGE = XB + WB;
  static constexpr int XI = BM / 8 / NW;
  static constexpr int WI = WB / 1024 / NW;
  static constexpr int WCH = BN / 8;
  static constexpr int WRPI = 64 / WCH;
  static constexpr int LDS = NST == 23 ? 2 * XB + 3 * WB : NST * STAGE;
};

template <class C, class Epi, bool XHEAD = false>
DI void gemm_tile(const u16* __restrict__ X, int ldx, const u16* __restrict__ W, int ldw, int m0, int n0, int ncols,
                  int kbeg, int nk, const Epi& epi) {
  const int tid = threadIdx.x, lane = tid & 63, wid = __builtin_amdgcn_readfirstlane(tid >> 6);
  const int wm = wid / C::WN_, wn = wid % C::WN_;
  const int r = lane & 31, h = lane >> 5;
  unsigned xoff[C::XI], woff[C::WI];
#pragma unroll
  for (int i = 0; i < C::XI; ++i) {
    const int row = 8 * (wid * C::XI + i) + (lane >> 3);
    xoff[i] = (unsigned)(row * ldx + (((lane & 7) ^ ((row >> 1) & 7)) * 8)) * 2u;
  }
#pragma unroll
  for (int i = 0; i < C::WI; ++i) {
    const int k = C::WRPI * (wid * C::WI + i) + lane / C::WCH;
    int col = (((lane % C::WCH) ^ ((k & 3) << 2)) * 8);
    if (n0 + col > ncols - 8) col = ncols - 8 - n0;
    woff[i] = (unsigned)(k * ldw + col) * 2u;
  }
  const u16* xs = XHEAD ? X + (size_t)m0 * ldx : X + (size_t)m0 * ldx + kbeg;
  const int kt0 = kbeg >> 6;
  auto xk = [&](int kt) { return XHEAD ? xs + (size_t)((kt + kt0) >> 1) * S_ * DHB + ((kt + kt0) & 1) * 64 : xs + kt * 64; };
  const u16* ws = W + (size_t)kbeg * ldw + n0;
  auto stage = [&](int buf, int kt) {
    const u16* xb = xk(kt);
    const u16* wb = ws + (size_t)kt * 64 * ldw;
    const unsigned lb = buf * C::STAGE;
#pragma unroll
    for (int i = 0; i < C::XI; ++i) dma16(xb, xoff[i], lb + (wid * C::XI + i) * 1024);
#pragma unroll
    for (int i = 0; i < C::WI; ++i) dma16(wb, woff[i], lb + C::XB + (wid * C::WI + i) * 1024);
  };
  unsigned xa[4];
#pragma unroll
  for (int ks = 0; ks < 4; ++ks) xa[ks] = (wm * C::TM + r) * 128 + (((2 * ks + h) ^ ((r >> 1) & 7)) * 16);
  unsigned wa[C::NT];
  {
    const int q = (lane & 15) >> 2, p = lane & 3, cb = (lane >> 4) & 1;
#pragma unroll
    for (int nt = 0; nt < C::NT; ++nt) {
      const int nb = wn * C::NT + nt;
      wa[nt] = C::XB + (8 * h + q) * C::WROW + ((nb ^ q) * 64) + cb * 32 + p * 8;
    }
  }
  f32x16 acc[C::NT][C::MT];
#pragma unroll
  for (int nt = 0; nt < C::NT; ++nt)
#pragma unroll
    for (int mt = 0; mt < C::MT; ++mt) acc[nt][mt] = f32x16{};

  if constexpr (C::NST == 23) {
    auto stX = [&](int kt, int i) { dma16(xk(kt), xoff[i], (kt & 1) * C::XB + (wid * C::XI + i) * 1024); };
    auto stW = [&](int kt, int slot, int i) { dma16(ws + (size_t)kt * 64 * ldw, woff[i], 2 * C::XB + slot * C::WB + (wid * C::WI + i) * 1024); };
#pragma unroll
    for (int i = 0; i < C::XI; ++i) stX(0, i);
#pragma unroll
    for (int i = 0; i < C::WI; ++i) stW(0, 0, i);
#pragma unroll
    for (int i = 0; i < C::WI; ++i) stW(1, 1, i);
    u32x4 pfd = {0u, 0u, 0u, 0u};
    {
      const int rows = nk * 8, r0 = ((m0 >> 8) & 7) * rows;
      int col = (tid & 31) * 8;
      if (n0 + col > ncols - 8) col = ncols - 8 - n0;
      const u16* pw = ws + (size_t)(r0 + (tid >> 5)) * ldw + col;
#pragma unroll
      for (int i = 0; i < 5; ++i)
        asm volatile("global_load_dwordx4 %0, %1, off" : "+v"(pfd) : "v"(pw + (size_t)min(16 * i, rows - 16) * ldw) : "memory");
    }
    asm volatile("s_waitcnt vmcnt(%0)" :: "i"(C::WI + 5) : "memory");
    BARRIER();
    int wc = 0, wnx = 2;
    for (int kt = 0; kt < nk; ++kt) {
      const unsigned lx = (kt & 1) * C::XB, lw = C::XB + wc * C::WB;
      const bool moreX = kt + 1 < nk, moreW = kt + 2 < nk;
#pragma unroll
      for (int ks = 0; ks < 4; ++ks) {
        bf16x8 xf[C::MT], wf[C::NT];
#pragma unroll
        for (int mt = 0; mt < C::MT; ++mt) xf[mt] = lds_read128(lx + xa[ks] + mt * 4096);
#pragma unroll
        for (int nt = 0; nt < C::NT; ++nt)
          wf[nt] = cat8(lds_read_tr(lw + wa[nt] + (16 * ks) * C::WROW), lds_read_tr(lw + wa[nt] + (16 * ks + 4) * C::WROW));
        if (ks < 2) { if (moreX) { stX(kt + 1, 2 * ks); stX(kt + 1, 2 * ks + 1); } }
        else { if (moreW) { stW(kt + 2, wnx, 2 * (ks - 2)); stW(kt + 2, wnx, 2 * (ks - 2) + 1); } }
#pragma unroll
        for (int nt = 0; nt < C::NT; ++nt)
#pragma unroll
          for (int mt = 0; mt < C::MT; ++mt) acc[nt][mt] = MFMA32(wf[nt], xf[mt], acc[nt][mt]);
      }
      if (moreW) asm volatile("s_waitcnt vmcnt(%0)" :: "i"(C::WI) : "memory");
      else WAIT_VM0();
      BARRIER();
      wc = wc == 2 ? 0 : wc + 1;
      wnx = wnx == 2 ? 0 : wnx + 1;
    }
    asm volatile("" :: "v"(pfd));
  } else {
  constexpr int AHEAD = C::NST - 1, PER = C::XI + C::WI;
  stage(0, 0);
  u32x4 pfd = {0u, 0u, 0u, 0u};
  if constexpr (Epi::PREFW) {
    const int rows = nk * 8, r0 = ((m0 >> 8) & 7) * rows;
    int col = (tid & 31) * 8;
    if (n0 + col > ncols - 8) col = ncols - 8 - n0;
    const u16* pw = ws + (size_t)(r0 + (tid >> 5)) * ldw + col;
    for (int i = 0; i < rows; i += 16)
      asm volatile("global_load_dwordx4 %0, %1, off" : "+v"(pfd) : "v"(pw + (size_t)i * ldw) : "memory");
  }
  if constexpr (AHEAD == 2) { stage(1, 1); asm volatile("s_waitcnt vmcnt(%0)" :: "i"(PER) : "memory"); }
  else WAIT_VM0();
  asm volatile("" :: "v"(pfd));
  BARRIER();
  int cur = 0, nxt = AHEAD;
  for (int kt = 0; kt < nk; ++kt) {
    const unsigned lb = cur * C::STAGE;
    const bool more = kt + AHEAD < nk;
    const u16* xb = xk(kt + AHEAD);
    const u16* wb = ws + (size_t)(kt + AHEAD) * 64 * ldw;
    const unsigned nb = nxt * C::STAGE;
    cur = cur + 1 == C::NST ? 0 : cur + 1;
    nxt = nxt + 1 == C::NST ? 0 : nxt + 1;
#pragma unroll
    for (int ks = 0; ks < 4; ++ks) {
      bf16x8 xf[C::MT], wf[C::NT];
#pragma unroll
      for (int mt = 0; mt < C::MT; ++mt) xf[mt] = lds_read128(lb + xa[ks] + mt * 4096);
#pragma unroll
      for (int nt = 0; nt < C::NT; ++nt)
        wf[nt] = cat8(lds_read_tr(lb + wa[nt] + (16 * ks) * C::WROW), lds_read_tr(lb + wa[nt] + (16 * ks + 4) * C::WROW));
      if (more) {
        constexpr int TOT = C::XI + C::WI;
#pragma unroll
        for (int i = ks * TOT / 4; i < (ks + 1) * TOT / 4; ++i) {
          if (i < C::XI) dma16(xb, xoff[i], nb + (wid * C::XI + i) * 1024);
          else dma16(wb, woff[i - C::XI], nb + C::XB + (wid * C::WI + i - C::XI) * 1024);
        }
      }
#pragma unroll
      for (int nt = 0; nt < C::NT; ++nt)
#pragma unroll
        for (int mt = 0; mt < C::MT; ++mt) acc[nt][mt] = MFMA32(wf[nt], xf[mt], acc[nt][mt]);
    }
    if (AHEAD == 2 && more) asm volatile("s_waitcnt vmcnt(%0)" :: "i"(PER) : "memory");
    else WAIT_VM0();
    BARRIER();
  }
  }
  if constexpr (Epi::DIRECT) {
    f32x4* dst = (f32x4*)epi.out + (size_t)wid * (C::NT * C::MT * 4 * 64) + lane;
#pragma unroll
    for (int nt = 0; nt < C::NT; ++nt)
#pragma unroll
      for (int mt = 0; mt < C::MT; ++mt)
#pragma unroll
        for (int g = 0; g < 4; ++g) {
          f32x4 v = {acc[nt][mt][4 * g], acc[nt][mt][4 * g + 1], acc[nt][mt][4 * g + 2], acc[nt][mt][4 * g + 3]};
          dst[((nt * C::MT + mt) * 4 + g) * 64] = v;
        }
  } else {
    static_assert(C::TN == 64, "staging layout");
    const unsigned sb = wid * (C::TM * 128);
#pragma unroll
    for (int nt = 0; nt < C::NT; ++nt)
#pragma unroll
      for (int g = 0; g < 4; ++g) {
        const int nl = nt * 32 + 8 * g + 4 * h;
        const float sc = epi.scale_of(n0 + wn * C::TN + nl);
        const unsigned a = sb + r * 128 + (((nl >> 3) ^ (r & 7)) * 16) + (nl & 4) * 2;
#pragma unroll
        for (int mt = 0; mt < C::MT; ++mt) {
          u32x2 v = {cvtpk(acc[nt][mt][4 * g] * sc, acc[nt][mt][4 * g + 1] * sc),
                     cvtpk(acc[nt][mt][4 * g + 2] * sc, acc[nt][mt][4 * g + 3] * sc)};
          *(lds_u32x2*)(uintptr_t)(a + mt * 4096) = v;
        }
      }
    epi.template store<C::TM>(sb, m0 + wm * C::TM, n0 + wn * C::TN, lane);
  }
}

template <int BM, int BN, int WM, int WN, int NST>
struct GC : GCfg<BM, BN, WM, WN, NST> { static constexpr int WN_ = WN; };

struct EpiPlain {
  static constexpr bool DIRECT = false, PREFW = false;
  u16* out; int ld; float scale;
  DI float scale_of(int) const { return scale; }
  template <int TM> DI void store(unsigned sb, int mb, int nb, int lane) const {
    const int rr = lane >> 3, c = lane & 7;
    u16* dst = out + (size_t)(mb + rr) * ld + nb + c * 8;
    const unsigned a = sb + rr * 128 + ((c ^ (rr & 7)) * 16);
#pragma unroll
    for (int it = 0; it < TM / 8; ++it) {
      const bf16x8 v = lds_read128(a + it * 1024);
      *(bf16x8*)(dst + (size_t)it * 8 * ld) = v;
    }
  }
};
struct EpiHead {
  static constexpr bool DIRECT = false, PREFW = true;
  u16* out; float scale;
  DI float scale_of(int) const { return scale; }
  template <int TM> DI void store(unsigned sb, int mb, int nb, int lane) const {
    const int rr = lane >> 3, c = lane & 7;
    u16* dst = out + ((size_t)(nb >> 7) * S_ + mb + rr) * DHB + (nb & 127) + c * 8;
    const unsigned a = sb + rr * 128 + ((c ^ (rr & 7)) * 16);
#pragma unroll
    for (int it = 0; it < TM / 8; ++it) {
      const bf16x8 v = lds_read128(a + it * 1024);
      *(bf16x8*)(dst + (size_t)it * 8 * DHB) = v;
    }
  }
};
struct EpiQKVA {
  static constexpr bool DIRECT = false, PREFW = true;
  u16* out; float qscale;
  DI float scale_of(int n) const { return n < DM ? qscale : 1.f; }
  template <int TM> DI void store(unsigned sb, int mb, int nb, int lane) const {
    const int rr = lane >> 4, gq = lane & 15;
    const int n = nb + gq * 4;
    if (n >= 3 * DM) return;
    const int which = n / DM, rem = n - which * DM, head = rem / DHA, dd = rem - head * DHA;
    u16* dst = out + ((size_t)((which * HA + head) * S_ + mb + rr)) * 32;
#pragma unroll 4
    for (int it = 0; it < TM / 4; ++it) {
      const int row = it * 4 + rr;
      const u32x2 v = *(const lds_u32x2*)(uintptr_t)(sb + row * 128 + (((gq >> 1) ^ (row & 7)) * 16) + (gq & 1) * 8);
      u16* d = dst + it * 4 * 32;
      *(u32x2*)(d + dd) = v;
      if (dd == 16) {
        u32x2 z0 = {which == 2 ? 0x3F80u : 0u, 0u};
        u32x4 z1 = {0u, 0u, 0u, 0u};
        *(u32x2*)(d + 20) = z0;
        *(u32x4*)(d + 24) = z1;
      }
    }
  }
};
struct EpiF32 {
  static constexpr bool DIRECT = true, PREFW = false;
  float* out;
};

typedef GC<256, 256, 2, 4, 23> G1;
typedef GC<256, 128, 4, 2, 3> G2;

constexpr float LOG2E = 1.4426950408889634f;

__global__ __launch_bounds__(512, 2) void k_proj(const u16* __restrict__ Ax, const u16* __restrict__ Awqkv,
                                                 const u16* __restrict__ Bx, const u16* __restrict__ Bckv,
                                                 const u16* __restrict__ Bwq, const u16* __restrict__ Bwuk,
                                                 const u16* __restrict__ Bwuv, u16* __restrict__ qkvA,
                                                 u16* __restrict__ qB, u16* __restrict__ kB, u16* __restrict__ vB) {
  const int b = blockIdx.x, xcd = b & 7, idx = b >> 3;
  const int tn = xcd + 8 * (idx >> 3), tm = idx & 7;
  const int m0 = tm * 256;
  if (tn < 8) {
    EpiQKVA e{qkvA, 0.22360679774997896f * LOG2E};
    gemm_tile<G1>(Ax, DM, Awqkv, 3 * DM, m0, tn * 256, 3 * DM, 0, DM / 64, e);
  } else if (tn < 24) {
    EpiHead e{qB, 0.08838834764831845f * LOG2E};
    gemm_tile<G1>(Bx, DM, Bwq, NB, m0, (tn - 8) * 256, NB, 0, DM / 64, e);
  } else {
    const bool isv = tn >= 40;
    EpiHead e{isv ? vB : kB, 1.f};
    gemm_tile<G1>(Bckv, DC, isv ? Bwuv : Bwuk, NB, m0, (tn - (isv ? 40 : 24)) * 256, NB, 0, DC / 64, e);
  }
}

constexpr int SPLITK = 5;
__global__ __launch_bounds__(512, 2) void k_out(const u16* __restrict__ oA, const u16* __restrict__ Awo,
                                                const u16* __restrict__ ctx, const u16* __restrict__ Bwo,
                                                u16* __restrict__ out, float* __restrict__ part) {
  const int b = blockIdx.x;
  if (b < 40 * SPLITK) {
    const int tm = b & 7, rest = b >> 3, tn = rest % 5, sp = rest / 5;
    EpiF32 e{part + ((size_t)sp * 40 + tm * 5 + tn) * (256 * 128)};
    gemm_tile<G2, EpiF32, true>(ctx, DHB, Bwo, DM, tm * 256, tn * 128, DM, sp * 13 * 64, sp == 4 ? 12 : 13, e);
  } else {
    const int t = b - 40 * SPLITK, tm = t & 7, tn = t >> 3;
    EpiPlain e{out, 2 * DM, 1.f};
    gemm_tile<G2>(oA, DM, Awo, DM, tm * 256, tn * 128, DM, 0, DM / 64, e);
  }
}

__global__ __launch_bounds__(256) void k_reduce(const float* __restrict__ part, u16* __restrict__ out) {
  const int i = blockIdx.x * 256 + threadIdx.x;
  const int tile = i >> 13, w = (i >> 10) & 7, j = (i >> 6) & 15, lane = i & 63;
  const int nt = j >> 3, mt = (j >> 2) & 1, g = j & 3;
  const int tm = tile / 5, tn = tile - tm * 5;
  const int m = tm * 256 + (w >> 1) * 64 + mt * 32 + (lane & 31);
  const int n = tn * 128 + (w & 1) * 64 + nt * 32 + 8 * g + 4 * (lane >> 5);
  const f32x4* p = (const f32x4*)part + i;
  f32x4 s = p[0];
#pragma unroll
  for (int sp = 1; sp < SPLITK; ++sp) s += p[(size_t)sp * 40 * 8192];
  u32x2 v = {cvtpk(s[0], s[1]), cvtpk(s[2], s[3])};
  *(u32x2*)(out + (size_t)m * 2 * DM + DM + n) = v;
}

constexpr float THR = 8.f;
DI bf16x8 pack8(const f32x16& x, int s) {
  u32x4 w = {cvtpk(x[8 * s], x[8 * s + 1]), cvtpk(x[8 * s + 2], x[8 * s + 3]), cvtpk(x[8 * s + 4], x[8 * s + 5]),
             cvtpk(x[8 * s + 6], x[8 * s + 7])};
  return __builtin_bit_cast(bf16x8, w);
}
DI f32x16 splat16(float v) {
  f32x16 x;
#pragma unroll
  for (int i = 0; i < 16; ++i) x[i] = v;
  return x;
}
DI float max16(const f32x16& x) {
  float a = fmaxf(fmaxf(x[0], x[1]), x[2]);
#pragma unroll
  for (int i = 3; i < 15; i += 2) a = fmaxf(fmaxf(a, x[i]), x[i + 1]);
  return fmaxf(a, x[15]);
}

#define SBAR() __builtin_amdgcn_sched_barrier(0)
constexpr float BIGSUM = 4096.f;
DI float sum16(const f32x16& x) {
  float a = (x[0] + x[1]) + (x[2] + x[3]), b = (x[4] + x[5]) + (x[6] + x[7]);
  float c = (x[8] + x[9]) + (x[10] + x[11]), d = (x[12] + x[13]) + (x[14] + x[15]);
  return (a + b) + (c + d);
}
DI void exp16(f32x16& x) {
#pragma unroll
  for (int i = 0; i < 16; ++i) x[i] = __builtin_amdgcn_exp2f(x[i]);
}
DI void attnB_item(const u16* __restrict__ Q, const u16* __restrict__ K, const u16* __restrict__ V,
                   u16* __restrict__ ctx, int head, int jitem) {
  const int tid = threadIdx.x, lane = tid & 63, wid = __builtin_amdgcn_readfirstlane(tid >> 6);
  const int r = lane & 31, h = lane >> 5;
  constexpr unsigned KBUF = 0, VBUF = 32768, TB = 16384, STG = 65536;
  unsigned koff, voff;
  {
    const int l4 = lane >> 4, c = lane & 15;
    koff = (unsigned)((16 * wid + l4) * DHB + ((c ^ l4) * 8)) * 2u;
    voff = (unsigned)((16 * wid + l4) * DHB + ((c ^ (l4 << 2)) * 8)) * 2u;
  }
  const u16* Kh = K + (size_t)head * S_ * DHB;
  const u16* Vh = V + (size_t)head * S_ * DHB;
  auto stageK = [&](int buf, int t) {
    const u16* g = Kh + (size_t)t * 64 * DHB;
    dma_base(KBUF + buf * TB + wid * 4096);
    dma_piece<0>(g, koff); dma_piece_x<1024, 64>(g, koff); dma_piece_x<2048, 128>(g, koff); dma_piece_x<3072, 192>(g, koff);
  };
  auto stageV = [&](int buf, int t) {
    const u16* g = Vh + (size_t)t * 64 * DHB;
    dma_base(VBUF + buf * TB + wid * 4096);
    dma_piece<0>(g, voff); dma_piece<1024>(g, voff); dma_piece<2048>(g, voff); dma_piece<3072>(g, voff);
  };
  unsigned ka[8], va[4];
#pragma unroll
  for (int ks = 0; ks < 8; ++ks) {
    ka[ks] = KBUF + r * 256 + (((2 * ks + h) ^ (r & 15)) * 16);
    asm volatile("" : "+v"(ka[ks]));
  }
  {
    const int q = (lane & 15) >> 2, p = lane & 3, cb = (lane >> 4) & 1;
#pragma unroll
    for (int db = 0; db < 4; ++db) va[db] = VBUF + (4 * h + q) * 256 + ((db ^ q) * 64) + cb * 32 + p * 8;
  }
  bf16x8 qf[8];
  f32x16 o[4], negm;
  float mcur, lsum;
  auto loadQ = [&](int qb) {
    int lz = threadIdx.x;
    asm volatile("" : "+v"(lz));
    const u16* qp = Q + ((size_t)head * S_ + qb * 128) * DHB + (unsigned)((wid * 32 + (lz & 31)) * DHB + ((lz >> 5) & 1) * 8);
#pragma unroll
    for (int ks = 0; ks < 8; ++ks) qf[ks] = *(const bf16x8*)(qp + ks * 16);
  };
  stageK(0, 0); stageK(1, 1); stageV(0, 0);
  loadQ(15 - jitem);

  auto qk_abs = [&](unsigned off) {
    bf16x8 kf[8];
#pragma unroll
    for (int ks = 0; ks < 8; ++ks) kf[ks] = lds_read128(off + ka[ks]);
    f32x16 s = MFMA32(kf[0], qf[0], f32x16{});
#pragma unroll
    for (int ks = 1; ks < 8; ++ks) s = MFMA32(kf[ks], qf[ks], s);
    return s;
  };
  auto remax = [&](const f32x16& st, bool first, f32x16& s) {
    const float tm = xhalf_max(max16(st));
    const float mnew = first ? tm : fmaxf(tm, mcur);
    const float delta = mnew - mcur;
    const float f = __builtin_amdgcn_exp2f(-fmaxf(delta, 0.f));
    mcur = mnew;
    negm = splat16(-mnew);
#pragma unroll
    for (int i = 0; i < 16; ++i) s[i] = __builtin_amdgcn_exp2f(st[i] - mnew);
#pragma unroll
    for (int db = 0; db < 4; ++db)
#pragma unroll
      for (int i = 0; i < 16; ++i) o[db][i] *= f;
    lsum *= f;
    return delta;
  };
  auto slow = [&](int blk) {
    int lz = threadIdx.x;
    asm volatile("" : "+v"(lz));
    const u16* kp = Kh + (size_t)blk * 32 * DHB + (unsigned)((lz & 31) * DHB + ((lz >> 5) & 1) * 8);
    f32x16 st = f32x16{};
#pragma unroll
    for (int ks = 0; ks < 8; ++ks) {
      st = MFMA32(*(const bf16x8*)(kp + ks * 16), qf[ks], st);
      asm volatile("" ::: "memory");
      SBAR();
    }
    return st;
  };
  auto pv = [&](unsigned off, const f32x16& p) {
#pragma unroll
    for (int sx = 0; sx < 2; ++sx) {
      const bf16x8 pf = pack8(p, sx);
#pragma unroll
      for (int db = 0; db < 4; ++db) {
        const bf16x8 vf = cat8(lds_read_tr(off + va[db] + (16 * sx) * 256), lds_read_tr(off + va[db] + (16 * sx + 8) * 256));
        o[db] = MFMA32(vf, pf, o[db]);
      }
    }
  };
#define EXP2(x) x = __builtin_amdgcn_exp2f(x)
#define VRD(dst, off) dst = cat8(lds_read_tr(off), lds_read_tr((off) + 2048))

#pragma unroll 1
  for (int pass = 0; pass < 2; ++pass) {
    const int qb = pass ? jitem : 15 - jitem;
    const int ntile = 2 * qb + 2;
    const int kbd = 4 * qb + wid;
    auto maskblk = [&](f32x16& s, int kb) {
      int lz = threadIdx.x;
      asm volatile("" : "+v"(lz));
      const int dq = (kbd - kb) * 32 + (lz & 31) - 4 * ((lz >> 5) & 1);
#pragma unroll
      for (int i = 0; i < 16; ++i)
        if ((i & 3) + 8 * (i >> 2) > dq) s[i] = -__builtin_inff();
    };
    {
      float z = 0.f;
      asm volatile("" : "+v"(z));
#pragma unroll
      for (int db = 0; db < 4; ++db) o[db] = splat16(z);
      lsum = z;
    }
#pragma unroll
    for (int ks = 0; ks < 8; ++ks) asm volatile("" : "+v"(qf[ks]));
    WAIT_VM0();
    BARRIER();
    {
      f32x16 sa, sb;
      bf16x8 kg[8];
      float pa;
      {
        f32x16 st = qk_abs(0);
        if (kbd == 0) maskblk(st, 0);
        const float m = xhalf_max(max16(st));
        mcur = m;
        negm = splat16(-m);
#pragma unroll
        for (int i = 0; i < 8; ++i) sa[i] = __builtin_amdgcn_exp2f(st[i] - m);
#pragma unroll
        for (int i = 8; i < 16; ++i) sa[i] = st[i] - m;
        pa = ((sa[0] + sa[1]) + (sa[2] + sa[3])) + ((sa[4] + sa[5]) + (sa[6] + sa[7]));
#pragma unroll
        for (int ks = 0; ks < 8; ++ks) kg[ks] = lds_read128(ka[ks] + 8192);
      }
      auto tile = [&](auto BUFC, auto LASTC, int t) {
        constexpr unsigned B_ = decltype(BUFC)::value;
        constexpr bool LAST = decltype(LASTC)::value;
        constexpr unsigned vo = B_ * TB, vn = TB - vo;
        constexpr unsigned kn = TB - vo, kd = vo;
        const u16* kbn = Kh + (size_t)min(t + 2, 31) * 64 * DHB;
        const u16* vbn = Vh + (size_t)min(t + 1, 31) * 64 * DHB;
#define DMAK(i) do { if ((i) == 0) { dma_base(KBUF + kd + wid * 4096); dma_piece<0>(kbn, koff); } else dma_piece_x<(i) * 1024, (i) * 64>(kbn, koff); } while (0)
#define DMAV(i) do { if ((i) == 0) dma_base(VBUF + vn + wid * 4096); dma_piece<(i) * 1024>(vbn, voff); } while (0)
        bf16x8 kf[8], vf[8], vg[8];
#define KF(i) kf[i] = lds_read128(kn + ka[i])
#define KG(i) kg[i] = lds_read128(kn + ka[i] + 8192)
        sb = MFMA32(kg[0], qf[0], negm);
        VRD(vf[0], vo + va[0]); DMAV(0);
        EXP2(sa[8]); EXP2(sa[9]);
        SBAR();
        sb = MFMA32(kg[1], qf[1], sb);
        VRD(vf[1], vo + va[1]); DMAV(1);
        EXP2(sa[10]); EXP2(sa[11]);
        SBAR();
        sb = MFMA32(kg[2], qf[2], sb);
        VRD(vf[2], vo + va[2]); DMAV(2);
        EXP2(sa[12]); EXP2(sa[13]);
        float pa2 = (sa[8] + sa[9]) + (sa[10] + sa[11]);
        SBAR();
        sb = MFMA32(kg[3], qf[3], sb);
        VRD(vf[3], vo + va[3]); DMAV(3);
        EXP2(sa[14]); EXP2(sa[15]);
        pa += sa[12] + sa[13];
        SBAR();
        sb = MFMA32(kg[4], qf[4], sb);
        DMAK(0);
        pa2 += sa[14] + sa[15];
        pa += pa2;
        if (__any(!(pa <= BIGSUM))) {
          f32x16 st = slow(2 * t);
          if (2 * t >= kbd) maskblk(st, 2 * t);
          const float delta = remax(st, false, sa);
#pragma unroll
          for (int i = 0; i < 16; ++i) sb[i] -= delta;
          pa = sum16(sa);
        }
        sb = MFMA32(kg[5], qf[5], sb);
        VRD(vf[4], vo + va[0] + 4096); VRD(vf[5], vo + va[1] + 4096);
        const bf16x8 p0 = pack8(sa, 0);
        lsum += pa;
        SBAR();
        sb = MFMA32(kg[6], qf[6], sb);
        VRD(vf[6], vo + va[2] + 4096); DMAK(1);
        const bf16x8 p1 = pack8(sa, 1);
        SBAR();
        sb = MFMA32(kg[7], qf[7], sb);
        VRD(vf[7], vo + va[3] + 4096); DMAK(2);
        SBAR();
        o[0] = MFMA32(vf[0], p0, o[0]);
        KF(0); DMAK(3);
        if (2 * t + 1 >= kbd) maskblk(sb, 2 * t + 1);
        SBAR();
        o[1] = MFMA32(vf[1], p0, o[1]);
        KF(1);
        EXP2(sb[0]); EXP2(sb[1]); EXP2(sb[2]);
        SBAR();
        o[2] = MFMA32(vf[2], p0, o[2]);
        KF(2);
        EXP2(sb[3]); EXP2(sb[4]);
        SBAR();
        o[3] = MFMA32(vf[3], p0, o[3]);
        KF(3);
        EXP2(sb[5]); EXP2(sb[6]);
        float pb = (sb[0] + sb[1]) + (sb[2] + sb[3]);
        SBAR();
        o[0] = MFMA32(vf[4], p1, o[0]);
        KF(4);
        EXP2(sb[7]); EXP2(sb[8]);
        SBAR();
        o[1] = MFMA32(vf[5], p1, o[1]);
        KF(5);
        EXP2(sb[9]); EXP2(sb[10]);
        float pb2 = (sb[4] + sb[5]) + (sb[6] + sb[7]);
        SBAR();
        o[2] = MFMA32(vf[6], p1, o[2]);
        KF(6);
        EXP2(sb[11]); EXP2(sb[12]);
        SBAR();
        o[3] = MFMA32(vf[7], p1, o[3]);
        KF(7);
        EXP2(sb[13]); EXP2(sb[14]);
        pb += (sb[8] + sb[9]) + sb[10];
        SBAR();
        sa = MFMA32(kf[0], qf[0], negm);
        VRD(vg[0], vo + va[0] + 8192);
        EXP2(sb[15]);
        pb2 += (sb[11] + sb[12]) + sb[13];
        SBAR();
        sa = MFMA32(kf[1], qf[1], sa);
        VRD(vg[1], vo + va[1] + 8192);
        pb += sb[14] + sb[15];
        pb += pb2;
        if (__any(!(pb <= BIGSUM))) {
          f32x16 st = slow(2 * t + 1);
          if (2 * t + 1 >= kbd) maskblk(st, 2 * t + 1);
          const float delta = remax(st, false, sb);
#pragma unroll
          for (int i = 0; i < 16; ++i) sa[i] -= delta;
          pb = sum16(sb);
        }
        sa = MFMA32(kf[2], qf[2], sa);
        VRD(vg[2], vo + va[2] + 8192);
        const bf16x8 r0 = pack8(sb, 0);
        lsum += pb;
        SBAR();
        sa = MFMA32(kf[3], qf[3], sa);
        VRD(vg[3], vo + va[3] + 8192);
        const bf16x8 r1 = pack8(sb, 1);
        SBAR();
        sa = MFMA32(kf[4], qf[4], sa);
        VRD(vg[4], vo + va[0] + 12288);
        SBAR();
        sa = MFMA32(kf[5], qf[5], sa);
        VRD(vg[5], vo + va[1] + 12288);
        SBAR();
        sa = MFMA32(kf[6], qf[6], sa);
        VRD(vg[6], vo + va[2] + 12288);
        SBAR();
        sa = MFMA32(kf[7], qf[7], sa);
        VRD(vg[7], vo + va[3] + 12288);
        SBAR();
        o[0] = MFMA32(vg[0], r0, o[0]);
        KG(0);
        if (2 * t + 2 >= kbd) maskblk(sa, 2 * t + 2);
        SBAR();
        o[1] = MFMA32(vg[1], r0, o[1]);
        KG(1); EXP2(sa[0]); EXP2(sa[1]);
        SBAR();
        o[2] = MFMA32(vg[2], r0, o[2]);
        KG(2); EXP2(sa[2]); EXP2(sa[3]);
        SBAR();
        o[3] = MFMA32(vg[3], r0, o[3]);
        KG(3); EXP2(sa[4]); EXP2(sa[5]);
        SBAR();
        o[0] = MFMA32(vg[4], r1, o[0]);
        KG(4); EXP2(sa[6]); EXP2(sa[7]);
        pa = (sa[0] + sa[1]) + (sa[2] + sa[3]);
        SBAR();
        o[1] = MFMA32(vg[5], r1, o[1]);
        KG(5);
        pa += (sa[4] + sa[5]) + (sa[6] + sa[7]);
        SBAR();
        o[2] = MFMA32(vg[6], r1, o[2]);
        KG(6); KG(7);
        SBAR();
        o[3] = MFMA32(vg[7], r1, o[3]);
#undef KF
#undef KG
        WAIT_VM0();
        BARRIER();
#undef DMAK
#undef DMAV
      };
      typedef std::integral_constant<unsigned, 0> C0;
      typedef std::integral_constant<unsigned, 1> C1;
      int t = 0;
      do {
        tile(C0{}, std::false_type{}, t);
        tile(C1{}, std::false_type{}, t + 1);
        t += 2;
      } while (t < ntile);
    }
    if (pass == 0) { stageK(0, 0); stageK(1, 1); stageV(0, 0); }
    const float inv = 1.f / xhalf_sum(lsum);
    {
      int lz = threadIdx.x;
      asm volatile("" : "+v"(lz));
      const int lane = lz & 63, r = lz & 31, h = (lz >> 5) & 1;
      const unsigned sb_ = STG + wid * 4096;
      u16* op = ctx + ((size_t)head * S_ + qb * 128 + wid * 32 + (lane >> 3)) * DHB + (lane & 7) * 8;
#pragma unroll
      for (int hf = 0; hf < 2; ++hf) {
#pragma unroll
        for (int d2 = 0; d2 < 2; ++d2)
#pragma unroll
          for (int g = 0; g < 4; ++g) {
            const f32x16& x = o[2 * hf + d2];
            u32x2 v = {cvtpk(x[4 * g] * inv, x[4 * g + 1] * inv), cvtpk(x[4 * g + 2] * inv, x[4 * g + 3] * inv)};
            *(lds_u32x2*)(uintptr_t)(sb_ + r * 128 + (((4 * d2 + g) ^ (r & 7)) * 16) + h * 8) = v;
          }
#pragma unroll
        for (int it = 0; it < 4; ++it) {
          const int rr = it * 8 + (lane >> 3);
          const bf16x8 v = lds_read128(sb_ + rr * 128 + (((lane & 7) ^ (rr & 7)) * 16));
          *(bf16x8*)(op + (size_t)it * 8 * DHB + hf * 64) = v;
        }
      }
    }
    if (pass == 0) loadQ(jitem);
  }
#undef EXP2
#undef VRD
}

DI void attnA_block(const u16* __restrict__ qkv, u16* __restrict__ oA, int head, int qb) {
  const int tid = threadIdx.x, lane = tid & 63, wid = __builtin_amdgcn_readfirstlane(tid >> 6);
  const int r = lane & 31, h = lane >> 5;
  const int q0 = qb * 256 + wid * 64;
  constexpr unsigned KBUF = 0, VBUF = 16384, TB = 8192;
  const u16* Qh = qkv + (size_t)(0 * HA + head) * S_ * 32;
  const u16* Kh = qkv + (size_t)(1 * HA + head) * S_ * 32;
  const u16* Vh = qkv + (size_t)(2 * HA + head) * S_ * 32;
  unsigned koff, voff;
  {
    const int row = 32 * wid + (lane >> 2), c = lane & 3;
    koff = (unsigned)(row * 64 + ((c ^ ((row >> 2) & 3)) * 16));
    voff = (unsigned)(row * 64 + c * 16);
  }
  auto stage = [&](int buf, int t) {
    dma_base(KBUF + buf * TB + wid * 2048);
    dma_piece<0>(Kh + (size_t)t * 128 * 32, koff); dma_piece<1024>(Kh + (size_t)t * 128 * 32, koff);
    dma_base(VBUF + buf * TB + wid * 2048);
    dma_piece<0>(Vh + (size_t)t * 128 * 32, voff); dma_piece<1024>(Vh + (size_t)t * 128 * 32, voff);
  };
  stage(0, 0);
  bf16x8 qf[2][2];
#pragma unroll
  for (int qt = 0; qt < 2; ++qt)
#pragma unroll
    for (int ks = 0; ks < 2; ++ks) qf[qt][ks] = *(const bf16x8*)(Qh + (size_t)(q0 + 32 * qt + r) * 32 + ks * 16 + h * 8);
  unsigned ka[2], va;
#pragma unroll
  for (int ks = 0; ks < 2; ++ks) {
    ka[ks] = KBUF + r * 64 + (((2 * ks + h) ^ ((r >> 2) & 3)) * 16);
    asm volatile("" : "+v"(ka[ks]));
  }
  {
    const int q = (lane & 15) >> 2, p = lane & 3, cb = (lane >> 4) & 1;
    va = VBUF + (4 * h + q) * 64 + cb * 32 + p * 8;
  }
  f32x16 o[2], negm[2];
  float mcur[2];
#pragma unroll
  for (int qt = 0; qt < 2; ++qt) { o[qt] = f32x16{}; negm[qt] = f32x16{}; mcur[qt] = 0.f; }
#pragma unroll
  for (int qt = 0; qt < 2; ++qt) { asm volatile("" : "+v"(qf[qt][0])); asm volatile("" : "+v"(qf[qt][1])); }
  WAIT_VM0();
  BARRIER();
  constexpr int NKT = S_ / 128;
#define EXP2(x) x = __builtin_amdgcn_exp2f(x)
#define EXP4(s, i) EXP2(s[i]); EXP2(s[i + 1]); EXP2(s[i + 2]); EXP2(s[i + 3])
#define VRDA(dst, off) dst = cat8(lds_read_tr(off), lds_read_tr((off) + 512))
  auto slow_tile = [&](bool first) {
#pragma unroll 1
    for (int kb = 0; kb < 4; ++kb) {
      const bf16x8 k0 = lds_read128(ka[0] + kb * 2048), k1 = lds_read128(ka[1] + kb * 2048);
      bf16x8 v0, v1;
      VRDA(v0, va + kb * 2048); VRDA(v1, va + kb * 2048 + 1024);
#pragma unroll
      for (int qt = 0; qt < 2; ++qt) {
        f32x16 st = MFMA32(k0, qf[qt][0], f32x16{});
        st = MFMA32(k1, qf[qt][1], st);
        const float tm = xhalf_max(max16(st));
        const float mnew = (first && kb == 0) ? tm : fmaxf(tm, mcur[qt]);
        const float f = __builtin_amdgcn_exp2f(-fmaxf(mnew - mcur[qt], 0.f));
        mcur[qt] = mnew;
#pragma unroll
        for (int i = 0; i < 16; ++i) { st[i] = __builtin_amdgcn_exp2f(st[i] - mnew); o[qt][i] *= f; }
        o[qt] = MFMA32(v0, pack8(st, 0), o[qt]);
        o[qt] = MFMA32(v1, pack8(st, 1), o[qt]);
      }
    }
    negm[0] = splat16(-mcur[0]);
    negm[1] = splat16(-mcur[1]);
  };
  for (int t = 0; t < NKT; ++t) {
    if (t + 1 < NKT) stage((t + 1) & 1, t + 1);
    const f32x16 os0 = o[0], os1 = o[1];
    f32x16 sa0, sa1, sb0, sb1;
    bf16x8 k0, k1, v0, v1;
    k0 = lds_read128(ka[0]); k1 = lds_read128(ka[1]);
    sa0 = MFMA32(k0, qf[0][0], negm[0]); sa1 = MFMA32(k0, qf[1][0], negm[1]);
    sa0 = MFMA32(k1, qf[0][1], sa0); sa1 = MFMA32(k1, qf[1][1], sa1);
    k0 = lds_read128(ka[0] + 2048); k1 = lds_read128(ka[1] + 2048);
#define UNIT(C0, C1, N0, N1, kb)                                                                   \
    do {                                                                                             \
      VRDA(v0, va + (kb) * 2048); VRDA(v1, va + (kb) * 2048 + 1024);                                 \
      SBAR();                                                                                        \
      if ((kb) < 3) N0 = MFMA32(k0, qf[0][0], negm[0]);                                              \
      EXP4(C0, 0);                                                                                   \
      SBAR();                                                                                        \
      if ((kb) < 3) N1 = MFMA32(k0, qf[1][0], negm[1]);                                              \
      EXP4(C0, 4);                                                                                   \
      SBAR();                                                                                        \
      if ((kb) < 3) N0 = MFMA32(k1, qf[0][1], N0);                                                   \
      EXP4(C0, 8);                                                                                   \
      const bf16x8 p00 = pack8(C0, 0);                                                               \
      SBAR();                                                                                        \
      if ((kb) < 3) N1 = MFMA32(k1, qf[1][1], N1);                                                   \
      EXP4(C0, 12);                                                                                  \
      if ((kb) < 2) { k0 = lds_read128(ka[0] + ((kb) + 2) * 2048); k1 = lds_read128(ka[1] + ((kb) + 2) * 2048); } \
      SBAR();                                                                                        \
      o[0] = MFMA32(v0, p00, o[0]);                                                                  \
      EXP4(C1, 0);                                                                                   \
      const bf16x8 p01 = pack8(C0, 1);                                                               \
      SBAR();                                                                                        \
      o[0] = MFMA32(v1, p01, o[0]);                                                                  \
      EXP4(C1, 4); EXP4(C1, 8);                                                                      \
      const bf16x8 p10 = pack8(C1, 0);                                                               \
      SBAR();                                                                                        \
      o[1] = MFMA32(v0, p10, o[1]);                                                                  \
      EXP4(C1, 12);                                                                                  \
      const bf16x8 p11 = pack8(C1, 1);                                                               \
      SBAR();                                                                                        \
      o[1] = MFMA32(v1, p11, o[1]);                                                                  \
    } while (0)
    UNIT(sa0, sa1, sb0, sb1, 0);
    UNIT(sb0, sb1, sa0, sa1, 1);
    UNIT(sa0, sa1, sb0, sb1, 2);
    UNIT(sb0, sb1, sa0, sa1, 3);
#undef UNIT
    const float lim = t == 0 ? -1.f : 1048576.f;
    const bool bad = h && !((o[0][8] - os0[8] <= lim) && (o[1][8] - os1[8] <= lim));
    if (__any(bad)) {
      o[0] = os0; o[1] = os1;
      slow_tile(t == 0);
    }
    ka[0] ^= TB; ka[1] ^= TB; va ^= TB;
    WAIT_VM0();
    BARRIER();
  }
#undef EXP2
#undef EXP4
#undef VRDA
#pragma unroll
  for (int qt = 0; qt < 2; ++qt) {
    const float l = __shfl(o[qt][8], r + 32);
    const float inv = 1.f / l;
    u16* op = oA + (size_t)(q0 + 32 * qt + r) * DM + head * DHA + 4 * h;
#pragma unroll
    for (int g = 0; g < 3; ++g) {
      if (g < 2 || h == 0) {
        u32x2 v = {cvtpk(o[qt][4 * g] * inv, o[qt][4 * g + 1] * inv), cvtpk(o[qt][4 * g + 2] * inv, o[qt][4 * g + 3] * inv)};
        *(u32x2*)(op + 8 * g) = v;
      }
    }
  }
}

constexpr int AQT = 2;
constexpr int NBLK_B = HB * 8, NBLK_A = HA * (S_ / (128 * AQT));
__global__ __launch_bounds__(256, 2) void k_attn(const u16* __restrict__ qB, const u16* __restrict__ kB,
                                                 const u16* __restrict__ vB, u16* __restrict__ ctx,
                                                 const u16* __restrict__ qkvA, u16* __restrict__ oA) {
  const int b = blockIdx.x;
  if (b < NBLK_B) {
    const int head = (b & 7) + 8 * (b >> 6), j = (b >> 3) & 7;
    attnB_item(qB, kB, vB, ctx, head, j);
  } else {
    const int a = b - NBLK_B;
    constexpr int PER = S_ / (128 * AQT);
    const int head = (a & 7) + 8 * (a / (8 * PER)), qb = (a >> 3) % PER;
    attnA_block(qkvA, oA, head, qb);
  }
}

extern "C" void kernel_launch(void* const* d_in, const int* in_sizes, int n_in, void* d_out, int out_size, void* d_ws,
                              size_t ws_size, hipStream_t stream) {
  const u16* Ax = (const u16*)d_in[0];
  const u16* Awqkv = (const u16*)d_in[1];
  const u16* Awo = (const u16*)d_in[2];
  const u16* Bx = (const u16*)d_in[3];
  const u16* Bckv = (const u16*)d_in[4];
  const u16* Bwq = (const u16*)d_in[5];
  const u16* Bwuk = (const u16*)d_in[6];
  const u16* Bwuv = (const u16*)d_in[7];
  const u16* Bwo = (const u16*)d_in[8];
  u16* out = (u16*)d_out;
  char* ws = (char*)d_ws;
  u16* qkvA = (u16*)ws;                 ws += (size_t)3 * HA * S_ * 32 * 2;
  u16* qB = (u16*)ws;                   ws += (size_t)S_ * NB * 2;
  u16* kB = (u16*)ws;                   ws += (size_t)S_ * NB * 2;
  u16* vB = (u16*)ws;                   ws += (size_t)S_ * NB * 2;
  u16* ctx = (u16*)ws;                  ws += (size_t)S_ * NB * 2;
  u16* oA = (u16*)ws;                   ws += (size_t)S_ * DM * 2;
  float* part = (float*)ws;             ws += (size_t)SPLITK * S_ * DM * 4;

  (void)hipFuncSetAttribute((const void*)k_proj, hipFuncAttributeMaxDynamicSharedMemorySize, G1::LDS);
  (void)hipFuncSetAttribute((const void*)k_out, hipFuncAttributeMaxDynamicSharedMemorySize, G2::LDS);
  (void)hipFuncSetAttribute((const void*)k_attn, hipFuncAttributeMaxDynamicSharedMemorySize, 81920);
  k_proj<<<448, 512, G1::LDS, stream>>>(Ax, Awqkv, Bx, Bckv, Bwq, Bwuk, Bwuv, qkvA, qB, kB, vB);
  k_attn<<<NBLK_B + NBLK_A, 256, 81920, stream>>>(qB, kB, vB, ctx, qkvA, oA);
  k_out<<<40 * SPLITK + 40, 512, G2::LDS, stream>>>(oA, Awo, ctx, Bwo, out, part);
  k_reduce<<<S_ * DM / 4 / 256, 256, 0, stream>>>(part, out);
}
```
